```python
import torch

def _softmax(x, axis=-1):
    return torch.softmax(x, dim=axis)
def _gelu(x):
    return torch.nn.functional.gelu(x, approximate="tanh")
def _silu(x):
    return torch.nn.functional.silu(x)
def _sigmoid(x):
    return torch.sigmoid(x)
def _layernorm(x, axis=-1, eps=1e-5):
    m = x.mean(dim=axis, keepdim=True)
    v = x.var(dim=axis, keepdim=True, unbiased=False)
    return (x - m) / torch.sqrt(v + eps)
def _scatter_add(tgt, idx, src, axis=0):
    return tgt.clone().index_add_(axis, idx, src)
def _segment_sum(src, seg, n):
    out = torch.zeros((n,) + src.shape[1:], dtype=src.dtype)
    return out.index_add_(0, seg, src)
def _pad(x, pw):
    flat = tuple(v for pair in reversed(pw) for v in pair)
    return torch.nn.functional.pad(x, flat)

kernel_name = 'concat_heads__ssm_conv_h3__sliding_block__prompt'

def setup_inputs():

    _w = (lambda a: (lambda t: t.to(torch.bfloat16) if t.is_floating_point() else t)(torch.as_tensor(a)))
    def _A_setup():
        B, S, D = 1, 2048, 1024
        ks = 4
        return {
            "q": _w((lambda *s: torch.randn(*s, dtype=torch.bfloat16) * 0.05)(B, S, D)),
            "k": _w((lambda *s: torch.randn(*s, dtype=torch.bfloat16) * 1.6)(B, S, D)),
            "v": _w((lambda *s: torch.randn(*s, dtype=torch.bfloat16) * 0.8)(B, S, D)),
            "shift_k": _w((lambda *s: torch.randn(*s, dtype=torch.bfloat16) * 1.6)(D, ks) * 0.5),
            "ssm_k": _w((lambda *s: torch.randn(*s, dtype=torch.bfloat16) * 1.6)(D, S) * (1.0 / S ** 0.5)),
            "ssm_D": _w((lambda *s: torch.randn(*s, dtype=torch.bfloat16) * 0.8)(D)),
        }
    _A_in = _A_setup()

    def _B_setup():
        B, S, D = 1, 2048, 1024
        return {
            "x": _w((lambda *s: torch.randn(*s, dtype=torch.bfloat16) * 0.2)(B, S, D)),
            "wqkv": _w((lambda *s: torch.randn(*s, dtype=torch.bfloat16) * 0.2)(D, 3 * D)),
            "wo": _w((lambda *s: torch.randn(*s, dtype=torch.bfloat16) * 0.05)(D, D)),
        }
    _B_in = _B_setup()

    return {
        "A_q": _A_in["q"],
        "A_k": _A_in["k"],
        "A_v": _A_in["v"],
        "A_shift_k": _A_in["shift_k"],
        "A_ssm_k": _A_in["ssm_k"],
        "A_ssm_D": _A_in["ssm_D"],
        "B_x": _B_in["x"],
        "B_wqkv": _B_in["wqkv"],
        "B_wo": _B_in["wo"],
    }

def reference(A_q, A_k, A_v, A_shift_k, A_ssm_k, A_ssm_D, B_x, B_wqkv, B_wo):
    def _A_ref(q, k, v, shift_k, ssm_k, ssm_D):
        B, S, D = q.shape
        ks = shift_k.shape[-1]
        k_pad = _pad(k, ((0, 0), (ks - 1, 0), (0, 0)))
        k_shift = torch.zeros_like(k)
        for t in range(ks):
            k_shift = k_shift + k_pad[:, t:t + S, :] * shift_k[:, ks - 1 - t][None, None, :]
        kv = k_shift * v
        L = 2 * S
        kv_pad = _pad(kv, ((0, 0), (0, S), (0, 0)))
        kf_pad = _pad(ssm_k, ((0, 0), (0, S)))
        kv_f = (lambda x, axes=(-2,-1): torch.fft.rfft2(x.float(), dim=axes))(kv_pad[:, None, :, :], axes=(1, 2))
        kf_f = (lambda x, axes=(-2,-1): torch.fft.rfft2(x.float(), dim=axes))(kf_pad[:, None, :], axes=(1, 2))
        kf_f = (lambda x, *d: x.permute(*d))(kf_f, 1, 2, 0)
        y_f = kv_f * kf_f[None, :, :, :]
        y = (lambda x, s, axes=(-2,-1): torch.fft.irfft2(x, s=s, dim=axes).to(torch.bfloat16))(y_f, (1, L), axes=(1, 2))[:, 0, :S, :]
        y = y + kv * ssm_D[None, None, :]
        return q * y

    def _B_ref(x, wqkv, wo):
        B, S, D = x.shape
        H = 32; Dh = D // H; W = 256
        qkv = (lambda x, *d: x.permute(*d))((x @ wqkv).reshape(B, S, 3, H, Dh), 2, 0, 3, 1, 4)
        q, k, v = qkv[0], qkv[1], qkv[2]
        scores = torch.einsum("bhqd,bhkd->bhqk", q, k) * (Dh ** -0.5)
        idx = torch.arange(S)
        mask = (idx[:, None] - idx[None, :] >= 0) & (idx[:, None] - idx[None, :] < W)
        scores = torch.where(mask[None, None], scores, -1e9)
        p = _softmax(scores, axis=-1)
        o = torch.einsum("bhqk,bhkd->bhqd", p, v)
        return (lambda x, *d: x.permute(*d))(o, 0, 2, 1, 3).reshape(B, S, D) @ wo

    a = _A_ref(A_q, A_k, A_v, A_shift_k, A_ssm_k, A_ssm_D)
    b = _B_ref(B_x, B_wqkv, B_wo)
    return torch.concatenate([a, b], axis=-1)
```

```cpp
#include <hip/hip_runtime.h>
#include <hip/hip_bf16.h>
typedef unsigned short u16;
__device__ __forceinline__ float b2f(u16 v){ return __uint_as_float(((unsigned)v)<<16); }
__device__ __forceinline__ u16 f2b(float f){ __hip_bfloat16 b = __float2bfloat16(f); return *(u16*)&b; }
__device__ __forceinline__ float rb(float f){ return b2f(f2b(f)); }
constexpr int S=2048, D=1024;
__global__ void k_kv(const u16* k,const u16* v,const u16* sk,u16* kv){
  int idx=blockIdx.x*256+threadIdx.x; int s=idx/D,d=idx%D;
  float acc=0.f;
  for(int t=0;t<4;t++){ int ss=s+t-3; float kk= ss>=0? b2f(k[ss*D+d]):0.f; acc=rb(acc+rb(kk*b2f(sk[d*4+3-t]))); }
  kv[idx]=f2b(acc*b2f(v[idx]));
}
__global__ void k_conv(const u16* kv,const u16* h,const u16* Dv,const u16* q,u16* out){
  int idx=blockIdx.x*256+threadIdx.x; int s=idx/D,d=idx%D;
  float acc=0.f;
  for(int t=0;t<=s;t++) acc+= b2f(kv[t*D+d])*b2f(h[d*S+s-t]);
  float y=rb(acc); y=rb(y+rb(b2f(kv[idx])*b2f(Dv[d])));
  out[s*2*D+d]=f2b(b2f(q[idx])*y);
}
__global__ void k_gemm(const u16* A,const u16* B,u16* C,int N,int K,int ldc){
  int n=blockIdx.x*256+threadIdx.x; int m=blockIdx.y;
  float acc=0.f;
  for(int k=0;k<K;k++) acc+=b2f(A[m*K+k])*b2f(B[k*N+n]);
  C[m*ldc+n]=f2b(acc);
}
__global__ void k_attn(const u16* qkv,u16* o){
  int idx=blockIdx.x*256+threadIdx.x; int s=idx/32,h=idx%32;
  float q[32]; for(int i=0;i<32;i++) q[i]=b2f(qkv[s*3072+h*32+i]);
  int lo=s-255<0?0:s-255;
  float m=-1e30f;
  for(int t=lo;t<=s;t++){ float a=0; for(int i=0;i<32;i++) a+=q[i]*b2f(qkv[t*3072+1024+h*32+i]); a*=0.17677669529f; m=fmaxf(m,a);}
  float acc[32]; for(int i=0;i<32;i++) acc[i]=0; float l=0;
  for(int t=lo;t<=s;t++){ float a=0; for(int i=0;i<32;i++) a+=q[i]*b2f(qkv[t*3072+1024+h*32+i]); a*=0.17677669529f; float p=__expf(a-m); l+=p; for(int i=0;i<32;i++) acc[i]+=p*b2f(qkv[t*3072+2048+h*32+i]);}
  for(int i=0;i<32;i++) o[s*1024+h*32+i]=f2b(acc[i]/l);
}
extern "C" void kernel_launch(void* const* d_in, const int* in_sizes, int n_in, void* d_out, int out_size, void* d_ws, size_t ws_size, hipStream_t stream){
  const u16* q=(const u16*)d_in[0]; const u16* k=(const u16*)d_in[1]; const u16* v=(const u16*)d_in[2];
  const u16* sk=(const u16*)d_in[3]; const u16* h=(const u16*)d_in[4]; const u16* Dv=(const u16*)d_in[5];
  const u16* x=(const u16*)d_in[6]; const u16* wqkv=(const u16*)d_in[7]; const u16* wo=(const u16*)d_in[8];
  u16* out=(u16*)d_out; u16* ws=(u16*)d_ws;
  u16* kv=ws; u16* qkv=ws+S*D; u16* o=qkv+S*3*D;
  k_kv<<<S*D/256,256,0,stream>>>(k,v,sk,kv);
  k_conv<<<S*D/256,256,0,stream>>>(kv,h,Dv,q,out);
  k_gemm<<<dim3(3072/256,S),256,0,stream>>>(x,wqkv,qkv,3072,1024,3072);
  k_attn<<<S*32/256,256,0,stream>>>(qkv,o);
  k_gemm<<<dim3(1024/256,S),256,0,stream>>>(o,wo,out+1024,1024,1024,2048);
}
```

```cpp
#include <hip/hip_runtime.h>
#include <hip/hip_bf16.h>
#include <stdint.h>
#if defined(__HIP_DEVICE_COMPILE__)
#define NOPK __attribute__((target("no-packed-fp32-ops")))
#else
#define NOPK
#endif
typedef unsigned short u16;
typedef unsigned long long u64;
typedef short bf16x8 __attribute__((ext_vector_type(8)));
typedef short s16x4 __attribute__((ext_vector_type(4)));
typedef float f32x16 __attribute__((ext_vector_type(16)));
typedef float f32x4 __attribute__((ext_vector_type(4)));
typedef float f32x2 __attribute__((ext_vector_type(2)));
typedef __bf16 bf2 __attribute__((ext_vector_type(2)));
typedef unsigned u32x4 __attribute__((ext_vector_type(4)));
typedef unsigned u32x2 __attribute__((ext_vector_type(2)));
#define LAS __attribute__((address_space(3)))
#define GAS __attribute__((address_space(1)))
typedef LAS const char* lptr;
typedef LAS char* lwptr;
typedef GAS u64 gu64;
#define DI __device__ __forceinline__ NOPK
#define MFMA32(a,b,c) __builtin_amdgcn_mfma_f32_32x32x16_bf16((a),(b),(c),0,0,0)
#define MFMA16(a,b,c) __builtin_amdgcn_mfma_f32_16x16x32_bf16((a),(b),(c),0,0,0)
#define SBAR() __builtin_amdgcn_sched_barrier(0)
#define PIN(x) asm volatile("":"+v"(x))
#define MEMFENCE() asm volatile("":::"memory")
#define BAR() asm volatile("s_barrier":::"memory")
#define WAITV(n) asm volatile("s_waitcnt vmcnt(" #n ")":::"memory")
#define WAITL() asm volatile("s_waitcnt lgkmcnt(0)":::"memory")
#define RLX_AGENT __ATOMIC_RELAXED, __HIP_MEMORY_SCOPE_AGENT
#define RLX_WG __ATOMIC_RELAXED, __HIP_MEMORY_SCOPE_WORKGROUP
#define SC1 16
#define B8(x) __builtin_bit_cast(bf16x8,(x))

#define PREPSZ (4*MB)
constexpr unsigned MB=1u<<20;
constexpr unsigned WS_KV=0, WS_O=8*MB, WS_XT=12*MB, WS_Y=16*MB, WS_F=20*MB, WS_END=21*MB, WS_ST=24*MB;
constexpr unsigned F_E=WS_F, F_W=WS_F+4096;
enum { FK_KV0=0, FK_KV1, FK_O0, FK_O1, FK_X, FK_Y };
constexpr int G1_SLOT=40960;
constexpr int HR=57344;
constexpr int L_CV=122880, CV_W=8704;
constexpr int L_MISC=L_CV+4*CV_W;
constexpr int L_WO=L_MISC-131072;
constexpr int L_CARRY=L_MISC;
constexpr int L_CNT=L_MISC+2176;
constexpr int L_JUNK=L_CNT+256;
constexpr int L_MASK=L_JUNK+1536;
constexpr int LDS_BYTES=163840;
static_assert(L_MASK+2048<=LDS_BYTES,"lds");
enum { C_RD=0, C_KV0, C_KV1, C_H0, C_H1, C_CV, C_E, C_L, C_W1, C_W2, C_W3, C_O0, C_O1, C_EF, C_LF, C_Y, C_XF, C_YF, C_NONCE=24 };
constexpr unsigned SPIN_CAP=1u<<16, LSPIN_CAP=1u<<20;

DI unsigned pk(float a,float b){ f32x2 v={a,b}; bf2 r=__builtin_convertvector(v,bf2); return __builtin_bit_cast(unsigned,r); }
DI float asf(unsigned u){ return __builtin_bit_cast(float,u); }
DI unsigned asu(float f){ return __builtin_bit_cast(unsigned,f); }
DI bool allof(bool p){ return __builtin_amdgcn_ballot_w64(p)==~0ull; }
DI float lo16(unsigned u){ return asf(u<<16); }
DI float hi16(unsigned u){ return asf(u&0xffff0000u); }
DI bf16x8 ldsr128(lptr p){ return *(const LAS bf16x8*)p; }
DI s16x4 ldstr(lptr p){ return __builtin_amdgcn_ds_read_tr16_b64_v4i16((LAS s16x4*)p); }
DI bf16x8 cat(s16x4 a,s16x4 b){ return __builtin_shufflevector(a,b,0,1,2,3,4,5,6,7); }
DI u32x4 pack8(const f32x16& x,int s,float sc){ u32x4 r; r[0]=pk(x[8*s]*sc,x[8*s+1]*sc); r[1]=pk(x[8*s+2]*sc,x[8*s+3]*sc); r[2]=pk(x[8*s+4]*sc,x[8*s+5]*sc); r[3]=pk(x[8*s+6]*sc,x[8*s+7]*sc); return r; }
DI u32x4 pack8(const f32x16& x,int s){ u32x4 r; r[0]=pk(x[8*s],x[8*s+1]); r[1]=pk(x[8*s+2],x[8*s+3]); r[2]=pk(x[8*s+4],x[8*s+5]); r[3]=pk(x[8*s+6],x[8*s+7]); return r; }
#define DMA4_BODY(FL) "s_mov_b32 %0, m0\n\ts_mov_b32 m0, %3\n\ts_nop 0\n\t" \
   "global_load_lds_dwordx4 %1, %2" FL "\n\tglobal_load_lds_dwordx4 %1, %2 offset:1024" FL "\n\tglobal_load_lds_dwordx4 %1, %2 offset:2048" FL "\n\tglobal_load_lds_dwordx4 %1, %2 offset:3072" FL "\n\ts_mov_b32 m0, %0"
DI void dma4(unsigned voff,const void* sbase,unsigned ldsaddr){ unsigned keep; asm volatile(DMA4_BODY("") : "=&s"(keep) : "v"(voff), "s"(sbase), "s"(ldsaddr) : "memory"); }
DI void dma4_sc1(unsigned voff,const void* sbase,unsigned ldsaddr){ unsigned keep; asm volatile(DMA4_BODY(" sc1") : "=&s"(keep) : "v"(voff), "s"(sbase), "s"(ldsaddr) : "memory"); }
DI void dma4v(unsigned v0,unsigned v1,unsigned v2,unsigned v3,const void* sbase,unsigned ldsaddr){
  unsigned keep;
  asm volatile("s_mov_b32 %0, m0\n\ts_mov_b32 m0, %6\n\ts_nop 0\n\t"
    "global_load_lds_dwordx4 %1, %5\n\tglobal_load_lds_dwordx4 %2, %5 offset:1024\n\tglobal_load_lds_dwordx4 %3, %5 offset:2048\n\tglobal_load_lds_dwordx4 %4, %5 offset:3072\n\ts_mov_b32 m0, %0"
    : "=&s"(keep) : "v"(v0),"v"(v1),"v"(v2),"v"(v3), "s"(sbase), "s"(ldsaddr) : "memory");
}
DI void touch(unsigned voff,const void* sbase,unsigned junk){
  unsigned keep;
  asm volatile("s_mov_b32 %0, m0\n\ts_mov_b32 m0, %3\n\ts_nop 0\n\tglobal_load_lds_dword %1, %2\n\ts_mov_b32 m0, %0" : "=&s"(keep) : "v"(voff), "s"(sbase), "s"(junk) : "memory");
}
DI void touch64(const void* a,unsigned junk){
  unsigned keep;
  asm volatile("s_mov_b32 %0, m0\n\ts_mov_b32 m0, %2\n\ts_nop 0\n\tglobal_load_lds_dword %1, off\n\ts_mov_b32 m0, %0" : "=&s"(keep) : "v"(a), "s"(junk) : "memory");
}
DI bool flags_ready(const gu64* p,u64 nonce){ u64 v=__hip_atomic_load(p,RLX_AGENT); bool r=allof(v==nonce); MEMFENCE(); return r; }
DI void wait_flags(const gu64* p,u64 nonce){
#pragma unroll 1
  for(unsigned sp=0;sp<SPIN_CAP;sp++){
    u64 v=__hip_atomic_load(p,RLX_AGENT);
    if(allof(v==nonce)) break;
    __builtin_amdgcn_s_sleep(1);
  }
  MEMFENCE();
}
DI void lds_wait(LAS unsigned* p,unsigned n){
#pragma unroll 1
  for(unsigned sp=0;sp<LSPIN_CAP;sp++){
    unsigned v=__hip_atomic_load(p,RLX_WG);
    if(__builtin_amdgcn_readfirstlane(v)>=n) break;
    __builtin_amdgcn_s_sleep(1);
  }
  MEMFENCE();
}
DI void arrive(LAS unsigned* cnt){
  unsigned one=1u;
  asm volatile("s_mov_b64 exec, 1\n\tds_add_u32 %0, %1\n\ts_mov_b64 exec, -1" :: "v"(cnt), "v"(one) : "memory");
}
DI void arrive_publish(LAS unsigned* cnt,unsigned n,gu64* flag,u64 nonce,int lane){
  unsigned one=1u, old;
  asm volatile("s_mov_b64 exec, 1\n\tds_add_rtn_u32 %0, %1, %2\n\ts_mov_b64 exec, -1\n\ts_waitcnt lgkmcnt(0)" : "=&v"(old) : "v"(cnt), "v"(one) : "memory");
  if(__builtin_amdgcn_readfirstlane(old)==n-1){ if(lane==0) __hip_atomic_store(flag,nonce,RLX_AGENT); }
  MEMFENCE();
}
DI gu64* flagp(char* ws,int blk,int k){ return (gu64*)(ws+F_W+(unsigned)(blk*8+k)*128u); }
DI int blk_of(int R,int C){ return (((R&7)*4+(C&3))<<3)|((R>>3)*4+(C>>2)); }

DI float fmax3(float a,float b,float c){ return __builtin_fmaxf(__builtin_fmaxf(a,b),c); }
DI float xhalf_max(float m){ auto rr=__builtin_amdgcn_permlane32_swap(asu(m),asu(m),false,false); return __builtin_fmaxf(asf(rr[0]),asf(rr[1])); }
struct G1F { bf16x8 x0,x1; s16x4 w[6]; };

template<bool CHECK>
DI void attention(char* lds,LAS unsigned* cnt,char* ws,__amdgpu_buffer_rsrc_t wsr,int b,int R,int C,int wid,int lane,u64 nonce){
  const int late=wid>>2, jq=wid&3;
  const int T=4*R+jq;
  lptr KL=(lptr)lds+(1-late)*HR+jq*4096+lane*16;
  lptr QL=(lptr)lds+(1-late)*HR+49152+jq*2048+lane*16;
  const f32x16 zero=f32x16{};
  const float ninf=-__builtin_inff();
  const int dl=(lane&31)-4*(lane>>5);
  bf16x8 kf[9][2];
  const bf16x8 q0=ldsr128(QL), q1=ldsr128(QL+1024);
  kf[8][0]=ldsr128(KL+8*4096); kf[8][1]=ldsr128(KL+8*4096+1024);
#pragma unroll
  for(int u=0;u<4;u++){ kf[u][0]=ldsr128(KL+u*4096); kf[u][1]=ldsr128(KL+u*4096+1024); }
  f32x16 sd,sm,s[3];
  float m;
#pragma unroll
  for(int n=0;n<11;n++){
    SBAR();
    if(n==0){ sd=MFMA32(kf[8][0],q0,zero); sd=MFMA32(kf[8][1],q1,sd); PIN(sd); }
    else if(n==1){ sm=MFMA32(kf[0][0],q0,zero); sm=MFMA32(kf[0][1],q1,sm); PIN(sm); }
    else if(n<9){ const int u=n-1; s[u%3]=MFMA32(kf[u][0],q0,zero); s[u%3]=MFMA32(kf[u][1],q1,s[u%3]); PIN(s[u%3]); }
    if(n<4){ const int u=n+4; kf[u][0]=ldsr128(KL+u*4096); kf[u][1]=ldsr128(KL+u*4096+1024); }
    if(n==3){
#pragma unroll
      for(int i=0;i<16;i++){ const int a=(i&3)+8*(i>>2); sm[i]=(a<=dl)?sd[i]:((CHECK&&T<8)?ninf:sm[i]); }
    }
    if(n>=3){
      const int t=n-3; const f32x16& x=(t==0)?sm:s[t%3];
      float a=fmax3(x[0],x[1],x[2]), c=fmax3(x[3],x[4],x[5]);
      a=fmax3(a,x[6],x[7]); c=fmax3(c,x[8],x[9]); a=fmax3(a,x[10],x[11]); c=fmax3(c,x[12],x[13]); a=fmax3(a,x[14],x[15]);
      if(t==0) m=__builtin_fmaxf(a,c);
      else { const float mm=fmax3(m,a,c); m=(!CHECK||T-8+t>=0)?mm:m; }
      PIN(m);
    }
  }
  SBAR();
  bf16x8 vfr[2][2];
  vfr[0][0]=ldsr128(KL+8*4096+2048); vfr[0][1]=ldsr128(KL+8*4096+3072);
  vfr[1][0]=ldsr128(KL+2048); vfr[1][1]=ldsr128(KL+3072);
  const u32x4 mk0=*(const LAS u32x4*)((lptr)lds+L_MASK+lane*16), mk1=*(const LAS u32x4*)((lptr)lds+L_MASK+1024+lane*16);
  m=xhalf_max(m);
  f32x16 negm;
#pragma unroll
  for(int i=0;i<16;i++) negm[i]=-m;
  PIN(negm);
  WAITV(0);
  if(late){ arrive_publish(cnt+C_Y,4,flagp(ws,b,FK_Y),nonce,lane); arrive(cnt+C_W1); }
  else if(wid&1) arrive(cnt+C_H1);
  const unsigned one2=((((lane>>2)^(lane>>4))&1)==0)?0x3f803f80u:0u;
  const u32x4 ones={one2,one2,one2,one2};
  f32x16 o; f32x4 ls;
  u32x4 p0,p1;
#pragma unroll
  for(int u=0;u<=8;u++){
    SBAR();
    u32x4 n0,n1;
    if(u+1<8){ const int t=u+1; s[t&1]=MFMA32(kf[t][0],q0,negm); s[t&1]=MFMA32(kf[t][1],q1,s[t&1]); }
    if(u<8){
      f32x16& x=(u==0)?sm:s[u&1];
#pragma unroll
      for(int i=0;i<16;i++) x[i]=__builtin_amdgcn_exp2f((u==0)?x[i]-m:x[i]);
      n0=pack8(x,0); n1=pack8(x,1);
      if(CHECK && u>0){ if(T-8+u<0){ n0=u32x4{0,0,0,0}; n1=u32x4{0,0,0,0}; } }
    }
    if(u==1){
      const u32x4 d0=p0&mk0, d1=p1&mk1;
      o=MFMA32(vfr[0][0],B8(d0),zero); o=MFMA32(vfr[0][1],B8(d1),o);
      o=MFMA32(vfr[1][0],B8(p0^d0),o); o=MFMA32(vfr[1][1],B8(p1^d1),o);
      vfr[1][0]=ldsr128(KL+1*4096+2048); vfr[1][1]=ldsr128(KL+1*4096+3072);
      vfr[0][0]=ldsr128(KL+2*4096+2048); vfr[0][1]=ldsr128(KL+2*4096+3072);
      ls=MFMA16(B8(ones),B8(p0),(f32x4{0,0,0,0})); ls=MFMA16(B8(ones),B8(p1),ls);
    } else if(u>1){
      const int t=u-1;
      o=MFMA32(vfr[t&1][0],B8(p0),o); o=MFMA32(vfr[t&1][1],B8(p1),o);
      if(t+2<8){ vfr[t&1][0]=ldsr128(KL+(t+2)*4096+2048); vfr[t&1][1]=ldsr128(KL+(t+2)*4096+3072); }
      ls=MFMA16(B8(ones),B8(p0),ls); ls=MFMA16(B8(ones),B8(p1),ls);
    }
    if(u>1 && u<7 && !CHECK){
#pragma unroll
      for(int g_=0;g_<6;g_++){ __builtin_amdgcn_sched_group_barrier(0x8,1,0); __builtin_amdgcn_sched_group_barrier(0x402,4,0); }
    }
    if(u+1<8) PIN(s[(u+1)&1]);
    if(u>0){ PIN(ls); PIN(o); }
    if(u<8){ PIN(n0); PIN(n1); p0=n0; p1=n1; }
  }
  SBAR();
  arrive(cnt+(late?C_L:C_E));
  const float il=__builtin_amdgcn_rcpf(ls[0]);
  const unsigned oo=WS_O+(unsigned)(T*64+2*(2*C+late))*1024u+lane*16u;
  __builtin_amdgcn_raw_buffer_store_b128(pack8(o,0,il),wsr,oo,0,SC1);
  __builtin_amdgcn_raw_buffer_store_b128(pack8(o,1,il),wsr,oo+1024,0,SC1);
}

__global__ __launch_bounds__(512,2) NOPK void fused(const u16* Aq,const u16* Ak,const u16* Av,const u16* Ask,const u16* Ah,const u16* AD,
                                               const u16* X,const u16* W,const u16* WO,u16* out,char* ws){
  extern __shared__ __attribute__((aligned(16))) char lds[];
  const int tid=threadIdx.x, lane=tid&63; const int wid=__builtin_amdgcn_readfirstlane(tid>>6);
  const int b=blockIdx.x, xcd=b&7, j=b>>3;
  const int R=(xcd>>2)*8+(j>>2), C=(xcd&3)*4+(j&3);
  const int wgi=xcd*32+j;
  const int R2=xcd*2+(j>>4), C2=j&15;
  const unsigned lds0=(unsigned)(uintptr_t)lds;
  LAS unsigned* cnt=(LAS unsigned*)((lwptr)lds+L_CNT);
  const __amdgpu_buffer_rsrc_t wsr=__builtin_amdgcn_make_buffer_rsrc(ws,0,WS_END,0x00020000);
  u64 nonce;
  auto wo_block=[&](int i) NOPK __attribute__((always_inline)) {
    const int hd=2*((i+C2)&15)+(i>>4);
    const unsigned vo=(unsigned)((lane>>3)*2048+C2*128+(((lane&7)^(((lane>>4)&1)<<2))*16));
    dma4v(vo,vo+16384-1024,vo+2*(16384-1024),vo+3*(16384-1024),(const char*)WO+(size_t)hd*65536,lds0+L_WO+(31-i)*4096);
  };
  if(__builtin_expect(wid>=4,1)){
    const int v=wid-4;
    if(__builtin_expect(wid<6,1)){
      unsigned ao[8], bo[12];
#pragma unroll
      for(int i=0;i<8;i++) ao[i]=(unsigned)((R*128+64*v+8*i+(lane>>3))*2048+(((lane&7)^(4*(i&1)+(lane>>4)))*16)-(i&3)*1024);
#pragma unroll
      for(int i=0;i<12;i++){ const int p=12*v+i; bo[i]=(unsigned)((8*(p&7)+(lane>>3))*6144+(p>>3)*2048+C*128+(((lane&7)^(((lane>>4)&1)<<2))*16)-(i&3)*1024); }
      auto issue=[&](int kt,int slot) NOPK __attribute__((always_inline)) {
        const char* xb=(const char*)X+kt*128; const char* wb=(const char*)W+(size_t)kt*64*6144;
        const unsigned la=lds0+slot*G1_SLOT+v*8192, lb=lds0+slot*G1_SLOT+16384+v*12288;
        dma4v(ao[0],ao[1],ao[2],ao[3],xb,la); dma4v(bo[0],bo[1],bo[2],bo[3],wb,lb);
        dma4v(ao[4],ao[5],ao[6],ao[7],xb,la+4096); dma4v(bo[4],bo[5],bo[6],bo[7],wb,lb+4096);
        dma4v(bo[8],bo[9],bo[10],bo[11],wb,lb+8192);
      };
      issue(0,0);
      int slot=1;
#pragma unroll 1
      for(int k=0;k<15;k++){
        if(k==0){ issue(1,1); slot=2; }
        asm volatile("s_waitcnt vmcnt(20)\n\ts_barrier":::"memory");
        if(k<14){ issue(k+2,slot); slot=(slot==2)?0:slot+1; }
      }
    } else {
      const int hv=wid-6;
      const unsigned junk=lds0+L_JUNK;
      u64 E=__hip_atomic_load((gu64*)(ws+F_E),RLX_AGENT);
      if(hv==0){
#pragma unroll
        for(int c=0;c<4;c++){ *(LAS unsigned*)((lwptr)lds+L_CV+c*CV_W+lane*4)=0u; *(LAS unsigned*)((lwptr)lds+L_CV+c*CV_W+4352+lane*4)=0u; }
        if(lane<24) cnt[lane]=0u;
      } else {
        const int bq=lane&31, hi=lane>>5; u32x4 mk[2];
#pragma unroll
        for(int d=0;d<8;d++){ const int i=2*d; const int a=(i&3)+8*(i>>2)+4*hi; mk[d>>2][d&3]=((a<=bq)?0xffffu:0u)|((a+1<=bq)?0xffff0000u:0u); }
        *(LAS u32x4*)((lwptr)lds+L_MASK+lane*16)=mk[0]; *(LAS u32x4*)((lwptr)lds+L_MASK+1024+lane*16)=mk[1];
      }
      BAR();
      { const int ib=(3-(int)((uintptr_t)W>>7)-4*(xcd&3))&7;
        if(ib<4){ if(lane<48) touch((unsigned)(((j*2+hv)*48+lane)*2048+(4*(xcd&3)+ib)*128),W,junk); } }
      const __amdgpu_buffer_rsrc_t kr_=__builtin_amdgcn_make_buffer_rsrc((void*)Ak,0,PREPSZ,0x00020000);
      const __amdgpu_buffer_rsrc_t vr_=__builtin_amdgcn_make_buffer_rsrc((void*)Av,0,PREPSZ,0x00020000);
      const __amdgpu_buffer_rsrc_t sr_=__builtin_amdgcn_make_buffer_rsrc((void*)Ask,0,8192,0x00020000);
      const int rg=lane>>3, cg=lane&7;
      const int s0=j*64+rg*8, ch0=xcd*128+hv*64+cg*8;
      u32x4 kk[11], vv[8], sk[4];
      const unsigned ro=(unsigned)(s0*2048+ch0*2);
      if(hv==0){ const int kb=(3-(int)((uintptr_t)X>>7))&7; touch((unsigned)(((xcd>>2)*1024+j*32+(lane&31))*2048+(kb+8*(lane>>5))*128),X,junk); }
#pragma unroll
      for(int i=0;i<4;i++) kk[i]=__builtin_amdgcn_raw_buffer_load_b128(kr_,ro+(unsigned)((i-3)*2048),0,0);
      BAR();
#pragma unroll
      for(int i=4;i<11;i++) kk[i]=__builtin_amdgcn_raw_buffer_load_b128(kr_,ro+(unsigned)((i-3)*2048),0,0);
      BAR();
#pragma unroll
      for(int i=0;i<4;i++) vv[i]=__builtin_amdgcn_raw_buffer_load_b128(vr_,ro+(unsigned)(i*2048),0,0);
#pragma unroll
      for(int i=0;i<4;i++) sk[i]=__builtin_amdgcn_raw_buffer_load_b128(sr_,(unsigned)(ch0*8+i*16),0,0);
      BAR();
#pragma unroll
      for(int i=4;i<8;i++) vv[i]=__builtin_amdgcn_raw_buffer_load_b128(vr_,ro+(unsigned)(i*2048),0,0);
      BAR();
      if(hv==1 && lane<6) touch((unsigned)(F_W+(b*8+lane)*128),ws,junk);
      BAR();
      SBAR();
      asm volatile("":"+v"(E),"+v"(kk[0]),"+v"(kk[1]),"+v"(kk[2]),"+v"(kk[3]),"+v"(kk[4]),"+v"(kk[5]),"+v"(kk[6]),"+v"(kk[7]),"+v"(kk[8]),"+v"(kk[9]),"+v"(kk[10]));
      asm volatile("":"+v"(vv[0]),"+v"(vv[1]),"+v"(vv[2]),"+v"(vv[3]),"+v"(vv[4]),"+v"(vv[5]),"+v"(vv[6]),"+v"(vv[7]),"+v"(sk[0]),"+v"(sk[1]),"+v"(sk[2]),"+v"(sk[3]));
#pragma unroll
      for(int cc=0;cc<4;cc++){
#pragma unroll
        for(int c=2*cc;c<2*cc+2;c++){
          const unsigned w01=sk[c>>1][(c&1)*2], w23=sk[c>>1][(c&1)*2+1];
          const float w0=lo16(w01),w1=hi16(w01),w2=lo16(w23),w3=hi16(w23);
          float kf[11];
#pragma unroll
          for(int i=0;i<11;i++) kf[i]=(c&1)?hi16(kk[i][c>>1]):lo16(kk[i][c>>1]);
          float o[8];
#pragma unroll
          for(int i=0;i<8;i++){ float vf=(c&1)?hi16(vv[i][c>>1]):lo16(vv[i][c>>1]); o[i]=(kf[i]*w3+kf[i+1]*w2+kf[i+2]*w1+kf[i+3]*w0)*vf; }
          u32x4 r; r[0]=pk(o[0],o[1]); r[1]=pk(o[2],o[3]); r[2]=pk(o[4],o[5]); r[3]=pk(o[6],o[7]);
          __builtin_amdgcn_raw_buffer_store_b128(r,wsr,WS_XT+(unsigned)(ch0+c)*4096u+(unsigned)s0*2u,0,SC1);
        }
        SBAR(); BAR(); SBAR();
      }
      nonce=E*6364136223846793005ull+1442695040888963407ull;
      if(hv==0 && lane==0) *(LAS u64*)(cnt+C_NONCE)=nonce;
      { const char* hb=(const char*)Ah+(size_t)(wgi*4+2*hv)*4096;
        dma4(lane*16,hb,lds0+L_CV+(2*hv)*CV_W+256);
        asm volatile("s_waitcnt vmcnt(4) lgkmcnt(0)\n\ts_barrier":::"memory");
        if(hv==0 && lane==0) __hip_atomic_store(flagp(ws,b,FK_X),nonce,RLX_AGENT);
        dma4(lane*16,hb+4096,lds0+L_CV+(2*hv+1)*CV_W+256); }
      BAR();
      if(hv==1) touch((unsigned)(lane*128),(const char*)WO+(size_t)wgi*8192,junk);
      BAR();
      u64 fxv=__hip_atomic_load(flagp(ws,(lane&31)*8+xcd,FK_X),RLX_AGENT);
      BAR();
      SBAR(); PIN(fxv);
      if(hv==1){ if(allof(fxv==nonce)){ arrive(cnt+C_XF); WAITL(); } }
      BAR();
    }
    nonce=*(LAS u64*)(cnt+C_NONCE);
    const int ch=wgi*4+v;
    const bool xr=__builtin_amdgcn_readfirstlane(cnt[C_XF])!=0;
    if(xr){ dma4_sc1(lane*16,ws+WS_XT+(size_t)ch*4096,lds0+L_CV+v*CV_W+4608); WAITV(4); }
    else WAITV(0);
    BAR();
    if(!xr){ wait_flags(flagp(ws,(lane&31)*8+xcd,FK_X),nonce); dma4_sc1(lane*16,ws+WS_XT+(size_t)ch*4096,lds0+L_CV+v*CV_W+4608); }
    {
      lwptr LW=(lwptr)lds+L_CV+v*CV_W;
      const int g=lane>>4,q=(lane&15)>>2,p=lane&3;
      lptr ha=(lptr)LW+256+8*(4*p+8*g+q-12);
      lptr xa=(lptr)LW+4608+8*(p+8-8*g-q);
      lwptr yw=LW+256+2*lane;
      PIN(ha); PIN(xa); PIN(yw);
      bf16x8 A[16],B0[16],B1[16];
#pragma unroll
      for(int d=0;d<16;d++) A[d]=cat(ldstr(ha+256*d),ldstr(ha+256*d+32));
      WAITV(0);
      float zf=0.f; PIN(zf);
#define MM0(z,a,b) asm volatile("v_mfma_f32_16x16x32_bf16 %0, %1, %2, 0" : "=&v"(z) : "v"(a), "v"(b))
#define MMA(z,a,b) asm volatile("v_mfma_f32_16x16x32_bf16 %0, %1, %2, %0" : "+v"(z) : "v"(a), "v"(b))
#define LDB(P,e) B##P[e]=cat(ldstr(xa+256*(e)+128*(P)+32),ldstr(xa+256*(e)+128*(P)))
#define ROR(d,s,z) asm volatile("v_add_f32_dpp %0, %1, %2 wave_ror:1 row_mask:0xf bank_mask:0xf" : "=&v"(d) : "v"(s), "v"(z))
#define SHR(d,s,z) asm volatile("v_add_f32_dpp %0, %1, %2 wave_shr:1 row_mask:0xf bank_mask:0xf" : "+v"(d) : "v"(s), "v"(z))
#define STY(c,w) do{ *(LAS u16*)(yw+256*(c))=(u16)(w); *(LAS u16*)(yw+256*(c)+128)=(u16)((w)>>16); }while(0)
#define NOP1() asm volatile("s_nop 0")
      SBAR();
f32x4 z0,z1,z2,z3,z4,z5,z6,z7,z8,z9,z10,z11,z12,z13,z14,z15,z16,z17,z18,z19,z20,z21,z22,z23,z24,z25,z26,z27,z28,z29,z30,z31;
float t2_0,t1_0,u_0,t2_1,t1_1,u_1,t2_2,t1_2,u_2,t2_3,t1_3,u_3,t2_4,t1_4,u_4,t2_5,t1_5,u_5,t2_6,t1_6,u_6,t2_7,t1_7,u_7,t2_8,t1_8,u_8,t2_9,t1_9,u_9,t2_10,t1_10,u_10,t2_11,t1_11,u_11,t2_12,t1_12,u_12,t2_13,t1_13,u_13,t2_14,t1_14,u_14,t2_15,t1_15,u_15,t2_16,t1_16,u_16,t2_17,t1_17,u_17,t2_18,t1_18,u_18,t2_19,t1_19,u_19,t2_20,t1_20,u_20,t2_21,t1_21,u_21,t2_22,t1_22,u_22,t2_23,t1_23,u_23,t2_24,t1_24,u_24,t2_25,t1_25,u_25,t2_26,t1_26,u_26,t2_27,t1_27,u_27,t2_28,t1_28,u_28,t2_29,t1_29,u_29,t2_30,t1_30,u_30,t2_31,t1_31,u_31;
unsigned w_0,w_1,w_2,w_3,w_4,w_5,w_6,w_7,w_8,w_9,w_10,w_11,w_12,w_13,w_14,w_15;

LDB(0,0); LDB(1,0);
LDB(0,1); LDB(1,1);
LDB(0,2); LDB(1,2);
SBAR();
MM0(z0,A[0],B0[0]); LDB(0,3); SBAR();
MM0(z1,A[0],B1[0]); LDB(1,3); SBAR();
MM0(z2,A[1],B0[0]); LDB(0,4); SBAR();
MM0(z3,A[1],B1[0]); LDB(1,4); SBAR();
MMA(z2,A[0],B0[1]);  SBAR();
MMA(z3,A[0],B1[1]);  SBAR();
MM0(z4,A[2],B0[0]); LDB(0,5); SBAR();
MM0(z5,A[2],B1[0]); LDB(1,5); SBAR();
MMA(z4,A[1],B0[1]);  SBAR();
MMA(z5,A[1],B1[1]);  SBAR();
MMA(z4,A[0],B0[2]); ROR(t2_0,zf,z0[2]); ROR(t2_1,z0[3],z1[2]); SBAR();
MMA(z5,A[0],B1[2]); SHR(t2_0,z0[3],z0[2]); SHR(t2_1,z1[3],z1[2]); SBAR();
MM0(z6,A[3],B0[0]); LDB(0,6); SBAR();
MM0(z7,A[3],B1[0]); LDB(1,6); SBAR();
MMA(z6,A[2],B0[1]); ROR(t1_0,zf,z0[1]); ROR(t1_1,t2_0,z1[1]); SBAR();
MMA(z7,A[2],B1[1]); SHR(t1_0,t2_0,z0[1]); SHR(t1_1,t2_1,z1[1]); SBAR();
MMA(z6,A[1],B0[2]); ROR(u_0,zf,z0[0]); ROR(u_1,t1_0,z1[0]); SBAR();
MMA(z7,A[1],B1[2]); SHR(u_0,t1_0,z0[0]); SHR(u_1,t1_1,z1[0]); SBAR();
MMA(z6,A[0],B0[3]); w_0=pk(u_0,u_1); PIN(w_0); SBAR();
MMA(z7,A[0],B1[3]); STY(0,w_0); SBAR();
MM0(z8,A[4],B0[0]); LDB(0,7); SBAR();
MM0(z9,A[4],B1[0]); LDB(1,7); SBAR();
MMA(z8,A[3],B0[1]); ROR(t2_2,z1[3],z2[2]); ROR(t2_3,z2[3],z3[2]); SBAR();
MMA(z9,A[3],B1[1]); SHR(t2_2,z2[3],z2[2]); SHR(t2_3,z3[3],z3[2]); SBAR();
MMA(z8,A[2],B0[2]); ROR(t1_2,t2_1,z2[1]); ROR(t1_3,t2_2,z3[1]); SBAR();
MMA(z9,A[2],B1[2]); SHR(t1_2,t2_2,z2[1]); SHR(t1_3,t2_3,z3[1]); SBAR();
MMA(z8,A[1],B0[3]); ROR(u_2,t1_1,z2[0]); ROR(u_3,t1_2,z3[0]); SBAR();
MMA(z9,A[1],B1[3]); SHR(u_2,t1_2,z2[0]); SHR(u_3,t1_3,z3[0]); SBAR();
MMA(z8,A[0],B0[4]); w_1=pk(u_2,u_3); PIN(w_1); SBAR();
MMA(z9,A[0],B1[4]); STY(1,w_1); SBAR();
MM0(z10,A[5],B0[0]); LDB(0,8); SBAR();
MM0(z11,A[5],B1[0]); LDB(1,8); SBAR();
MMA(z10,A[4],B0[1]); ROR(t2_4,z3[3],z4[2]); ROR(t2_5,z4[3],z5[2]); SBAR();
MMA(z11,A[4],B1[1]); SHR(t2_4,z4[3],z4[2]); SHR(t2_5,z5[3],z5[2]); SBAR();
MMA(z10,A[3],B0[2]); ROR(t1_4,t2_3,z4[1]); ROR(t1_5,t2_4,z5[1]); SBAR();
MMA(z11,A[3],B1[2]); SHR(t1_4,t2_4,z4[1]); SHR(t1_5,t2_5,z5[1]); SBAR();
MMA(z10,A[2],B0[3]); ROR(u_4,t1_3,z4[0]); ROR(u_5,t1_4,z5[0]); SBAR();
MMA(z11,A[2],B1[3]); SHR(u_4,t1_4,z4[0]); SHR(u_5,t1_5,z5[0]); SBAR();
MMA(z10,A[1],B0[4]); w_2=pk(u_4,u_5); PIN(w_2); SBAR();
MMA(z11,A[1],B1[4]); STY(2,w_2); SBAR();
MMA(z10,A[0],B0[5]); ROR(t2_6,z5[3],z6[2]); ROR(t2_7,z6[3],z7[2]); SBAR();
MMA(z11,A[0],B1[5]); SHR(t2_6,z6[3],z6[2]); SHR(t2_7,z7[3],z7[2]); SBAR();
MM0(z12,A[6],B0[0]); LDB(0,9); SBAR();
MM0(z13,A[6],B1[0]); LDB(1,9); SBAR();
MMA(z12,A[5],B0[1]); ROR(t1_6,t2_5,z6[1]); ROR(t1_7,t2_6,z7[1]); SBAR();
MMA(z13,A[5],B1[1]); SHR(t1_6,t2_6,z6[1]); SHR(t1_7,t2_7,z7[1]); SBAR();
MMA(z12,A[4],B0[2]); ROR(u_6,t1_5,z6[0]); ROR(u_7,t1_6,z7[0]); SBAR();
MMA(z13,A[4],B1[2]); SHR(u_6,t1_6,z6[0]); SHR(u_7,t1_7,z7[0]); SBAR();
MMA(z12,A[3],B0[3]); w_3=pk(u_6,u_7); PIN(w_3); SBAR();
MMA(z13,A[3],B1[3]); STY(3,w_3); SBAR();
MMA(z12,A[2],B0[4]); ROR(t2_8,z7[3],z8[2]); ROR(t2_9,z8[3],z9[2]); SBAR();
MMA(z13,A[2],B1[4]); SHR(t2_8,z8[3],z8[2]); SHR(t2_9,z9[3],z9[2]); SBAR();
MMA(z12,A[1],B0[5]); ROR(t1_8,t2_7,z8[1]); ROR(t1_9,t2_8,z9[1]); SBAR();
MMA(z13,A[1],B1[5]); SHR(t1_8,t2_8,z8[1]); SHR(t1_9,t2_9,z9[1]); SBAR();
MMA(z12,A[0],B0[6]); ROR(u_8,t1_7,z8[0]); ROR(u_9,t1_8,z9[0]); SBAR();
MMA(z13,A[0],B1[6]); SHR(u_8,t1_8,z8[0]); SHR(u_9,t1_9,z9[0]); SBAR();
MM0(z14,A[7],B0[0]); LDB(0,10); SBAR();
MM0(z15,A[7],B1[0]); LDB(1,10); SBAR();
MMA(z14,A[6],B0[1]); w_4=pk(u_8,u_9); PIN(w_4); SBAR();
MMA(z15,A[6],B1[1]); STY(4,w_4); SBAR();
MMA(z14,A[5],B0[2]); ROR(t2_10,z9[3],z10[2]); ROR(t2_11,z10[3],z11[2]); SBAR();
MMA(z15,A[5],B1[2]); SHR(t2_10,z10[3],z10[2]); SHR(t2_11,z11[3],z11[2]); SBAR();
MMA(z14,A[4],B0[3]); ROR(t1_10,t2_9,z10[1]); ROR(t1_11,t2_10,z11[1]); SBAR();
MMA(z15,A[4],B1[3]); SHR(t1_10,t2_10,z10[1]); SHR(t1_11,t2_11,z11[1]); SBAR();
MMA(z14,A[3],B0[4]); ROR(u_10,t1_9,z10[0]); ROR(u_11,t1_10,z11[0]); SBAR();
MMA(z15,A[3],B1[4]); SHR(u_10,t1_10,z10[0]); SHR(u_11,t1_11,z11[0]); SBAR();
MMA(z14,A[2],B0[5]); w_5=pk(u_10,u_11); PIN(w_5); SBAR();
MMA(z15,A[2],B1[5]); STY(5,w_5); SBAR();
MMA(z14,A[1],B0[6]); ROR(t2_12,z11[3],z12[2]); ROR(t2_13,z12[3],z13[2]); SBAR();
MMA(z15,A[1],B1[6]); SHR(t2_12,z12[3],z12[2]); SHR(t2_13,z13[3],z13[2]); SBAR();
MMA(z14,A[0],B0[7]); ROR(t1_12,t2_11,z12[1]); ROR(t1_13,t2_12,z13[1]); SBAR();
MMA(z15,A[0],B1[7]); SHR(t1_12,t2_12,z12[1]); SHR(t1_13,t2_13,z13[1]); SBAR();
MM0(z16,A[8],B0[0]); LDB(0,11); SBAR();
MM0(z17,A[8],B1[0]); LDB(1,11); SBAR();
MMA(z16,A[7],B0[1]); ROR(u_12,t1_11,z12[0]); ROR(u_13,t1_12,z13[0]); SBAR();
MMA(z17,A[7],B1[1]); SHR(u_12,t1_12,z12[0]); SHR(u_13,t1_13,z13[0]); SBAR();
MMA(z16,A[6],B0[2]); w_6=pk(u_12,u_13); PIN(w_6); SBAR();
MMA(z17,A[6],B1[2]); STY(6,w_6); SBAR();
MMA(z16,A[5],B0[3]);  SBAR();
MMA(z17,A[5],B1[3]);  SBAR();
MMA(z16,A[4],B0[4]); ROR(t2_14,z13[3],z14[2]); ROR(t2_15,z14[3],z15[2]); SBAR();
MMA(z17,A[4],B1[4]); SHR(t2_14,z14[3],z14[2]); SHR(t2_15,z15[3],z15[2]); SBAR();
MMA(z16,A[3],B0[5]); ROR(t1_14,t2_13,z14[1]); ROR(t1_15,t2_14,z15[1]); SBAR();
MMA(z17,A[3],B1[5]); SHR(t1_14,t2_14,z14[1]); SHR(t1_15,t2_15,z15[1]); SBAR();
MMA(z16,A[2],B0[6]); ROR(u_14,t1_13,z14[0]); ROR(u_15,t1_14,z15[0]); SBAR();
MMA(z17,A[2],B1[6]); SHR(u_14,t1_14,z14[0]); SHR(u_15,t1_15,z15[0]); SBAR();
MMA(z16,A[1],B0[7]); w_7=pk(u_14,u_15); PIN(w_7); SBAR();
MMA(z17,A[1],B1[7]); STY(7,w_7); SBAR();
MMA(z16,A[0],B0[8]);  SBAR();
MMA(z17,A[0],B1[8]);  SBAR();
MM0(z18,A[9],B0[0]); LDB(0,12); SBAR();
MM0(z19,A[9],B1[0]); LDB(1,12); SBAR();
MMA(z18,A[8],B0[1]);  SBAR();
MMA(z19,A[8],B1[1]);  SBAR();
MMA(z18,A[7],B0[2]);  SBAR();
MMA(z19,A[7],B1[2]);  SBAR();
MMA(z18,A[6],B0[3]);  SBAR();
MMA(z19,A[6],B1[3]);  SBAR();
MMA(z18,A[5],B0[4]); ROR(t2_16,z15[3],z16[2]); ROR(t2_17,z16[3],z17[2]); SBAR();
MMA(z19,A[5],B1[4]); SHR(t2_16,z16[3],z16[2]); SHR(t2_17,z17[3],z17[2]); SBAR();
MMA(z18,A[4],B0[5]); ROR(t1_16,t2_15,z16[1]); ROR(t1_17,t2_16,z17[1]); SBAR();
MMA(z19,A[4],B1[5]); SHR(t1_16,t2_16,z16[1]); SHR(t1_17,t2_17,z17[1]); SBAR();
MMA(z18,A[3],B0[6]); ROR(u_16,t1_15,z16[0]); ROR(u_17,t1_16,z17[0]); SBAR();
MMA(z19,A[3],B1[6]); SHR(u_16,t1_16,z16[0]); SHR(u_17,t1_17,z17[0]); SBAR();
MMA(z18,A[2],B0[7]); w_8=pk(u_16,u_17); PIN(w_8); SBAR();
MMA(z19,A[2],B1[7]); STY(8,w_8); SBAR();
MMA(z18,A[1],B0[8]);  SBAR();
MMA(z19,A[1],B1[8]);  SBAR();
MMA(z18,A[0],B0[9]);  SBAR();
MMA(z19,A[0],B1[9]);  SBAR();
MM0(z20,A[10],B0[0]); LDB(0,13); SBAR();
MM0(z21,A[10],B1[0]); LDB(1,13); SBAR();
MMA(z20,A[9],B0[1]);  SBAR();
MMA(z21,A[9],B1[1]);  SBAR();
MMA(z20,A[8],B0[2]);  SBAR();
MMA(z21,A[8],B1[2]);  SBAR();
MMA(z20,A[7],B0[3]);  SBAR();
MMA(z21,A[7],B1[3]);  SBAR();
MMA(z20,A[6],B0[4]); ROR(t2_18,z17[3],z18[2]); ROR(t2_19,z18[3],z19[2]); SBAR();
MMA(z21,A[6],B1[4]); SHR(t2_18,z18[3],z18[2]); SHR(t2_19,z19[3],z19[2]); SBAR();
MMA(z20,A[5],B0[5]); ROR(t1_18,t2_17,z18[1]); ROR(t1_19,t2_18,z19[1]); SBAR();
MMA(z21,A[5],B1[5]); SHR(t1_18,t2_18,z18[1]); SHR(t1_19,t2_19,z19[1]); SBAR();
MMA(z20,A[4],B0[6]); ROR(u_18,t1_17,z18[0]); ROR(u_19,t1_18,z19[0]); SBAR();
MMA(z21,A[4],B1[6]); SHR(u_18,t1_18,z18[0]); SHR(u_19,t1_19,z19[0]); SBAR();
MMA(z20,A[3],B0[7]); w_9=pk(u_18,u_19); PIN(w_9); SBAR();
MMA(z21,A[3],B1[7]); STY(9,w_9); SBAR();
MMA(z20,A[2],B0[8]);  SBAR();
MMA(z21,A[2],B1[8]);  SBAR();
MMA(z20,A[1],B0[9]);  SBAR();
MMA(z21,A[1],B1[9]);  SBAR();
MMA(z20,A[0],B0[10]);  SBAR();
MMA(z21,A[0],B1[10]);  SBAR();
MM0(z22,A[11],B0[0]); LDB(0,14); SBAR();
MM0(z23,A[11],B1[0]); LDB(1,14); SBAR();
MMA(z22,A[10],B0[1]);  SBAR();
MMA(z23,A[10],B1[1]);  SBAR();
MMA(z22,A[9],B0[2]);  SBAR();
MMA(z23,A[9],B1[2]);  SBAR();
MMA(z22,A[8],B0[3]);  SBAR();
MMA(z23,A[8],B1[3]);  SBAR();
MMA(z22,A[7],B0[4]); ROR(t2_20,z19[3],z20[2]); ROR(t2_21,z20[3],z21[2]); SBAR();
MMA(z23,A[7],B1[4]); SHR(t2_20,z20[3],z20[2]); SHR(t2_21,z21[3],z21[2]); SBAR();
MMA(z22,A[6],B0[5]); ROR(t1_20,t2_19,z20[1]); ROR(t1_21,t2_20,z21[1]); SBAR();
MMA(z23,A[6],B1[5]); SHR(t1_20,t2_20,z20[1]); SHR(t1_21,t2_21,z21[1]); SBAR();
MMA(z22,A[5],B0[6]); ROR(u_20,t1_19,z20[0]); ROR(u_21,t1_20,z21[0]); SBAR();
MMA(z23,A[5],B1[6]); SHR(u_20,t1_20,z20[0]); SHR(u_21,t1_21,z21[0]); SBAR();
MMA(z22,A[4],B0[7]); w_10=pk(u_20,u_21); PIN(w_10); SBAR();
MMA(z23,A[4],B1[7]); STY(10,w_10); SBAR();
MMA(z22,A[3],B0[8]);  SBAR();
MMA(z23,A[3],B1[8]);  SBAR();
MMA(z22,A[2],B0[9]);  SBAR();
MMA(z23,A[2],B1[9]);  SBAR();
MMA(z22,A[1],B0[10]);  SBAR();
MMA(z23,A[1],B1[10]);  SBAR();
MMA(z22,A[0],B0[11]);  SBAR();
MMA(z23,A[0],B1[11]);  SBAR();
MM0(z24,A[12],B0[0]); LDB(0,15); SBAR();
MM0(z25,A[12],B1[0]); LDB(1,15); SBAR();
MMA(z24,A[11],B0[1]);  SBAR();
MMA(z25,A[11],B1[1]);  SBAR();
MMA(z24,A[10],B0[2]);  SBAR();
MMA(z25,A[10],B1[2]);  SBAR();
MMA(z24,A[9],B0[3]);  SBAR();
MMA(z25,A[9],B1[3]);  SBAR();
MMA(z24,A[8],B0[4]); ROR(t2_22,z21[3],z22[2]); ROR(t2_23,z22[3],z23[2]); SBAR();
MMA(z25,A[8],B1[4]); SHR(t2_22,z22[3],z22[2]); SHR(t2_23,z23[3],z23[2]); SBAR();
MMA(z24,A[7],B0[5]); ROR(t1_22,t2_21,z22[1]); ROR(t1_23,t2_22,z23[1]); SBAR();
MMA(z25,A[7],B1[5]); SHR(t1_22,t2_22,z22[1]); SHR(t1_23,t2_23,z23[1]); SBAR();
MMA(z24,A[6],B0[6]); ROR(u_22,t1_21,z22[0]); ROR(u_23,t1_22,z23[0]); SBAR();
MMA(z25,A[6],B1[6]); SHR(u_22,t1_22,z22[0]); SHR(u_23,t1_23,z23[0]); SBAR();
MMA(z24,A[5],B0[7]); w_11=pk(u_22,u_23); PIN(w_11); SBAR();
MMA(z25,A[5],B1[7]); STY(11,w_11); SBAR();
MMA(z24,A[4],B0[8]);  SBAR();
MMA(z25,A[4],B1[8]);  SBAR();
MMA(z24,A[3],B0[9]);  SBAR();
MMA(z25,A[3],B1[9]);  SBAR();
MMA(z24,A[2],B0[10]);  SBAR();
MMA(z25,A[2],B1[10]);  SBAR();
MMA(z24,A[1],B0[11]);  SBAR();
MMA(z25,A[1],B1[11]);  SBAR();
MMA(z24,A[0],B0[12]);  SBAR();
MMA(z25,A[0],B1[12]);  SBAR();
MM0(z26,A[13],B0[0]);  SBAR();
MM0(z27,A[13],B1[0]);  SBAR();
MMA(z26,A[12],B0[1]);  SBAR();
MMA(z27,A[12],B1[1]);  SBAR();
MMA(z26,A[11],B0[2]);  SBAR();
MMA(z27,A[11],B1[2]);  SBAR();
MMA(z26,A[10],B0[3]);  SBAR();
MMA(z27,A[10],B1[3]);  SBAR();
MMA(z26,A[9],B0[4]); ROR(t2_24,z23[3],z24[2]); ROR(t2_25,z24[3],z25[2]); SBAR();
MMA(z27,A[9],B1[4]); SHR(t2_24,z24[3],z24[2]); SHR(t2_25,z25[3],z25[2]); SBAR();
MMA(z26,A[8],B0[5]); ROR(t1_24,t2_23,z24[1]); ROR(t1_25,t2_24,z25[1]); SBAR();
MMA(z27,A[8],B1[5]); SHR(t1_24,t2_24,z24[1]); SHR(t1_25,t2_25,z25[1]); SBAR();
MMA(z26,A[7],B0[6]); ROR(u_24,t1_23,z24[0]); ROR(u_25,t1_24,z25[0]); SBAR();
MMA(z27,A[7],B1[6]); SHR(u_24,t1_24,z24[0]); SHR(u_25,t1_25,z25[0]); SBAR();
MMA(z26,A[6],B0[7]); w_12=pk(u_24,u_25); PIN(w_12); SBAR();
MMA(z27,A[6],B1[7]); STY(12,w_12); SBAR();
MMA(z26,A[5],B0[8]);  SBAR();
MMA(z27,A[5],B1[8]);  SBAR();
MMA(z26,A[4],B0[9]);  SBAR();
MMA(z27,A[4],B1[9]);  SBAR();
MMA(z26,A[3],B0[10]);  SBAR();
MMA(z27,A[3],B1[10]);  SBAR();
MMA(z26,A[2],B0[11]);  SBAR();
MMA(z27,A[2],B1[11]);  SBAR();
MMA(z26,A[1],B0[12]);  SBAR();
MMA(z27,A[1],B1[12]);  SBAR();
MMA(z26,A[0],B0[13]);  SBAR();
MMA(z27,A[0],B1[13]);  SBAR();
MM0(z28,A[14],B0[0]);  SBAR();
MM0(z29,A[14],B1[0]);  SBAR();
MMA(z28,A[13],B0[1]);  SBAR();
MMA(z29,A[13],B1[1]);  SBAR();
MMA(z28,A[12],B0[2]);  SBAR();
MMA(z29,A[12],B1[2]);  SBAR();
MMA(z28,A[11],B0[3]);  SBAR();
MMA(z29,A[11],B1[3]);  SBAR();
MMA(z28,A[10],B0[4]); ROR(t2_26,z25[3],z26[2]); ROR(t2_27,z26[3],z27[2]); SBAR();
MMA(z29,A[10],B1[4]); SHR(t2_26,z26[3],z26[2]); SHR(t2_27,z27[3],z27[2]); SBAR();
MMA(z28,A[9],B0[5]); ROR(t1_26,t2_25,z26[1]); ROR(t1_27,t2_26,z27[1]); SBAR();
MMA(z29,A[9],B1[5]); SHR(t1_26,t2_26,z26[1]); SHR(t1_27,t2_27,z27[1]); SBAR();
MMA(z28,A[8],B0[6]); ROR(u_26,t1_25,z26[0]); ROR(u_27,t1_26,z27[0]); SBAR();
MMA(z29,A[8],B1[6]); SHR(u_26,t1_26,z26[0]); SHR(u_27,t1_27,z27[0]); SBAR();
MMA(z28,A[7],B0[7]); w_13=pk(u_26,u_27); PIN(w_13); SBAR();
MMA(z29,A[7],B1[7]); STY(13,w_13); SBAR();
MMA(z28,A[6],B0[8]);  SBAR();
MMA(z29,A[6],B1[8]);  SBAR();
MMA(z28,A[5],B0[9]);  SBAR();
MMA(z29,A[5],B1[9]);  SBAR();
MMA(z28,A[4],B0[10]);  SBAR();
MMA(z29,A[4],B1[10]);  SBAR();
MMA(z28,A[3],B0[11]);  SBAR();
MMA(z29,A[3],B1[11]);  SBAR();
MMA(z28,A[2],B0[12]);  SBAR();
MMA(z29,A[2],B1[12]);  SBAR();
MMA(z28,A[1],B0[13]);  SBAR();
MMA(z29,A[1],B1[13]);  SBAR();
MMA(z28,A[0],B0[14]);  SBAR();
MMA(z29,A[0],B1[14]);  SBAR();
MM0(z30,A[15],B0[0]);  SBAR();
MM0(z31,A[15],B1[0]);  SBAR();
MMA(z30,A[14],B0[1]);  SBAR();
MMA(z31,A[14],B1[1]);  SBAR();
MMA(z30,A[13],B0[2]);  SBAR();
MMA(z31,A[13],B1[2]);  SBAR();
MMA(z30,A[12],B0[3]);  SBAR();
MMA(z31,A[12],B1[3]);  SBAR();
MMA(z30,A[11],B0[4]); ROR(t2_28,z27[3],z28[2]); ROR(t2_29,z28[3],z29[2]); SBAR();
MMA(z31,A[11],B1[4]); SHR(t2_28,z28[3],z28[2]); SHR(t2_29,z29[3],z29[2]); SBAR();
MMA(z30,A[10],B0[5]); ROR(t1_28,t2_27,z28[1]); ROR(t1_29,t2_28,z29[1]); SBAR();
MMA(z31,A[10],B1[5]); SHR(t1_28,t2_28,z28[1]); SHR(t1_29,t2_29,z29[1]); SBAR();
MMA(z30,A[9],B0[6]); ROR(u_28,t1_27,z28[0]); ROR(u_29,t1_28,z29[0]); SBAR();
MMA(z31,A[9],B1[6]); SHR(u_28,t1_28,z28[0]); SHR(u_29,t1_29,z29[0]); SBAR();
MMA(z30,A[8],B0[7]); w_14=pk(u_28,u_29); PIN(w_14); SBAR();
MMA(z31,A[8],B1[7]); STY(14,w_14); SBAR();
MMA(z30,A[7],B0[8]);  SBAR();
MMA(z31,A[7],B1[8]);  SBAR();
MMA(z30,A[6],B0[9]);  SBAR();
MMA(z31,A[6],B1[9]);  SBAR();
MMA(z30,A[5],B0[10]);  SBAR();
MMA(z31,A[5],B1[10]);  SBAR();
MMA(z30,A[4],B0[11]);  SBAR();
MMA(z31,A[4],B1[11]);  SBAR();
MMA(z30,A[3],B0[12]);  SBAR();
MMA(z31,A[3],B1[12]);  SBAR();
MMA(z30,A[2],B0[13]);  SBAR();
MMA(z31,A[2],B1[13]);  SBAR();
MMA(z30,A[1],B0[14]);  SBAR();
MMA(z31,A[1],B1[14]);  SBAR();
MMA(z30,A[0],B0[15]);  SBAR();
MMA(z31,A[0],B1[15]);  SBAR();
NOP1();  SBAR();
NOP1();  SBAR();
NOP1();  SBAR();
NOP1();  SBAR();
NOP1();  SBAR();
NOP1();  SBAR();
NOP1();  SBAR();
NOP1();  SBAR();
NOP1(); ROR(t2_30,z29[3],z30[2]); ROR(t2_31,z30[3],z31[2]); SBAR();
NOP1(); SHR(t2_30,z30[3],z30[2]); SHR(t2_31,z31[3],z31[2]); SBAR();
NOP1(); ROR(t1_30,t2_29,z30[1]); ROR(t1_31,t2_30,z31[1]); SBAR();
NOP1(); SHR(t1_30,t2_30,z30[1]); SHR(t1_31,t2_31,z31[1]); SBAR();
NOP1(); ROR(u_30,t1_29,z30[0]); ROR(u_31,t1_30,z31[0]); SBAR();
NOP1(); SHR(u_30,t1_30,z30[0]); SHR(u_31,t1_31,z31[0]); SBAR();
NOP1(); w_15=pk(u_30,u_31); PIN(w_15); SBAR();
NOP1(); STY(15,w_15); SBAR();
      const unsigned yo=WS_Y+(unsigned)ch*4096u+lane*16u;
      u32x4 yr[4];
#pragma unroll
      for(int i=0;i<4;i++) yr[i]=*(const LAS u32x4*)(LW+256+i*1024+lane*16);
#pragma unroll
      for(int i=0;i<4;i++) __builtin_amdgcn_raw_buffer_store_b128(yr[i],wsr,yo+i*1024,0,SC1);
      WAITL();
      arrive(cnt+C_CV);
    }
    lds_wait(cnt+C_CV,4);
    wo_block(v); wo_block(4+v); if(v<2) wo_block(8+v);
    lds_wait(cnt+C_H1,2);
  } else {
    const int wr=wid>>1, wc=wid&1;
    const int r=lane&31, h=lane>>5;
    lptr L=(lptr)lds;
    lptr xa[4];
    { int row=wr*64+r; int sw=(row>>1)&7;
#pragma unroll
      for(int ks=0;ks<4;ks++) xa[ks]=L+row*128+(((2*ks+h)^sw)*16); }
    lptr wa;
    { int blk=(lane>>4)&1,q=(lane&15)>>2,p=lane&3; int c=(wc*4+2*blk+(p>>1))^(((q>>1)&1)<<2); wa=L+16384+(8*h+q)*128+c*16+8*(p&1); }
    f32x16 aq[2],ak[2],av[2];
#pragma unroll
    for(int i=0;i<2;i++){ aq[i]=f32x16{}; ak[i]=f32x16{}; av[i]=f32x16{}; }
    G1F F0,F1;
#define G1_RX(F,ks) do{ F.x0=ldsr128(xa[ks]+so); F.x1=ldsr128(xa[ks]+so+4096); }while(0)
#define G1_RW(F,ks,sg) do{ F.w[2*(sg)]=ldstr(wa+so+(sg)*8192+(16*(ks))*128); F.w[2*(sg)+1]=ldstr(wa+so+(sg)*8192+(16*(ks)+4)*128); }while(0)
#define G1_W(F,sg) cat(F.w[2*(sg)],F.w[2*(sg)+1])
#define G1_STEP(F,N,ks,LD) do{ \
    ak[0]=MFMA32(G1_W(F,1),F.x0,ak[0]); PIN(ak[0]); if(LD) G1_RX(N,ks); SBAR(); \
    ak[1]=MFMA32(G1_W(F,1),F.x1,ak[1]); PIN(ak[1]); if(LD) G1_RW(N,ks,1); SBAR(); \
    av[0]=MFMA32(F.x0,G1_W(F,2),av[0]); PIN(av[0]); if(LD) G1_RW(N,ks,2); SBAR(); \
    av[1]=MFMA32(F.x1,G1_W(F,2),av[1]); PIN(av[1]); if(LD) G1_RW(N,ks,0); SBAR(); \
    aq[0]=MFMA32(G1_W(F,0),F.x0,aq[0]); PIN(aq[0]); SBAR(); \
    aq[1]=MFMA32(G1_W(F,0),F.x1,aq[1]); PIN(aq[1]); SBAR(); }while(0)
    __builtin_amdgcn_s_setprio(2);
    asm volatile("":"+v"(aq[0]),"+v"(aq[1]),"+v"(ak[0]),"+v"(ak[1]),"+v"(av[0]),"+v"(av[1]));
    BAR();
    int so=0;
    G1_RX(F0,0); G1_RW(F0,0,1); G1_RW(F0,0,2); G1_RW(F0,0,0); SBAR();
#pragma unroll 1
    for(int t=0;t<15;t++){
      G1_STEP(F0,F1,1,true);
      G1_STEP(F1,F0,2,true);
      G1_STEP(F0,F1,3,true);
      so=(so==2*G1_SLOT)?0:so+G1_SLOT;
      asm volatile("s_waitcnt lgkmcnt(0)\n\ts_barrier":::"memory"); SBAR();
      G1_STEP(F1,F0,0,true);
    }
    G1_STEP(F0,F1,1,true);
    G1_STEP(F1,F0,2,true);
    G1_STEP(F0,F1,3,true);
    nonce=*(LAS u64*)(cnt+C_NONCE);
    WAITL();
    arrive(cnt+C_RD);
    SBAR();
    G1_STEP(F1,F0,0,false);
    const float qs=0.17677669529663687f*1.4426950408889634f;
    u32x4 kf[2][2],vf[2][2];
    const unsigned go=WS_KV+(unsigned)b*32768u+wc*16384u+(2*wr)*4096u+lane*16u;
#define KV_STORE() _Pragma("unroll") for(int rt=0;rt<2;rt++) _Pragma("unroll") for(int s=0;s<2;s++){ \
      __builtin_amdgcn_raw_buffer_store_b128(kf[rt][s],wsr,go+rt*4096+s*1024,0,SC1); __builtin_amdgcn_raw_buffer_store_b128(vf[rt][s],wsr,go+rt*4096+2048+s*1024,0,SC1); }
#pragma unroll
    for(int rt=0;rt<2;rt++)
#pragma unroll
      for(int s=0;s<2;s++){ kf[rt][s]=pack8(ak[rt],s); vf[rt][s]=pack8(av[rt],s); }
    if(wc==0){ KV_STORE(); }
    lds_wait(cnt+C_RD,4);
#pragma unroll
    for(int rt=0;rt<2;rt++){
      lwptr lo=(lwptr)lds+(1-wc)*HR+(8+2*wr+rt)*4096+lane*16;
      lwptr lq=(lwptr)lds+(1-wc)*HR+49152+(2*wr+rt)*2048+lane*16;
#pragma unroll
      for(int s=0;s<2;s++){ *(LAS u32x4*)(lo+s*1024)=kf[rt][s]; *(LAS u32x4*)(lo+2048+s*1024)=vf[rt][s]; *(LAS u32x4*)(lq+s*1024)=pack8(aq[rt],s,qs); }
    }
    if(wc==1){ lds_wait(cnt+C_KV0,2); KV_STORE(); }
    WAITV(0);
    arrive_publish(cnt+C_KV0+wc,2,flagp(ws,b,FK_KV0+wc),nonce,lane);
    const int Rs=R-2+wr;
    if(Rs>=0){
      const int bs=blk_of(Rs,C);
      wait_flags(flagp(ws,bs,FK_KV0+wc),nonce);
      const char* src=ws+WS_KV+(size_t)bs*32768+wc*16384;
#pragma unroll
      for(int i=0;i<4;i++) dma4_sc1(lane*16,src+i*4096,lds0+(1-wc)*HR+wr*16384+i*4096);
    } else {
#pragma unroll
      for(int i=0;i<16;i++) *(LAS u32x4*)((lwptr)lds+(1-wc)*HR+wr*16384+i*1024+lane*16)=u32x4{0,0,0,0};
    }
    if(wc==0){ asm volatile("s_waitcnt vmcnt(0) lgkmcnt(0)":::"memory"); arrive(cnt+C_H0); }
    lds_wait(cnt+C_H0,2);
  }
  const int late=wid>>2;
  if(R>=2) attention<false>(lds,cnt,ws,wsr,b,R,C,wid,lane,nonce);
  else     attention<true >(lds,cnt,ws,wsr,b,R,C,wid,lane,nonce);
  WAITV(0);
  arrive_publish(cnt+C_O0+late,4,flagp(ws,b,FK_O0+late),nonce,lane);
  if(!late){
    lds_wait(cnt+C_E,4);
    if(wid==3){ wo_block(22); wo_block(23); wait_flags(flagp(ws,blk_of(R2,lane&15),FK_O0),nonce); arrive(cnt+C_EF); }
    else { wo_block(10+wid*4); wo_block(11+wid*4); wo_block(12+wid*4); wo_block(13+wid*4); }
    WAITV(0);
    arrive(cnt+C_W2);
    lds_wait(cnt+C_EF,1);
    const int T2=R2*4+wid;
    const unsigned ob=WS_O+(unsigned)T2*65536u+lane*16u;
    u32x4 of[4][2];
#define G2_LDO(i) do{ const unsigned so_=(unsigned)(2*(((i)+C2)&15)+((i)>>4))*2048u; of[(i)&3][0]=__builtin_amdgcn_raw_buffer_load_b128(wsr,ob,so_,SC1); of[(i)&3][1]=__builtin_amdgcn_raw_buffer_load_b128(wsr,ob+1024,so_,SC1); }while(0)
#pragma unroll
    for(int i=0;i<4;i++) G2_LDO(i);
    const int h=lane>>5,blk=(lane>>4)&1,q=(lane&15)>>2,p=lane&3;
    lptr wa[2][2];
#pragma unroll
    for(int ct=0;ct<2;ct++){ int c=(ct*4+2*blk+(p>>1))^(((q>>1)&1)<<2); wa[ct][0]=(lptr)lds+L_WO+(4*h+q)*128+c*16+8*(p&1); wa[ct][1]=wa[ct][0]+65536; }
    f32x16 acc[2]; acc[0]=f32x16{}; acc[1]=f32x16{};
    s16x4 wf[2][8];
#define G2_LDW(i) do{ _Pragma("unroll") for(int s_=0;s_<2;s_++) _Pragma("unroll") for(int ct_=0;ct_<2;ct_++){ lptr a_=wa[ct_][(31-(i))>>4]+((31-(i))&15)*4096+s_*2048; \
      wf[(i)&1][4*s_+2*ct_]=ldstr(a_); wf[(i)&1][4*s_+2*ct_+1]=ldstr(a_+1024); } }while(0)
    lds_wait(cnt+C_W1,4);
    G2_LDW(0);
#pragma unroll
    for(int i=0;i<32;i++){
      SBAR();
      if(i==9) lds_wait(cnt+C_W2,4);
      if(i==12) lds_wait(cnt+C_LF,1);
      if(i==23) lds_wait(cnt+C_W3,4);
      if(i+1<32) G2_LDW(i+1);
#pragma unroll
      for(int s=0;s<2;s++)
#pragma unroll
        for(int ct=0;ct<2;ct++) acc[ct]=MFMA32(cat(wf[i&1][4*s+2*ct],wf[i&1][4*s+2*ct+1]),B8(of[i&3][s]),acc[ct]);
      if(i+1<32){
#pragma unroll
        for(int g_=0;g_<4;g_++){ __builtin_amdgcn_sched_group_barrier(0x100,2,0); __builtin_amdgcn_sched_group_barrier(0x8,1,0); }
      }
      PIN(acc[0]); PIN(acc[1]);
      SBAR();
      if(i+4<32) G2_LDO(i+4);
    }
    u16* orow=out+(size_t)(T2*32+(lane&31))*2048+1024+C2*64;
#pragma unroll
    for(int ct=0;ct<2;ct++){
#pragma unroll
      for(int gp=0;gp<2;gp++){
        unsigned a0=pk(acc[ct][8*gp],acc[ct][8*gp+1]), a1=pk(acc[ct][8*gp+2],acc[ct][8*gp+3]);
        unsigned b0=pk(acc[ct][8*gp+4],acc[ct][8*gp+5]), b1=pk(acc[ct][8*gp+6],acc[ct][8*gp+7]);
        auto r0=__builtin_amdgcn_permlane32_swap(a0,b0,false,false); auto r1=__builtin_amdgcn_permlane32_swap(a1,b1,false,false);
        u32x4 vv={r0[0],r1[0],r0[1],r1[1]};
        *(u32x4*)(orow+ct*32+gp*16+h*8)=vv;
      }
    }
  } else {
    const int v=wid-4;
    lds_wait(cnt+C_E,4); lds_wait(cnt+C_L,4);
    wo_block(24+v); wo_block(28+v);
    const int cg=lane&7, rq=lane>>3;
    const int ch0=xcd*128+(v&1)*64+cg*8;
    const int srow0=j*64+(v>>1)*32+rq*4;
    u32x4 qq[4]; u32x2 xx[8];
#pragma unroll
    for(int e=0;e<4;e++) qq[e]=*(const u32x4*)(Aq+(size_t)(srow0+e)*1024+ch0);
    const u32x4 dd=*(const u32x4*)(AD+ch0);
#pragma unroll
    for(int c=0;c<8;c++) xx[c]=__builtin_amdgcn_raw_buffer_load_b64(wsr,WS_XT+(unsigned)(ch0+c)*4096u+(unsigned)srow0*2u,0,SC1);
    WAITV(13);
    arrive(cnt+C_W3);
    if(v==0){ wait_flags(flagp(ws,blk_of(R2,lane&15),FK_O1),nonce); arrive(cnt+C_LF); }
    if(v==1){ wait_flags(flagp(ws,(lane&31)*8+xcd,FK_Y),nonce); arrive(cnt+C_YF); }
    lds_wait(cnt+C_YF,1);
    u32x2 y[8];
#pragma unroll
    for(int c=0;c<8;c++) y[c]=__builtin_amdgcn_raw_buffer_load_b64(wsr,WS_Y+(unsigned)(ch0+c)*4096u+(unsigned)srow0*2u,0,SC1);
#pragma unroll
    for(int e=0;e<4;e++){
      u32x4 r;
#pragma unroll
      for(int d=0;d<4;d++){
        float y0=(e&1)?hi16(y[2*d][e>>1]):lo16(y[2*d][e>>1]); float y1=(e&1)?hi16(y[2*d+1][e>>1]):lo16(y[2*d+1][e>>1]);
        float x0=(e&1)?hi16(xx[2*d][e>>1]):lo16(xx[2*d][e>>1]); float x1=(e&1)?hi16(xx[2*d+1][e>>1]):lo16(xx[2*d+1][e>>1]);
        r[d]=pk(lo16(qq[e][d])*__builtin_fmaf(x0,lo16(dd[d]),y0),hi16(qq[e][d])*__builtin_fmaf(x1,hi16(dd[d]),y1));
      }
      *(u32x4*)(out+(size_t)(srow0+e)*2048+ch0)=r;
    }
    if(b==0 && wid==7){
#pragma unroll 1
      for(int i=0;i<4;i++) wait_flags(flagp(ws,i*64+lane,FK_O1),nonce);
      if(lane==0) __hip_atomic_store((gu64*)(ws+F_E),nonce,RLX_AGENT);
    }
  }
}

extern "C" void kernel_launch(void* const* d_in, const int* in_sizes, int n_in, void* d_out, int out_size, void* d_ws, size_t ws_size, hipStream_t stream){
  (void)hipFuncSetAttribute((const void*)fused,hipFuncAttributeMaxDynamicSharedMemorySize,LDS_BYTES);
  fused<<<256,512,LDS_BYTES,stream>>>((const u16*)d_in[0],(const u16*)d_in[1],(const u16*)d_in[2],(const u16*)d_in[3],(const u16*)d_in[4],(const u16*)d_in[5],
                                      (const u16*)d_in[6],(const u16*)d_in[7],(const u16*)d_in[8],(u16*)d_out,(char*)d_ws);
}
```

```cpp
#include <hip/hip_runtime.h>
#include <hip/hip_bf16.h>
#include <stdint.h>
#if defined(__HIP_DEVICE_COMPILE__)
#define NOPK __attribute__((target("no-packed-fp32-ops")))
#else
#define NOPK
#endif
typedef unsigned short u16;
typedef unsigned long long u64;
typedef short bf16x8 __attribute__((ext_vector_type(8)));
typedef short s16x4 __attribute__((ext_vector_type(4)));
typedef float f32x16 __attribute__((ext_vector_type(16)));
typedef float f32x4 __attribute__((ext_vector_type(4)));
typedef float f32x2 __attribute__((ext_vector_type(2)));
typedef __bf16 bf2 __attribute__((ext_vector_type(2)));
typedef unsigned u32x4 __attribute__((ext_vector_type(4)));
typedef unsigned u32x2 __attribute__((ext_vector_type(2)));
#define LAS __attribute__((address_space(3)))
#define GAS __attribute__((address_space(1)))
typedef LAS const char* lptr;
typedef LAS char* lwptr;
typedef GAS u64 gu64;
#define DI __device__ __forceinline__ NOPK
#define MFMA32(a,b,c) __builtin_amdgcn_mfma_f32_32x32x16_bf16((a),(b),(c),0,0,0)
#define MFMA16(a,b,c) __builtin_amdgcn_mfma_f32_16x16x32_bf16((a),(b),(c),0,0,0)
#define SBAR() __builtin_amdgcn_sched_barrier(0)
#define PIN(x) asm volatile("":"+v"(x))
#define MEMFENCE() asm volatile("":::"memory")
#define BAR() asm volatile("s_barrier":::"memory")
#define WAITV(n) asm volatile("s_waitcnt vmcnt(" #n ")":::"memory")
#define WAITL() asm volatile("s_waitcnt lgkmcnt(0)":::"memory")
#define RLX_AGENT __ATOMIC_RELAXED, __HIP_MEMORY_SCOPE_AGENT
#define RLX_WG __ATOMIC_RELAXED, __HIP_MEMORY_SCOPE_WORKGROUP
#define SC1 16
#define XT_DIRECT
#define RAMP 99
#define NT 0
#define OAUX 16
#define B8(x) __builtin_bit_cast(bf16x8,(x))

#define PREPSZ (4*MB)
constexpr unsigned MB=1u<<20;
constexpr unsigned WS_KV=0, WS_O=8*MB, WS_XT=12*MB, WS_Y=16*MB, WS_F=20*MB, WS_END=21*MB, WS_ST=24*MB;
constexpr unsigned F_E=WS_F, F_W=WS_F+4096;
enum { FK_KV0=0, FK_KV1, FK_O0, FK_O1, FK_X, FK_Y };
constexpr int G1_SLOT=40960;
constexpr int HR=57344;
constexpr int L_CV=126976, CV_W=8480, CV_H=128, CV_X=128+4096+160;
constexpr int L_MISC=L_CV+4*CV_W;
constexpr int L_WO=L_MISC-131072;
constexpr int WO_P1=19;
static_assert(L_WO+(32-WO_P1)*4096>=HR+24576 && L_WO>=24576,"wo image");
constexpr int L_CNT=L_MISC;
constexpr int L_JUNK=L_CNT+128;
constexpr int L_MASK=L_JUNK+256;
constexpr int LDS_BYTES=L_MASK+2048;
static_assert(LDS_BYTES<=163840,"lds");
enum { C_RD=0, C_KV0=6, C_KV1, C_H0, C_H1, C_CV, C_E, C_L, C_W1, C_W2, C_W3, C_O0, C_O1, C_EF, C_LF, C_Y, C_X, C_XF, C_XR, C_YF, C_G2, C_HI, C_H0K=1, C_H0V=2, C_P1E=3, C_P1L=4 };
#define PREP_BAR(c) ((c)==1||(c)==3||(c)==4||(c)==6||(c)==8)
#define KV_PER 11
#define PFLAG ""
#define STAG 0
#define PREP0 4
#define CONV_START 30
#define CODE_LINES 256
constexpr unsigned SPIN_CAP=1u<<16, LSPIN_CAP=1u<<20;

DI unsigned pk(float a,float b){ f32x2 v={a,b}; bf2 r=__builtin_convertvector(v,bf2); return __builtin_bit_cast(unsigned,r); }
DI float asf(unsigned u){ return __builtin_bit_cast(float,u); }
DI unsigned asu(float f){ return __builtin_bit_cast(unsigned,f); }
DI bool allof(bool p){ return __builtin_amdgcn_ballot_w64(p)==~0ull; }
DI float lo16(unsigned u){ return asf(u<<16); }
DI float hi16(unsigned u){ return asf(u&0xffff0000u); }
DI bf16x8 ldsr128(lptr p){ return *(const LAS bf16x8*)p; }
DI s16x4 ldstr(lptr p){ return __builtin_amdgcn_ds_read_tr16_b64_v4i16((LAS s16x4*)p); }
DI bf16x8 cat(s16x4 a,s16x4 b){ return __builtin_shufflevector(a,b,0,1,2,3,4,5,6,7); }
DI u32x4 pack8(const f32x16& x,int s,float sc){ u32x4 r; r[0]=pk(x[8*s]*sc,x[8*s+1]*sc); r[1]=pk(x[8*s+2]*sc,x[8*s+3]*sc); r[2]=pk(x[8*s+4]*sc,x[8*s+5]*sc); r[3]=pk(x[8*s+6]*sc,x[8*s+7]*sc); return r; }
DI u32x4 pack8(const f32x16& x,int s){ u32x4 r; r[0]=pk(x[8*s],x[8*s+1]); r[1]=pk(x[8*s+2],x[8*s+3]); r[2]=pk(x[8*s+4],x[8*s+5]); r[3]=pk(x[8*s+6],x[8*s+7]); return r; }
#define DMA4_BODY(FL) "s_mov_b32 m0, %2\n\ts_nop 0\n\t" \
   "global_load_lds_dwordx4 %0, %1" FL "\n\tglobal_load_lds_dwordx4 %0, %1 offset:1024" FL "\n\tglobal_load_lds_dwordx4 %0, %1 offset:2048" FL "\n\tglobal_load_lds_dwordx4 %0, %1 offset:3072" FL
DI void dma4(unsigned voff,const void* sbase,unsigned ldsaddr){ asm volatile(DMA4_BODY(" nt") :: "v"(voff), "s"(sbase), "s"(ldsaddr) : "memory"); }
DI void dma4_sc1(unsigned voff,const void* sbase,unsigned ldsaddr){ asm volatile(DMA4_BODY(" sc1") :: "v"(voff), "s"(sbase), "s"(ldsaddr) : "memory"); }
DI void dma4v(unsigned v0,unsigned v1,unsigned v2,unsigned v3,const void* sbase,unsigned ldsaddr){
  asm volatile("s_mov_b32 m0, %5\n\ts_nop 0\n\t"
    "global_load_lds_dwordx4 %0, %4\n\tglobal_load_lds_dwordx4 %1, %4 offset:1024\n\tglobal_load_lds_dwordx4 %2, %4 offset:2048\n\tglobal_load_lds_dwordx4 %3, %4 offset:3072"
    :: "v"(v0),"v"(v1),"v"(v2),"v"(v3), "s"(sbase), "s"(ldsaddr) : "memory");
}
DI void dma4v_sc1(unsigned v0,unsigned v1,unsigned v2,unsigned v3,const void* sbase,unsigned ldsaddr){
  asm volatile("s_mov_b32 m0, %5\n\ts_nop 0\n\t"
    "global_load_lds_dwordx4 %0, %4 sc1\n\tglobal_load_lds_dwordx4 %1, %4 offset:1024 sc1\n\tglobal_load_lds_dwordx4 %2, %4 offset:2048 sc1\n\tglobal_load_lds_dwordx4 %3, %4 offset:3072 sc1"
    :: "v"(v0),"v"(v1),"v"(v2),"v"(v3), "s"(sbase), "s"(ldsaddr) : "memory");
}
DI void touch(unsigned voff,const void* sbase,unsigned junk){
  asm volatile("s_mov_b32 m0, %2\n\ts_nop 0\n\tglobal_load_lds_dword %0, %1" :: "v"(voff), "s"(sbase), "s"(junk) : "memory");
}
DI void touch64(const void* a,unsigned junk){
  asm volatile("s_mov_b32 m0, %1\n\ts_nop 0\n\tglobal_load_lds_dword %0, off" :: "v"(a), "s"(junk) : "memory");
}
DI bool flags_ready(const gu64* p,u64 nonce){ u64 v=__hip_atomic_load(p,RLX_AGENT); bool r=allof(v==nonce); MEMFENCE(); return r; }
template<int SL=2>
DI void wait_flags(const gu64* p,u64 nonce){
#pragma unroll 1
  for(unsigned sp=0;sp<SPIN_CAP;sp++){
    u64 v=__hip_atomic_load(p,RLX_AGENT);
    if(allof(v==nonce)) break;
    __builtin_amdgcn_s_sleep(SL);
  }
  MEMFENCE();
}
DI void lds_wait(LAS unsigned* p,unsigned n){
#pragma unroll 1
  for(unsigned sp=0;sp<LSPIN_CAP;sp++){
    unsigned v=__hip_atomic_load(p,RLX_WG);
    if(__builtin_amdgcn_readfirstlane(v)>=n) break;
    __builtin_amdgcn_s_sleep(1);
  }
  MEMFENCE();
}
DI void arrive(LAS unsigned* cnt){
  unsigned one=1u;
  asm volatile("s_mov_b64 exec, 1\n\tds_add_u32 %0, %1\n\ts_mov_b64 exec, -1" :: "v"(cnt), "v"(one) : "memory");
}
DI void arrive_publish(LAS unsigned* cnt,unsigned n,gu64* flag,u64 nonce,int lane){
  unsigned one=1u, old;
  asm volatile("s_mov_b64 exec, 1\n\tds_add_rtn_u32 %0, %1, %2\n\ts_mov_b64 exec, -1\n\ts_waitcnt lgkmcnt(0)" : "=&v"(old) : "v"(cnt), "v"(one) : "memory");
  if(__builtin_amdgcn_readfirstlane(old)==n-1){ if(lane==0) __hip_atomic_store(flag,nonce,RLX_AGENT); }
  MEMFENCE();
}
DI gu64* flagp(char* ws,int blk,int k){ return (gu64*)(ws+F_W+(unsigned)(blk*8+k)*128u); }
DI int blk_of(int R,int C){ return (((R&7)*4+(C&3))<<3)|((R>>3)*4+(C>>2)); }
DI float fmax3(float a,float b,float c){ return __builtin_fmaxf(__builtin_fmaxf(a,b),c); }
DI float xhalf_max(float m){ auto rr=__builtin_amdgcn_permlane32_swap(asu(m),asu(m),false,false); return __builtin_fmaxf(asf(rr[0]),asf(rr[1])); }
struct G1F { bf16x8 x0,x1; s16x4 w[6]; };

DI const gu64* kvflag(char* ws,int R,int C,int kind,int head,int lane){
  const int Cs=(C&12)|((C-1-kind)&3); const int rr=(lane<3)? lane : 2; const int Rl=(R-2+rr<0)? R : R-2+rr;
  return flagp(ws,blk_of(Rl,Cs),FK_KV0+head);
}
template<bool BOTH>
DI void kv_fetch(char* ws,unsigned lds0,int R,int C,int kind,int head,int half,int lane){
  const int Cs=(C&12)|((C-1-kind)&3);
#pragma unroll 1
  for(int i=(R<2)?2-R:0;i<3;i++){
    const char* src=ws+WS_KV+(size_t)blk_of(R-2+i,Cs)*32768+head*16384+kind*8192+half*4096;
    const unsigned dst=lds0+(1-head)*HR+(1-kind)*24576+i*8192+half*4096;
    dma4_sc1(lane*16,src,dst); if(BOTH) dma4_sc1(lane*16,src+4096,dst+4096);
  }
}
DI void o_touch(char* ws,unsigned junk,int R2,int j,int head,int lane,u64 nonce){
  wait_flags<4>(flagp(ws,blk_of(R2,2*(j&7)+(lane&1)),FK_O0+head),nonce);
#pragma unroll
  for(int k=0;k<2;k++){
    const unsigned off=WS_O+(unsigned)(R2*4+(lane>>4))*65536u+(unsigned)((2*(j&7)+k)*4096+head*2048+(lane&15)*128);
    asm volatile("s_mov_b32 m0, %2\n\ts_nop 0\n\tglobal_load_lds_dword %0, %1 sc1" :: "v"(off), "s"(ws), "s"(junk) : "memory");
  }
}
template<bool CHECK>
DI void attention(char* lds,LAS unsigned* cnt,char* ws,__amdgpu_buffer_rsrc_t wsr,int b,int R,int C,int wid,int lane,u64 nonce){
  const int late=wid>>2, jq=wid&3;
  const int T=4*R+jq;
  lptr VL=(lptr)lds+(1-late)*HR+jq*2048+lane*16;
  lptr KL=VL+24576;
  lptr QL=(lptr)lds+(1-late)*HR+49152+jq*2048+lane*16;
  const f32x16 zero=f32x16{};
  const float ninf=-__builtin_inff();
  const int dl=(lane&31)-4*(lane>>5);
  bf16x8 kf[9][2];
  const bf16x8 q0=ldsr128(QL), q1=ldsr128(QL+1024);
  kf[8][0]=ldsr128(KL+8*2048); kf[8][1]=ldsr128(KL+8*2048+1024);
#pragma unroll
  for(int u=0;u<4;u++){ kf[u][0]=ldsr128(KL+u*2048); kf[u][1]=ldsr128(KL+u*2048+1024); }
  f32x16 sd,sm,s[3];
  float m;
#pragma unroll
  for(int n=0;n<11;n++){
    SBAR();
    if(n==0){ sd=MFMA32(kf[8][0],q0,zero); sd=MFMA32(kf[8][1],q1,sd); PIN(sd); }
    else if(n==1){ sm=MFMA32(kf[0][0],q0,zero); sm=MFMA32(kf[0][1],q1,sm); PIN(sm); }
    else if(n<9){ const int u=n-1; s[u%3]=MFMA32(kf[u][0],q0,zero); s[u%3]=MFMA32(kf[u][1],q1,s[u%3]); PIN(s[u%3]); }
    if(n<4){ const int u=n+4; kf[u][0]=ldsr128(KL+u*2048); kf[u][1]=ldsr128(KL+u*2048+1024); }
    if(n==3){
#pragma unroll
      for(int i=0;i<16;i++){ const int a=(i&3)+8*(i>>2); sm[i]=(a<=dl)?sd[i]:((CHECK&&T<8)?ninf:sm[i]); }
    }
    if(n>=3){
      const int t=n-3; const f32x16& x=(t==0)?sm:s[t%3];
      float a=fmax3(x[0],x[1],x[2]), c=fmax3(x[3],x[4],x[5]);
      a=fmax3(a,x[6],x[7]); c=fmax3(c,x[8],x[9]); a=fmax3(a,x[10],x[11]); c=fmax3(c,x[12],x[13]); a=fmax3(a,x[14],x[15]);
      if(t==0) m=__builtin_fmaxf(a,c);
      else { const float mm=fmax3(m,a,c); m=(!CHECK||T-8+t>=0)?mm:m; }
      PIN(m);
    }
  }
  SBAR();
  arrive(cnt+(late?C_P1L:C_P1E));
  if(late){
    if(jq==0){ if(flags_ready(flagp(ws,(lane&31)*8+(b&7),FK_Y),nonce)) arrive(cnt+C_YF); }
    WAITV(0); arrive(cnt+C_W1); lds_wait(cnt+C_E,4);
  }
  bf16x8 vfr[2][2];
  vfr[0][0]=ldsr128(VL+8*2048); vfr[0][1]=ldsr128(VL+8*2048+1024);
  vfr[1][0]=ldsr128(VL); vfr[1][1]=ldsr128(VL+1024);
  const u32x4 mk0=*(const LAS u32x4*)((lptr)lds+L_MASK+lane*16), mk1=*(const LAS u32x4*)((lptr)lds+L_MASK+1024+lane*16);
  m=xhalf_max(m);
  f32x16 negm;
#pragma unroll
  for(int i=0;i<16;i++) negm[i]=-m;
  PIN(negm);
  const unsigned one2=((((lane>>2)^(lane>>4))&1)==0)?0x3f803f80u:0u;
  const u32x4 ones={one2,one2,one2,one2};
  f32x16 o; f32x4 ls;
  u32x4 p0,p1;
#pragma unroll
  for(int u=0;u<=8;u++){
    SBAR();
    u32x4 n0,n1;
    if(u+1<8){ const int t=u+1; s[t&1]=MFMA32(kf[t][0],q0,negm); s[t&1]=MFMA32(kf[t][1],q1,s[t&1]); }
    if(u<8){
      f32x16& x=(u==0)?sm:s[u&1];
#pragma unroll
      for(int i=0;i<16;i++) x[i]=__builtin_amdgcn_exp2f((u==0)?x[i]-m:x[i]);
      n0=pack8(x,0); n1=pack8(x,1);
      if(CHECK && u>0){ if(T-8+u<0){ n0=u32x4{0,0,0,0}; n1=u32x4{0,0,0,0}; } }
    }
    if(u==1){
      const u32x4 d0=p0&mk0, d1=p1&mk1;
      o=MFMA32(vfr[0][0],B8(d0),zero); o=MFMA32(vfr[0][1],B8(d1),o);
      o=MFMA32(vfr[1][0],B8(p0^d0),o); o=MFMA32(vfr[1][1],B8(p1^d1),o);
      vfr[1][0]=ldsr128(VL+1*2048); vfr[1][1]=ldsr128(VL+1*2048+1024);
      vfr[0][0]=ldsr128(VL+2*2048); vfr[0][1]=ldsr128(VL+2*2048+1024);
      ls=MFMA16(B8(ones),B8(p0),(f32x4{0,0,0,0})); ls=MFMA16(B8(ones),B8(p1),ls);
    } else if(u>1){
      const int t=u-1;
      o=MFMA32(vfr[t&1][0],B8(p0),o); o=MFMA32(vfr[t&1][1],B8(p1),o);
      if(t+2<8){ vfr[t&1][0]=ldsr128(VL+(t+2)*2048); vfr[t&1][1]=ldsr128(VL+(t+2)*2048+1024); }
      ls=MFMA16(B8(ones),B8(p0),ls); ls=MFMA16(B8(ones),B8(p1),ls);
    }
    if(u>1 && u<7 && !CHECK){
#pragma unroll
      for(int g_=0;g_<6;g_++){ __builtin_amdgcn_sched_group_barrier(0x8,1,0); __builtin_amdgcn_sched_group_barrier(0x402,4,0); }
    }
    if(u+1<8) PIN(s[(u+1)&1]);
    if(u>0){ PIN(ls); PIN(o); }
    if(u<8){ PIN(n0); PIN(n1); p0=n0; p1=n1; }
  }
  SBAR();
  arrive(cnt+(late?C_L:C_E));
  const float il=__builtin_amdgcn_rcpf(ls[0]);
  const unsigned oo=WS_O+(unsigned)(T*64+2*(2*C+late))*1024u+lane*16u;
  __builtin_amdgcn_raw_buffer_store_b128(pack8(o,0,il),wsr,oo,0,SC1);
  __builtin_amdgcn_raw_buffer_store_b128(pack8(o,1,il),wsr,oo+1024,0,SC1);
}

__global__ __launch_bounds__(512,2) NOPK void fused(const u16* Aq,const u16* Ak,const u16* Av,const u16* Ask,const u16* Ah,const u16* AD,
                                               const u16* X,const u16* W,const u16* WO,u16* out,char* ws){
  extern __shared__ __attribute__((aligned(16))) char lds[];
  const char* kpc; asm volatile("s_getpc_b64 %0":"=s"(kpc));
  asm volatile(""::"s"(X),"s"(W),"s"(ws),"s"(WO),"s"(Ak));
  const int tid=threadIdx.x, lane=tid&63; const int wid=__builtin_amdgcn_readfirstlane(tid>>6);
  const int b=blockIdx.x, xcd=b&7, j=b>>3;
  const int R=(xcd>>2)*8+(j>>2), C=(xcd&3)*4+(j&3);
  const int wgi=xcd*32+j;
  const int R2=(xcd>>1)*4+(j>>3), C2=(xcd&1)*8+(j&7), KO=2*(j&7)+4*(j>>3);
  const unsigned lds0=(unsigned)(uintptr_t)lds;
  LAS unsigned* cnt=(LAS unsigned*)((lwptr)lds+L_CNT);
  const __amdgpu_buffer_rsrc_t wsr=__builtin_amdgcn_make_buffer_rsrc(ws,0,WS_END,0x00020000);
  const __amdgpu_buffer_rsrc_t outr=__builtin_amdgcn_make_buffer_rsrc(out,0,8*MB,0x00020000);
  u64 nonce;
#define LOAD_NONCE() do{ asm volatile("s_load_dwordx2 %0, %1, 0x0\n\ts_waitcnt lgkmcnt(0)" : "=s"(nonce) : "s"(ws+F_E) : "memory"); \
    nonce=nonce*6364136223846793005ull+1442695040888963407ull; }while(0)
  auto wo_block=[&](int i) NOPK __attribute__((always_inline)) {
    const int hd=2*((i+KO)&15)+(i>>4);
    const unsigned vo=(unsigned)((lane>>3)*2048+C2*128+(((lane&7)^(((lane>>4)&1)<<2))*16));
    dma4v(vo,vo+16384-1024,vo+2*(16384-1024),vo+3*(16384-1024),(const char*)WO+(size_t)hd*65536,lds0+L_WO+(31-i)*4096);
  };
  if(__builtin_expect(wid>=4,1)){
    const int v=wid-4;
    const unsigned junk=lds0+L_JUNK;
    if(__builtin_expect(wid==4,1)){
      const unsigned a0=(unsigned)((lane>>3)*2048+(((lane&7)^(lane>>4))*16)), a1=(a0^64u)+(16384-1024);
      const unsigned b0=(unsigned)((lane>>3)*6144+(((lane&7)^(((lane>>4)&1)<<2))*16));
      const char* xb=(const char*)X+(size_t)R*128*2048; const char* wb=(const char*)W+(C&12)*128;
      const int cs[3]={(C&3)*128,2048+((C+1)&3)*128,4096+((C+2)&3)*128};
      unsigned la=lds0;
#pragma unroll 1
      for(int k=-2;k<14;k++){
        if(k>=0){ WAITV(40); BAR(); }
#pragma unroll
        for(int g=0;g<4;g++){
          dma4v(a0,a1,a0+2*(16384-1024),a1+2*(16384-1024),xb+g*65536,la+g*4096);
          if(g<3){
            dma4v(b0,b0+(49152-1024),b0+2*(49152-1024),b0+3*(49152-1024),wb+cs[g],la+16384+g*8192);
            dma4v(b0,b0+(49152-1024),b0+2*(49152-1024),b0+3*(49152-1024),wb+cs[g]+32*6144,la+16384+g*8192+4096);
          }
        }
        xb+=128; wb+=64*6144; la+=G1_SLOT; if(la==lds0+3*G1_SLOT) la=lds0;
      }
      LOAD_NONCE();
      WAITV(40); BAR();
    } else if(wid==5){
      __builtin_amdgcn_s_setprio(3);
      const char* ta; unsigned tstep;
      { const int sg=lane/20, i=lane-sg*20; const bool isx=i<8; const int rem=i-8, kr=(xcd>>2)*32+j;
        tstep=isx?128u:64u*6144u;
        ta=(isx? (const char*)X+(size_t)((xcd>>2)*1024+((xcd&3)*32+j)*8+i)*2048 : (const char*)W+(kr*6144+(rem>>2)*2048+((xcd&3)*4+(rem&3))*128))+(size_t)tstep*(1+sg);
        tstep*=3; }
#define STOUCH() do{ if(lane<60) touch64(ta,junk); ta+=tstep; }while(0)
      __builtin_amdgcn_s_sleep(10);
      STOUCH();
      if(lane<CODE_LINES/32) touch((unsigned)((j*(CODE_LINES/32)+lane)*128),kpc,junk);
      if(lane<6) touch((unsigned)(F_W+(b*8+lane)*128),ws,junk);
      if(j==0) *(LAS u32x4*)((lwptr)lds+L_CV+lane*16)=u32x4{0,0,0,0};
      if(lane<28) cnt[lane]=0u;
      { const int bq=lane&31, hi=lane>>5; u32x4 mk[2];
#pragma unroll
        for(int d=0;d<8;d++){ const int i=2*d; const int a=(i&3)+8*(i>>2)+4*hi; mk[d>>2][d&3]=((a<=bq)?0xffffu:0u)|((a+1<=bq)?0xffff0000u:0u); }
        *(LAS u32x4*)((lwptr)lds+L_MASK+lane*16)=mk[0]; *(LAS u32x4*)((lwptr)lds+L_MASK+1024+lane*16)=mk[1]; }
      LOAD_NONCE();
      BAR();
      int pc=(j==0)?1:0;
#pragma unroll 1
      for(int i=0;i<12;i++){
        if(i<4) STOUCH();
#pragma unroll 1
        for(;pc<33 && pc<(i+1)*KV_PER;pc++){
          const bool isk=pc<17; const int r0=isk? 4*pc-4 : 4*(pc-17);
          const unsigned vo=(unsigned)((lane>>4)*2048+(((lane&15)^(((r0>>3)&7)<<1))*16));
          const char* src=(const char*)(isk?Ak:Av)+(size_t)(j*64+r0)*2048+xcd*256;
          asm volatile("s_mov_b32 m0, %2\n\ts_nop 0\n\tglobal_load_lds_dwordx4 %0, %1" PFLAG :: "v"(vo), "s"(src), "s"(lds0+L_CV+pc*1024) : "memory");
        }
        if(i==PREP0-1) WAITV(1);
        BAR();
      }
      u64 fxv=__hip_atomic_load(flagp(ws,(lane&31)*8+xcd,FK_X),RLX_AGENT);
      BAR();
      SBAR(); PIN(fxv);
      if(allof(fxv==nonce)){ arrive(cnt+C_XF); WAITL(); }
      BAR();
      __builtin_amdgcn_s_setprio(0);
    } else {
      __builtin_amdgcn_s_setprio(1);
      const int hv=wid-6, rg=lane&7;
      u32x4 sk[4];
      LOAD_NONCE();
      BAR();
#pragma unroll
      for(int i=0;i<4;i++) sk[i]=*(const u32x4*)(Ask+(xcd*128+(hv*16+(i>>1)*8+(lane>>3))*4)*4+(i&1)*8);
#pragma unroll 1
      for(int i=0;i<PREP0;i++) BAR();
      SBAR();
      asm volatile("":"+v"(sk[0]),"+v"(sk[1]),"+v"(sk[2]),"+v"(sk[3]));
#pragma unroll 1
      for(int g=0;g<2;g++){
        const int c4=hv*16+g*8+(lane>>3);
        u32x2 kk[11], vv[8];
        lptr kb=(lptr)lds+L_CV+(8*rg+4)*256+(c4&1)*8;
#pragma unroll
        for(int i=0;i<3;i++) kk[i]=*(const LAS u32x2*)(kb+(i-3)*256+(((c4>>1)^(((rg-1)&7)<<1))*16));
#pragma unroll
        for(int i=0;i<8;i++){ kk[3+i]=*(const LAS u32x2*)(kb+i*256+(((c4>>1)^(rg<<1))*16)); vv[i]=*(const LAS u32x2*)(kb+16384+i*256+(((c4>>1)^(rg<<1))*16)); }
#pragma unroll
        for(int c=0;c<4;c++){
          const unsigned w01=sk[c>>1][(c&1)*2], w23=sk[c>>1][(c&1)*2+1];
          const float w0=lo16(w01),w1=hi16(w01),w2=lo16(w23),w3=hi16(w23);
          float kf[11];
#pragma unroll
          for(int i=0;i<11;i++) kf[i]=(c&1)?hi16(kk[i][c>>1]):lo16(kk[i][c>>1]);
          float o[8];
#pragma unroll
          for(int i=0;i<8;i++){ float vf=(c&1)?hi16(vv[i][c>>1]):lo16(vv[i][c>>1]); o[i]=(kf[i]*w3+kf[i+1]*w2+kf[i+2]*w1+kf[i+3]*w0)*vf; }
          u32x4 r; r[0]=pk(o[0],o[1]); r[1]=pk(o[2],o[3]); r[2]=pk(o[4],o[5]); r[3]=pk(o[6],o[7]);
          __builtin_amdgcn_raw_buffer_store_b128(r,wsr,WS_XT+(unsigned)(xcd*128+c4*4+c)*4096u+(unsigned)(j*128+rg*16),0,SC1);
          SBAR();
          if(c!=1){ BAR(); SBAR(); }
        }
        sk[0]=sk[2]; sk[1]=sk[3];
      }
      WAITV(0);
      BAR();
      if(hv==0 && lane==0) __hip_atomic_store(flagp(ws,b,FK_X),nonce,RLX_AGENT);
#pragma unroll 1
      for(int i=PREP0+8;i<=13;i++) BAR();
      __builtin_amdgcn_s_setprio(0);
      __builtin_amdgcn_s_sleep(8);
      dma4(lane*16,(const char*)Ah+(size_t)(wgi*4+2*hv)*4096,lds0+L_CV+(2*hv)*CV_W+CV_H);
      dma4(lane*16,(const char*)Ah+(size_t)(wgi*4+2*hv+1)*4096,lds0+L_CV+(2*hv+1)*CV_W+CV_H);
      BAR();
    }
    const int ch=wgi*4+v;
    const float Dc=lo16(AD[ch]);
    if(lane<32) *(LAS unsigned*)((lwptr)lds+L_CV+v*CV_W+lane*4)=0u;
    if(lane<40) *(LAS unsigned*)((lwptr)lds+L_CV+v*CV_W+CV_H+4096+lane*4)=0u;
    const bool xr=__builtin_amdgcn_readfirstlane(cnt[C_XF])!=0;
    if(xr){ dma4_sc1(lane*16,ws+WS_XT+(size_t)ch*4096,lds0+L_CV+v*CV_W+CV_X); WAITV(4); }
    else WAITV(0);
    __builtin_amdgcn_s_setprio(1);
    BAR();
    if(!xr){ wait_flags<4>(flagp(ws,(lane&31)*8+xcd,FK_X),nonce); dma4_sc1(lane*16,ws+WS_XT+(size_t)ch*4096,lds0+L_CV+v*CV_W+CV_X); }
    bool h1;
    {
      lwptr LW=(lwptr)lds+L_CV+v*CV_W;
      const int g=lane>>4,q=(lane&15)>>2,p=lane&3;
      lptr ha=(lptr)LW+CV_H+8*(4*p+8*g+q-12);
      lptr xa=(lptr)LW+CV_X+8*(p+8-8*g-q);
      PIN(ha); PIN(xa);
      bf16x8 A[16],B0[16],B1[16];
#pragma unroll
      for(int d=0;d<16;d++) A[d]=cat(ldstr(ha+256*d),ldstr(ha+256*d+32));
      WAITV(0);
      float zf=0.f; PIN(zf);
#define MM0(z,a,b) asm volatile("v_mfma_f32_16x16x32_bf16 %0, %1, %2, 0" : "=&v"(z) : "v"(a), "v"(b))
#define MMA(z,a,b) asm volatile("v_mfma_f32_16x16x32_bf16 %0, %1, %2, %0" : "+v"(z) : "v"(a), "v"(b))
#define LDB(P,e) B##P[e]=cat(ldstr(xa+256*(e)+128*(P)+32),ldstr(xa+256*(e)+128*(P)))
#define ROR(d,s,z) asm volatile("v_add_f32_dpp %0, %1, %2 wave_ror:1 row_mask:0xf bank_mask:0xf" : "=&v"(d) : "v"(s), "v"(z))
#define SHR(d,s,z) asm volatile("v_add_f32_dpp %0, %1, %2 wave_shr:1 row_mask:0xf bank_mask:0xf" : "+v"(d) : "v"(s), "v"(z))
#define ROR2(d,s,z,e,t,y) asm volatile("v_add_f32_dpp %0, %2, %3 wave_ror:1 row_mask:0xf bank_mask:0xf\n\tv_add_f32_dpp %1, %4, %5 wave_ror:1 row_mask:0xf bank_mask:0xf" : "=&v"(d), "=&v"(e) : "v"(s), "v"(z), "v"(t), "v"(y))
#define SHR2(d,s,z,e,t,y) asm volatile("v_add_f32_dpp %0, %2, %3 wave_shr:1 row_mask:0xf bank_mask:0xf\n\tv_add_f32_dpp %1, %4, %5 wave_shr:1 row_mask:0xf bank_mask:0xf" : "+v"(d), "+v"(e) : "v"(s), "v"(z), "v"(t), "v"(y))
      const unsigned yo=WS_Y+(unsigned)ch*4096u+lane*16u;
#define STY(C) __builtin_amdgcn_raw_buffer_store_b128(yw,wsr,yo+(C)*1024,0,SC1)
#define NOP1() asm volatile("s_nop 0")
#define VNOP() __builtin_amdgcn_s_setprio(1)
      unsigned xa_,xb_;
      lptr xe=(lptr)LW+CV_X+2*lane; PIN(xe);
#define LDX(c) do{ xa_=*(const LAS u16*)(xe+256*(c)); xb_=*(const LAS u16*)(xe+256*(c)+128); }while(0)
#define SHX() do{ xa_<<=16; xb_<<=16; PIN(xa_); PIN(xb_); }while(0)
#define FMD(z,x) do{ z[0]=__builtin_fmaf(asf(x),Dc,z[0]); PIN(z); }while(0)
      const gu64* kf1=kvflag(ws,R,C,v>>1,1,lane);
      u64 f1v; unsigned c1v;
#define HOOK1() do{ f1v=__hip_atomic_load(kf1,RLX_AGENT); c1v=*(volatile LAS unsigned*)(cnt+C_KV1); }while(0)
#define HOOK2() do{ h1=allof(f1v==nonce) && __builtin_amdgcn_readfirstlane(c1v)>=2; MEMFENCE(); if(h1) kv_fetch<false>(ws,lds0,R,C,v>>1,1,v&1,lane); }while(0)
      SBAR();
f32x4 z0,z1,z2,z3,z4,z5,z6,z7,z8,z9,z10,z11,z12,z13,z14,z15,z16,z17,z18,z19,z20,z21,z22,z23,z24,z25,z26,z27,z28,z29,z30,z31;
float t2_0,t1_0,u_0,t2_1,t1_1,u_1,t2_2,t1_2,u_2,t2_3,t1_3,u_3,t2_4,t1_4,u_4,t2_5,t1_5,u_5,t2_6,t1_6,u_6,t2_7,t1_7,u_7,t2_8,t1_8,u_8,t2_9,t1_9,u_9,t2_10,t1_10,u_10,t2_11,t1_11,u_11,t2_12,t1_12,u_12,t2_13,t1_13,u_13,t2_14,t1_14,u_14,t2_15,t1_15,u_15,t2_16,t1_16,u_16,t2_17,t1_17,u_17,t2_18,t1_18,u_18,t2_19,t1_19,u_19,t2_20,t1_20,u_20,t2_21,t1_21,u_21,t2_22,t1_22,u_22,t2_23,t1_23,u_23,t2_24,t1_24,u_24,t2_25,t1_25,u_25,t2_26,t1_26,u_26,t2_27,t1_27,u_27,t2_28,t1_28,u_28,t2_29,t1_29,u_29,t2_30,t1_30,u_30,t2_31,t1_31,u_31;
u32x4 yw;

LDB(0,0); LDB(1,0);
LDB(0,1); LDB(1,1);
LDB(0,2); LDB(1,2);
LDX(0);
SBAR();
MM0(z0,A[0],B0[0]); SBAR(); LDB(0,3); SBAR();
MM0(z1,A[0],B1[0]); SBAR(); LDB(1,3); SBAR();
MM0(z2,A[1],B0[0]); SBAR(); LDB(0,4); SBAR();
MM0(z3,A[1],B1[0]); SBAR(); LDB(1,4); SBAR();
MMA(z2,A[0],B0[1]); SBAR();  SBAR();
MMA(z3,A[0],B1[1]); SBAR();  SBAR();
MM0(z4,A[2],B0[0]); SBAR();  SBAR();
MM0(z5,A[2],B1[0]); SBAR();  SBAR();
MMA(z4,A[1],B0[1]); SBAR();  SBAR();
MMA(z5,A[1],B1[1]); SBAR();  SBAR();
MMA(z4,A[0],B0[2]); SBAR(); ROR2(t2_0,zf,z0[2],t2_1,z0[3],z1[2]); SBAR();
MMA(z5,A[0],B1[2]); SBAR(); LDB(0,5); SBAR();
MM0(z6,A[3],B0[0]); SBAR(); SHR2(t2_0,z0[3],z0[2],t2_1,z1[3],z1[2]); SBAR();
MM0(z7,A[3],B1[0]); SBAR(); LDB(1,5); SBAR();
MMA(z6,A[2],B0[1]); SBAR(); ROR2(t1_0,zf,z0[1],t1_1,t2_0,z1[1]); SBAR();
MMA(z7,A[2],B1[1]); SBAR(); SHX(); SBAR();
MMA(z6,A[1],B0[2]); SBAR(); SHR2(t1_0,t2_0,z0[1],t1_1,t2_1,z1[1]); SBAR();
MMA(z7,A[1],B1[2]); SBAR(); FMD(z0,xa_); FMD(z1,xb_); SBAR();
MMA(z6,A[0],B0[3]); SBAR(); ROR2(u_0,zf,z0[0],u_1,t1_0,z1[0]); SBAR();
MMA(z7,A[0],B1[3]); SBAR(); LDX(1); SBAR();
MM0(z8,A[4],B0[0]); SBAR(); SHR2(u_0,t1_0,z0[0],u_1,t1_1,z1[0]); SBAR();
MM0(z9,A[4],B1[0]); SBAR(); yw[0]=pk(u_0,u_1); PIN(yw); SBAR();
MMA(z8,A[3],B0[1]); SBAR(); ROR2(t2_2,z1[3],z2[2],t2_3,z2[3],z3[2]); SBAR();
MMA(z9,A[3],B1[1]); SBAR(); LDB(0,6); SBAR();
MMA(z8,A[2],B0[2]); SBAR(); SHR2(t2_2,z2[3],z2[2],t2_3,z3[3],z3[2]); SBAR();
MMA(z9,A[2],B1[2]); SBAR(); LDB(1,6); SBAR();
MMA(z8,A[1],B0[3]); SBAR(); ROR2(t1_2,t2_1,z2[1],t1_3,t2_2,z3[1]); SBAR();
MMA(z9,A[1],B1[3]); SBAR(); SHX(); SBAR();
MMA(z8,A[0],B0[4]); SBAR(); SHR2(t1_2,t2_2,z2[1],t1_3,t2_3,z3[1]); SBAR();
MMA(z9,A[0],B1[4]); SBAR(); FMD(z2,xa_); FMD(z3,xb_); SBAR();
MM0(z10,A[5],B0[0]); SBAR(); ROR2(u_2,t1_1,z2[0],u_3,t1_2,z3[0]); SBAR();
MM0(z11,A[5],B1[0]); SBAR(); LDX(2); SBAR();
MMA(z10,A[4],B0[1]); SBAR(); SHR2(u_2,t1_2,z2[0],u_3,t1_3,z3[0]); SBAR();
MMA(z11,A[4],B1[1]); SBAR(); yw[1]=pk(u_2,u_3); PIN(yw); SBAR();
MMA(z10,A[3],B0[2]); SBAR(); ROR2(t2_4,z3[3],z4[2],t2_5,z4[3],z5[2]); SBAR();
MMA(z11,A[3],B1[2]); SBAR(); LDB(0,7); SBAR();
MMA(z10,A[2],B0[3]); SBAR(); SHR2(t2_4,z4[3],z4[2],t2_5,z5[3],z5[2]); SBAR();
MMA(z11,A[2],B1[3]); SBAR(); LDB(1,7); SBAR();
MMA(z10,A[1],B0[4]); SBAR(); ROR2(t1_4,t2_3,z4[1],t1_5,t2_4,z5[1]); SBAR();
MMA(z11,A[1],B1[4]); SBAR(); SHX(); SBAR();
MMA(z10,A[0],B0[5]); SBAR(); SHR2(t1_4,t2_4,z4[1],t1_5,t2_5,z5[1]); SBAR();
MMA(z11,A[0],B1[5]); SBAR(); FMD(z4,xa_); FMD(z5,xb_); SBAR();
MM0(z12,A[6],B0[0]); SBAR(); ROR2(u_4,t1_3,z4[0],u_5,t1_4,z5[0]); SBAR();
MM0(z13,A[6],B1[0]); SBAR(); LDX(3); SBAR();
MMA(z12,A[5],B0[1]); SBAR(); SHR2(u_4,t1_4,z4[0],u_5,t1_5,z5[0]); SBAR();
MMA(z13,A[5],B1[1]); SBAR(); yw[2]=pk(u_4,u_5); PIN(yw); SBAR();
MMA(z12,A[4],B0[2]); SBAR(); ROR2(t2_6,z5[3],z6[2],t2_7,z6[3],z7[2]); SBAR();
MMA(z13,A[4],B1[2]); SBAR(); LDB(0,8); SBAR();
MMA(z12,A[3],B0[3]); SBAR(); SHR2(t2_6,z6[3],z6[2],t2_7,z7[3],z7[2]); SBAR();
MMA(z13,A[3],B1[3]); SBAR(); LDB(1,8); SBAR();
MMA(z12,A[2],B0[4]); SBAR(); ROR2(t1_6,t2_5,z6[1],t1_7,t2_6,z7[1]); SBAR();
MMA(z13,A[2],B1[4]); SBAR(); SHX(); SBAR();
MMA(z12,A[1],B0[5]); SBAR(); SHR2(t1_6,t2_6,z6[1],t1_7,t2_7,z7[1]); SBAR();
MMA(z13,A[1],B1[5]); SBAR(); FMD(z6,xa_); FMD(z7,xb_); SBAR();
MMA(z12,A[0],B0[6]); SBAR(); ROR2(u_6,t1_5,z6[0],u_7,t1_6,z7[0]); SBAR();
MMA(z13,A[0],B1[6]); SBAR(); LDX(4); SBAR();
MM0(z14,A[7],B0[0]); SBAR(); SHR2(u_6,t1_6,z6[0],u_7,t1_7,z7[0]); SBAR();
MM0(z15,A[7],B1[0]); SBAR(); yw[3]=pk(u_6,u_7); PIN(yw); STY(0); SBAR();
MMA(z14,A[6],B0[1]); SBAR(); ROR2(t2_8,z7[3],z8[2],t2_9,z8[3],z9[2]); SBAR();
MMA(z15,A[6],B1[1]); SBAR(); LDB(0,9); SBAR();
MMA(z14,A[5],B0[2]); SBAR(); SHR2(t2_8,z8[3],z8[2],t2_9,z9[3],z9[2]); SBAR();
MMA(z15,A[5],B1[2]); SBAR(); LDB(1,9); SBAR();
MMA(z14,A[4],B0[3]); SBAR(); ROR2(t1_8,t2_7,z8[1],t1_9,t2_8,z9[1]); SBAR();
MMA(z15,A[4],B1[3]); SBAR(); SHX(); SBAR();
MMA(z14,A[3],B0[4]); SBAR(); SHR2(t1_8,t2_8,z8[1],t1_9,t2_9,z9[1]); SBAR();
MMA(z15,A[3],B1[4]); SBAR(); FMD(z8,xa_); FMD(z9,xb_); SBAR();
MMA(z14,A[2],B0[5]); SBAR(); ROR2(u_8,t1_7,z8[0],u_9,t1_8,z9[0]); SBAR();
MMA(z15,A[2],B1[5]); SBAR(); LDX(5); SBAR();
MMA(z14,A[1],B0[6]); SBAR(); SHR2(u_8,t1_8,z8[0],u_9,t1_9,z9[0]); SBAR();
MMA(z15,A[1],B1[6]); SBAR(); yw[0]=pk(u_8,u_9); PIN(yw); SBAR();
MMA(z14,A[0],B0[7]); SBAR(); ROR2(t2_10,z9[3],z10[2],t2_11,z10[3],z11[2]); SBAR();
MMA(z15,A[0],B1[7]); SBAR(); LDB(0,10); SBAR();
MM0(z16,A[8],B0[0]); SBAR(); SHR2(t2_10,z10[3],z10[2],t2_11,z11[3],z11[2]); SBAR();
MM0(z17,A[8],B1[0]); SBAR(); LDB(1,10); SBAR();
MMA(z16,A[7],B0[1]); SBAR(); ROR2(t1_10,t2_9,z10[1],t1_11,t2_10,z11[1]); SBAR();
MMA(z17,A[7],B1[1]); SBAR(); SHX(); SBAR();
MMA(z16,A[6],B0[2]); SBAR(); SHR2(t1_10,t2_10,z10[1],t1_11,t2_11,z11[1]); SBAR();
MMA(z17,A[6],B1[2]); SBAR(); FMD(z10,xa_); FMD(z11,xb_); SBAR();
MMA(z16,A[5],B0[3]); SBAR(); ROR2(u_10,t1_9,z10[0],u_11,t1_10,z11[0]); SBAR();
MMA(z17,A[5],B1[3]); SBAR(); LDX(6); SBAR();
MMA(z16,A[4],B0[4]); SBAR(); SHR2(u_10,t1_10,z10[0],u_11,t1_11,z11[0]); SBAR();
MMA(z17,A[4],B1[4]); SBAR(); yw[1]=pk(u_10,u_11); PIN(yw); SBAR();
MMA(z16,A[3],B0[5]); SBAR(); ROR2(t2_12,z11[3],z12[2],t2_13,z12[3],z13[2]); SBAR();
MMA(z17,A[3],B1[5]); SBAR(); LDB(0,11); SBAR();
MMA(z16,A[2],B0[6]); SBAR(); SHR2(t2_12,z12[3],z12[2],t2_13,z13[3],z13[2]); SBAR();
MMA(z17,A[2],B1[6]); SBAR(); LDB(1,11); SBAR();
MMA(z16,A[1],B0[7]); SBAR(); ROR2(t1_12,t2_11,z12[1],t1_13,t2_12,z13[1]); SBAR();
MMA(z17,A[1],B1[7]); SBAR(); SHX(); SBAR();
MMA(z16,A[0],B0[8]); SBAR(); SHR2(t1_12,t2_12,z12[1],t1_13,t2_13,z13[1]); SBAR();
MMA(z17,A[0],B1[8]); SBAR(); FMD(z12,xa_); FMD(z13,xb_); SBAR();
MM0(z18,A[9],B0[0]); SBAR(); ROR2(u_12,t1_11,z12[0],u_13,t1_12,z13[0]); SBAR();
MM0(z19,A[9],B1[0]); SBAR(); LDX(7); SBAR();
MMA(z18,A[8],B0[1]); SBAR(); SHR2(u_12,t1_12,z12[0],u_13,t1_13,z13[0]); SBAR();
MMA(z19,A[8],B1[1]); SBAR(); yw[2]=pk(u_12,u_13); PIN(yw); SBAR();
MMA(z18,A[7],B0[2]); SBAR(); ROR2(t2_14,z13[3],z14[2],t2_15,z14[3],z15[2]); SBAR();
MMA(z19,A[7],B1[2]); SBAR(); LDB(0,12); SBAR();
MMA(z18,A[6],B0[3]); SBAR(); SHR2(t2_14,z14[3],z14[2],t2_15,z15[3],z15[2]); SBAR();
MMA(z19,A[6],B1[3]); SBAR(); LDB(1,12); SBAR();
MMA(z18,A[5],B0[4]); SBAR(); ROR2(t1_14,t2_13,z14[1],t1_15,t2_14,z15[1]); SBAR();
MMA(z19,A[5],B1[4]); SBAR(); SHX(); SBAR();
MMA(z18,A[4],B0[5]); SBAR(); SHR2(t1_14,t2_14,z14[1],t1_15,t2_15,z15[1]); SBAR();
MMA(z19,A[4],B1[5]); SBAR(); FMD(z14,xa_); FMD(z15,xb_); SBAR();
MMA(z18,A[3],B0[6]); SBAR(); ROR2(u_14,t1_13,z14[0],u_15,t1_14,z15[0]); SBAR();
MMA(z19,A[3],B1[6]); SBAR(); LDX(8); SBAR();
MMA(z18,A[2],B0[7]); SBAR(); SHR2(u_14,t1_14,z14[0],u_15,t1_15,z15[0]); SBAR();
MMA(z19,A[2],B1[7]); SBAR(); yw[3]=pk(u_14,u_15); PIN(yw); STY(1); SBAR();
MMA(z18,A[1],B0[8]); SBAR(); ROR2(t2_16,z15[3],z16[2],t2_17,z16[3],z17[2]); SBAR();
MMA(z19,A[1],B1[8]); SBAR(); LDB(0,13); SBAR();
MMA(z18,A[0],B0[9]); SBAR(); SHR2(t2_16,z16[3],z16[2],t2_17,z17[3],z17[2]); SBAR();
MMA(z19,A[0],B1[9]); SBAR(); LDB(1,13); SBAR();
MM0(z20,A[10],B0[0]); SBAR(); ROR2(t1_16,t2_15,z16[1],t1_17,t2_16,z17[1]); SBAR();
MM0(z21,A[10],B1[0]); SBAR(); SHX(); SBAR();
MMA(z20,A[9],B0[1]); SBAR(); SHR2(t1_16,t2_16,z16[1],t1_17,t2_17,z17[1]); SBAR();
MMA(z21,A[9],B1[1]); SBAR(); FMD(z16,xa_); FMD(z17,xb_); SBAR();
MMA(z20,A[8],B0[2]); SBAR(); ROR2(u_16,t1_15,z16[0],u_17,t1_16,z17[0]); SBAR();
MMA(z21,A[8],B1[2]); SBAR(); LDX(9); SBAR();
MMA(z20,A[7],B0[3]); SBAR(); SHR2(u_16,t1_16,z16[0],u_17,t1_17,z17[0]); SBAR();
MMA(z21,A[7],B1[3]); SBAR(); yw[0]=pk(u_16,u_17); PIN(yw); SBAR();
MMA(z20,A[6],B0[4]); SBAR(); ROR2(t2_18,z17[3],z18[2],t2_19,z18[3],z19[2]); SBAR();
MMA(z21,A[6],B1[4]); SBAR(); LDB(0,14); SBAR();
MMA(z20,A[5],B0[5]); SBAR(); SHR2(t2_18,z18[3],z18[2],t2_19,z19[3],z19[2]); SBAR();
MMA(z21,A[5],B1[5]); SBAR(); LDB(1,14); SBAR();
MMA(z20,A[4],B0[6]); SBAR(); ROR2(t1_18,t2_17,z18[1],t1_19,t2_18,z19[1]); SBAR();
MMA(z21,A[4],B1[6]); SBAR(); SHX(); SBAR();
MMA(z20,A[3],B0[7]); SBAR(); SHR2(t1_18,t2_18,z18[1],t1_19,t2_19,z19[1]); SBAR();
MMA(z21,A[3],B1[7]); SBAR(); FMD(z18,xa_); FMD(z19,xb_); SBAR();
MMA(z20,A[2],B0[8]); SBAR(); ROR2(u_18,t1_17,z18[0],u_19,t1_18,z19[0]); SBAR();
MMA(z21,A[2],B1[8]); SBAR(); LDX(10); SBAR();
MMA(z20,A[1],B0[9]); SBAR(); SHR2(u_18,t1_18,z18[0],u_19,t1_19,z19[0]); SBAR();
MMA(z21,A[1],B1[9]); SBAR(); yw[1]=pk(u_18,u_19); PIN(yw); SBAR();
MMA(z20,A[0],B0[10]); SBAR();  SBAR();
MMA(z21,A[0],B1[10]); SBAR();  SBAR();
MM0(z22,A[11],B0[0]); SBAR();  SBAR();
MM0(z23,A[11],B1[0]); SBAR();  SBAR();
MMA(z22,A[10],B0[1]); SBAR();  SBAR();
MMA(z23,A[10],B1[1]); SBAR();  SBAR();
MMA(z22,A[9],B0[2]); SBAR();  SBAR();
MMA(z23,A[9],B1[2]); SBAR();  SBAR();
MMA(z22,A[8],B0[3]); SBAR();  SBAR();
MMA(z23,A[8],B1[3]); SBAR();  SBAR();
MMA(z22,A[7],B0[4]); SBAR(); ROR2(t2_20,z19[3],z20[2],t2_21,z20[3],z21[2]); SBAR();
MMA(z23,A[7],B1[4]); SBAR(); LDB(0,15); SBAR();
MMA(z22,A[6],B0[5]); SBAR(); SHR2(t2_20,z20[3],z20[2],t2_21,z21[3],z21[2]); SBAR();
MMA(z23,A[6],B1[5]); SBAR(); LDB(1,15); SBAR();
MMA(z22,A[5],B0[6]); SBAR(); ROR2(t1_20,t2_19,z20[1],t1_21,t2_20,z21[1]); SBAR();
MMA(z23,A[5],B1[6]); SBAR(); SHX(); SBAR();
MMA(z22,A[4],B0[7]); SBAR(); SHR2(t1_20,t2_20,z20[1],t1_21,t2_21,z21[1]); SBAR();
MMA(z23,A[4],B1[7]); SBAR(); FMD(z20,xa_); FMD(z21,xb_); SBAR();
MMA(z22,A[3],B0[8]); SBAR(); ROR2(u_20,t1_19,z20[0],u_21,t1_20,z21[0]); SBAR();
MMA(z23,A[3],B1[8]); SBAR(); LDX(11); SBAR();
MMA(z22,A[2],B0[9]); SBAR(); SHR2(u_20,t1_20,z20[0],u_21,t1_21,z21[0]); SBAR();
MMA(z23,A[2],B1[9]); SBAR(); yw[2]=pk(u_20,u_21); PIN(yw); SBAR();
MMA(z22,A[1],B0[10]); SBAR();  SBAR();
MMA(z23,A[1],B1[10]); SBAR();  SBAR();
MMA(z22,A[0],B0[11]); SBAR();  SBAR();
MMA(z23,A[0],B1[11]); SBAR();  SBAR();
MM0(z24,A[12],B0[0]); SBAR();  SBAR();
MM0(z25,A[12],B1[0]); SBAR();  SBAR();
MMA(z24,A[11],B0[1]); SBAR();  SBAR();
MMA(z25,A[11],B1[1]); SBAR();  SBAR();
MMA(z24,A[10],B0[2]); SBAR();  SBAR();
MMA(z25,A[10],B1[2]); SBAR();  SBAR();
MMA(z24,A[9],B0[3]); SBAR();  SBAR();
MMA(z25,A[9],B1[3]); SBAR();  SBAR();
MMA(z24,A[8],B0[4]); SBAR(); ROR2(t2_22,z21[3],z22[2],t2_23,z22[3],z23[2]); SBAR();
MMA(z25,A[8],B1[4]); SBAR(); VNOP(); SBAR();
MMA(z24,A[7],B0[5]); SBAR(); SHR2(t2_22,z22[3],z22[2],t2_23,z23[3],z23[2]); SBAR();
MMA(z25,A[7],B1[5]); SBAR(); VNOP(); SBAR();
MMA(z24,A[6],B0[6]); SBAR(); ROR2(t1_22,t2_21,z22[1],t1_23,t2_22,z23[1]); SBAR();
MMA(z25,A[6],B1[6]); SBAR(); SHX(); SBAR();
MMA(z24,A[5],B0[7]); SBAR(); SHR2(t1_22,t2_22,z22[1],t1_23,t2_23,z23[1]); SBAR();
MMA(z25,A[5],B1[7]); SBAR(); FMD(z22,xa_); FMD(z23,xb_); SBAR();
MMA(z24,A[4],B0[8]); SBAR(); ROR2(u_22,t1_21,z22[0],u_23,t1_22,z23[0]); SBAR();
MMA(z25,A[4],B1[8]); SBAR(); LDX(12); SBAR();
MMA(z24,A[3],B0[9]); SBAR(); SHR2(u_22,t1_22,z22[0],u_23,t1_23,z23[0]); SBAR();
MMA(z25,A[3],B1[9]); SBAR(); yw[3]=pk(u_22,u_23); PIN(yw); STY(2); SBAR();
MMA(z24,A[2],B0[10]); SBAR();  SBAR();
MMA(z25,A[2],B1[10]); SBAR();  SBAR();
MMA(z24,A[1],B0[11]); SBAR();  SBAR();
MMA(z25,A[1],B1[11]); SBAR();  SBAR();
MMA(z24,A[0],B0[12]); SBAR();  SBAR();
MMA(z25,A[0],B1[12]); SBAR();  SBAR();
MM0(z26,A[13],B0[0]); SBAR();  SBAR();
MM0(z27,A[13],B1[0]); SBAR();  SBAR();
MMA(z26,A[12],B0[1]); SBAR();  SBAR();
MMA(z27,A[12],B1[1]); SBAR();  SBAR();
MMA(z26,A[11],B0[2]); SBAR();  SBAR();
MMA(z27,A[11],B1[2]); SBAR();  SBAR();
MMA(z26,A[10],B0[3]); SBAR();  SBAR();
MMA(z27,A[10],B1[3]); SBAR();  SBAR();
MMA(z26,A[9],B0[4]); SBAR(); ROR2(t2_24,z23[3],z24[2],t2_25,z24[3],z25[2]); SBAR();
MMA(z27,A[9],B1[4]); SBAR(); VNOP(); SBAR();
MMA(z26,A[8],B0[5]); SBAR(); SHR2(t2_24,z24[3],z24[2],t2_25,z25[3],z25[2]); SBAR();
MMA(z27,A[8],B1[5]); SBAR(); VNOP(); SBAR();
MMA(z26,A[7],B0[6]); SBAR(); ROR2(t1_24,t2_23,z24[1],t1_25,t2_24,z25[1]); SBAR();
MMA(z27,A[7],B1[6]); SBAR(); SHX(); SBAR();
MMA(z26,A[6],B0[7]); SBAR(); SHR2(t1_24,t2_24,z24[1],t1_25,t2_25,z25[1]); SBAR();
MMA(z27,A[6],B1[7]); SBAR(); FMD(z24,xa_); FMD(z25,xb_); SBAR();
MMA(z26,A[5],B0[8]); SBAR(); ROR2(u_24,t1_23,z24[0],u_25,t1_24,z25[0]); SBAR();
MMA(z27,A[5],B1[8]); SBAR(); LDX(13); SBAR();
MMA(z26,A[4],B0[9]); SBAR(); SHR2(u_24,t1_24,z24[0],u_25,t1_25,z25[0]); SBAR();
MMA(z27,A[4],B1[9]); SBAR(); yw[0]=pk(u_24,u_25); PIN(yw); SBAR();
MMA(z26,A[3],B0[10]); SBAR();  SBAR();
MMA(z27,A[3],B1[10]); SBAR();  SBAR();
MMA(z26,A[2],B0[11]); SBAR();  SBAR();
MMA(z27,A[2],B1[11]); SBAR();  SBAR();
HOOK1(); SBAR();
MMA(z26,A[1],B0[12]); SBAR();  SBAR();
MMA(z27,A[1],B1[12]); SBAR();  SBAR();
MMA(z26,A[0],B0[13]); SBAR();  SBAR();
MMA(z27,A[0],B1[13]); SBAR();  SBAR();
MM0(z28,A[14],B0[0]); SBAR();  SBAR();
MM0(z29,A[14],B1[0]); SBAR();  SBAR();
MMA(z28,A[13],B0[1]); SBAR();  SBAR();
MMA(z29,A[13],B1[1]); SBAR();  SBAR();
MMA(z28,A[12],B0[2]); SBAR();  SBAR();
MMA(z29,A[12],B1[2]); SBAR();  SBAR();
MMA(z28,A[11],B0[3]); SBAR();  SBAR();
MMA(z29,A[11],B1[3]); SBAR();  SBAR();
MMA(z28,A[10],B0[4]); SBAR(); ROR2(t2_26,z25[3],z26[2],t2_27,z26[3],z27[2]); SBAR();
MMA(z29,A[10],B1[4]); SBAR(); VNOP(); SBAR();
MMA(z28,A[9],B0[5]); SBAR(); SHR2(t2_26,z26[3],z26[2],t2_27,z27[3],z27[2]); SBAR();
MMA(z29,A[9],B1[5]); SBAR(); VNOP(); SBAR();
MMA(z28,A[8],B0[6]); SBAR(); ROR2(t1_26,t2_25,z26[1],t1_27,t2_26,z27[1]); SBAR();
MMA(z29,A[8],B1[6]); SBAR(); SHX(); SBAR();
MMA(z28,A[7],B0[7]); SBAR(); SHR2(t1_26,t2_26,z26[1],t1_27,t2_27,z27[1]); SBAR();
MMA(z29,A[7],B1[7]); SBAR(); FMD(z26,xa_); FMD(z27,xb_); SBAR();
MMA(z28,A[6],B0[8]); SBAR(); ROR2(u_26,t1_25,z26[0],u_27,t1_26,z27[0]); SBAR();
MMA(z29,A[6],B1[8]); SBAR(); LDX(14); SBAR();
MMA(z28,A[5],B0[9]); SBAR(); SHR2(u_26,t1_26,z26[0],u_27,t1_27,z27[0]); SBAR();
MMA(z29,A[5],B1[9]); SBAR(); yw[1]=pk(u_26,u_27); PIN(yw); SBAR();
MMA(z28,A[4],B0[10]); SBAR();  SBAR();
MMA(z29,A[4],B1[10]); SBAR();  SBAR();
MMA(z28,A[3],B0[11]); SBAR();  SBAR();
MMA(z29,A[3],B1[11]); SBAR();  SBAR();
MMA(z28,A[2],B0[12]); SBAR();  SBAR();
MMA(z29,A[2],B1[12]); SBAR();  SBAR();
MMA(z28,A[1],B0[13]); SBAR();  SBAR();
MMA(z29,A[1],B1[13]); SBAR();  SBAR();
MMA(z28,A[0],B0[14]); SBAR();  SBAR();
MMA(z29,A[0],B1[14]); SBAR();  SBAR();
MM0(z30,A[15],B0[0]); SBAR();  SBAR();
MM0(z31,A[15],B1[0]); SBAR();  SBAR();
MMA(z30,A[14],B0[1]); SBAR();  SBAR();
MMA(z31,A[14],B1[1]); SBAR();  SBAR();
MMA(z30,A[13],B0[2]); SBAR();  SBAR();
MMA(z31,A[13],B1[2]); SBAR();  SBAR();
MMA(z30,A[12],B0[3]); SBAR();  SBAR();
MMA(z31,A[12],B1[3]); SBAR();  SBAR();
MMA(z30,A[11],B0[4]); SBAR(); ROR2(t2_28,z27[3],z28[2],t2_29,z28[3],z29[2]); SBAR();
MMA(z31,A[11],B1[4]); SBAR(); VNOP(); SBAR();
MMA(z30,A[10],B0[5]); SBAR(); SHR2(t2_28,z28[3],z28[2],t2_29,z29[3],z29[2]); SBAR();
HOOK2(); SBAR();
MMA(z31,A[10],B1[5]); SBAR(); VNOP(); SBAR();
MMA(z30,A[9],B0[6]); SBAR(); ROR2(t1_28,t2_27,z28[1],t1_29,t2_28,z29[1]); SBAR();
MMA(z31,A[9],B1[6]); SBAR(); SHX(); SBAR();
MMA(z30,A[8],B0[7]); SBAR(); SHR2(t1_28,t2_28,z28[1],t1_29,t2_29,z29[1]); SBAR();
MMA(z31,A[8],B1[7]); SBAR(); FMD(z28,xa_); FMD(z29,xb_); SBAR();
MMA(z30,A[7],B0[8]); SBAR(); ROR2(u_28,t1_27,z28[0],u_29,t1_28,z29[0]); SBAR();
MMA(z31,A[7],B1[8]); SBAR(); LDX(15); SBAR();
MMA(z30,A[6],B0[9]); SBAR(); SHR2(u_28,t1_28,z28[0],u_29,t1_29,z29[0]); SBAR();
MMA(z31,A[6],B1[9]); SBAR(); yw[2]=pk(u_28,u_29); PIN(yw); SBAR();
MMA(z30,A[5],B0[10]); SBAR();  SBAR();
MMA(z31,A[5],B1[10]); SBAR();  SBAR();
MMA(z30,A[4],B0[11]); SBAR();  SBAR();
MMA(z31,A[4],B1[11]); SBAR();  SBAR();
MMA(z30,A[3],B0[12]); SBAR();  SBAR();
MMA(z31,A[3],B1[12]); SBAR();  SBAR();
MMA(z30,A[2],B0[13]); SBAR();  SBAR();
MMA(z31,A[2],B1[13]); SBAR();  SBAR();
MMA(z30,A[1],B0[14]); SBAR();  SBAR();
MMA(z31,A[1],B1[14]); SBAR();  SBAR();
MMA(z30,A[0],B0[15]); SBAR();  SBAR();
MMA(z31,A[0],B1[15]); SBAR();  SBAR();
NOP1(); SBAR();  SBAR();
NOP1(); SBAR();  SBAR();
NOP1(); SBAR();  SBAR();
NOP1(); SBAR();  SBAR();
NOP1(); SBAR();  SBAR();
NOP1(); SBAR();  SBAR();
NOP1(); SBAR();  SBAR();
NOP1(); SBAR();  SBAR();
NOP1(); SBAR(); ROR2(t2_30,z29[3],z30[2],t2_31,z30[3],z31[2]); SBAR();
NOP1(); SBAR(); VNOP(); SBAR();
NOP1(); SBAR(); SHR2(t2_30,z30[3],z30[2],t2_31,z31[3],z31[2]); SBAR();
NOP1(); SBAR(); VNOP(); SBAR();
NOP1(); SBAR(); ROR2(t1_30,t2_29,z30[1],t1_31,t2_30,z31[1]); SBAR();
NOP1(); SBAR(); SHX(); SBAR();
NOP1(); SBAR(); SHR2(t1_30,t2_30,z30[1],t1_31,t2_31,z31[1]); SBAR();
NOP1(); SBAR(); FMD(z30,xa_); FMD(z31,xb_); SBAR();
NOP1(); SBAR(); ROR2(u_30,t1_29,z30[0],u_31,t1_30,z31[0]); SBAR();
NOP1(); SBAR(); VNOP(); SBAR();
NOP1(); SBAR(); SHR2(u_30,t1_30,z30[0],u_31,t1_31,z31[0]); SBAR();
NOP1(); SBAR(); yw[3]=pk(u_30,u_31); PIN(yw); STY(3); SBAR();
    }
    if(!h1){
      wait_flags<4>(kvflag(ws,R,C,v>>1,1,lane),nonce);
      lds_wait(cnt+C_KV1,2);
      kv_fetch<false>(ws,lds0,R,C,v>>1,1,v&1,lane);
    }
    WAITV(0);
    arrive_publish(cnt+C_H1,4,flagp(ws,b,FK_Y),nonce,lane);
    lds_wait(cnt+C_H1,4); lds_wait(cnt+C_P1E,4);
#pragma unroll 1
    for(int i=v;i<WO_P1;i+=4) wo_block(i);
    if(v<2) touch((unsigned)((512*(j>>3)+8*(j&7)+64*(lane>>3)+(lane&7))*2048+(xcd*2+v)*128),Aq,junk);
  } else {
    const int wr=wid>>1, wc=wid&1;
    const int r=lane&31, h=lane>>5;
    lptr L=(lptr)lds;
    lptr xa[4];
    { int row=wr*64+r; int sw=(row>>1)&7;
#pragma unroll
      for(int ks=0;ks<4;ks++) xa[ks]=L+row*128+(((2*ks+h)^sw)*16); }
    lptr wa;
    { int blk=(lane>>4)&1,q=(lane&15)>>2,p=lane&3; int c=(wc*4+2*blk+(p>>1))^(((q>>1)&1)<<2); wa=L+16384+(8*h+q)*128+c*16+8*(p&1); }
    f32x16 aq[2],ak[2],av[2];
#pragma unroll
    for(int i=0;i<2;i++){ aq[i]=f32x16{}; ak[i]=f32x16{}; av[i]=f32x16{}; }
    G1F F0,F1;
#define G1_RX(F,ks) do{ F.x0=ldsr128(xa[ks]+so); F.x1=ldsr128(xa[ks]+so+4096); }while(0)
#define G1_RW(F,ks,sg) do{ F.w[2*(sg)]=ldstr(wa+so+(sg)*8192+(16*(ks))*128); F.w[2*(sg)+1]=ldstr(wa+so+(sg)*8192+(16*(ks)+4)*128); }while(0)
#define G1_W(F,sg) cat(F.w[2*(sg)],F.w[2*(sg)+1])
#define G1_STEP(F,N,ks,LD) do{ \
    ak[0]=MFMA32(G1_W(F,1),F.x0,ak[0]); PIN(ak[0]); if(LD) G1_RX(N,ks); SBAR(); \
    ak[1]=MFMA32(G1_W(F,1),F.x1,ak[1]); PIN(ak[1]); if(LD) G1_RW(N,ks,1); SBAR(); \
    av[0]=MFMA32(F.x0,G1_W(F,2),av[0]); PIN(av[0]); if(LD) G1_RW(N,ks,2); SBAR(); \
    av[1]=MFMA32(F.x1,G1_W(F,2),av[1]); PIN(av[1]); if(LD) G1_RW(N,ks,0); SBAR(); \
    aq[0]=MFMA32(G1_W(F,0),F.x0,aq[0]); PIN(aq[0]); SBAR(); \
    aq[1]=MFMA32(G1_W(F,0),F.x1,aq[1]); PIN(aq[1]); SBAR(); }while(0)
    __builtin_amdgcn_s_setprio(2);
    asm volatile("":"+v"(aq[0]),"+v"(aq[1]),"+v"(ak[0]),"+v"(ak[1]),"+v"(av[0]),"+v"(av[1]));
    LOAD_NONCE();
    BAR();
    int so=0;
    G1_RX(F0,0); G1_RW(F0,0,1); G1_RW(F0,0,2); G1_RW(F0,0,0); SBAR();
#pragma unroll 1
    for(int t=0;t<15;t++){
      G1_STEP(F0,F1,1,true);
      G1_STEP(F1,F0,2,true);
      G1_STEP(F0,F1,3,true);
      so=(so==2*G1_SLOT)?0:so+G1_SLOT;
      WAITL(); BAR(); SBAR();
      G1_STEP(F1,F0,0,true);
    }
    G1_STEP(F0,F1,1,true);
    G1_STEP(F1,F0,2,true);
    G1_STEP(F0,F1,3,true);
    arrive(cnt+C_RD);
    SBAR();
    G1_STEP(F1,F0,0,false);
    const float qs=0.17677669529663687f*1.4426950408889634f;
    u32x4 kf[2][2],vf[2][2];
    const unsigned go=WS_KV+(unsigned)b*32768u+wc*16384u+(2*wr)*2048u+lane*16u;
#define KV_STORE() _Pragma("unroll") for(int rt=0;rt<2;rt++) _Pragma("unroll") for(int s=0;s<2;s++) __builtin_amdgcn_raw_buffer_store_b128(kf[rt][s],wsr,go+rt*2048+s*1024,0,SC1); \
      _Pragma("unroll") for(int rt=0;rt<2;rt++) _Pragma("unroll") for(int s=0;s<2;s++) __builtin_amdgcn_raw_buffer_store_b128(vf[rt][s],wsr,go+8192+rt*2048+s*1024,0,SC1)
#pragma unroll
    for(int rt=0;rt<2;rt++)
#pragma unroll
      for(int s=0;s<2;s++){ kf[rt][s]=pack8(ak[rt],s); vf[rt][s]=pack8(av[rt],s); }
    if(wc==0){ KV_STORE(); }
    lds_wait(cnt+C_RD,4);
#pragma unroll
    for(int rt=0;rt<2;rt++){
      lwptr lq=(lwptr)lds+(1-wc)*HR+49152+(2*wr+rt)*2048+lane*16;
#pragma unroll
      for(int s=0;s<2;s++) *(LAS u32x4*)(lq+s*1024)=pack8(aq[rt],s,qs);
    }
    if(R<2){
#pragma unroll 1
      for(int i=0;i<8*(2-R);i++) *(LAS u32x4*)((lwptr)lds+(1-wc)*HR+wr*24576+i*1024+lane*16)=u32x4{0,0,0,0};
    }
    if(wc==1){ lds_wait(cnt+C_KV0,2); KV_STORE(); }
    WAITV(0);
    arrive_publish(cnt+C_KV0+wc,2,flagp(ws,b,FK_KV0+wc),nonce,lane);
    if(wid==0){
      lds_wait(cnt+C_KV0,2);
      const int kind=(lane>>2)&1, ri=lane&3;
      const gu64* fp=kvflag(ws,R,C,kind,0,ri);
      unsigned todo=(R>=2)?0x77u:((R==1)?0x66u:0x44u);
#pragma unroll 1
      for(unsigned sp=0;sp<SPIN_CAP && todo;sp++){
        u64 fv=0;
        if(lane<8 && ((todo>>lane)&1)) fv=__hip_atomic_load(fp,RLX_AGENT);
        unsigned rdy=(unsigned)__builtin_amdgcn_ballot_w64(fv==nonce)&todo;
        MEMFENCE();
        todo&=~rdy;
        if(!rdy) __builtin_amdgcn_s_sleep(2);
#pragma unroll 1
        while(rdy){
          const int bit=__builtin_ctz(rdy); rdy&=rdy-1;
          const int kd=bit>>2, i=bit&3;
          const char* src=ws+WS_KV+(size_t)blk_of(R-2+i,(C&12)|((C-1-kd)&3))*32768+kd*8192;
          const unsigned dst=lds0+HR+(1-kd)*24576+i*8192;
          dma4_sc1(lane*16,src,dst); dma4_sc1(lane*16,src+4096,dst+4096);
        }
      }
      WAITV(0);
      arrive(cnt+C_H0K);
    } else lds_wait(cnt+C_H0K,1);
    __builtin_amdgcn_s_setprio(0);
  }
  const int late=wid>>2;
  if(R>=2) attention<false>(lds,cnt,ws,wsr,b,R,C,wid,lane,nonce);
  else     attention<true >(lds,cnt,ws,wsr,b,R,C,wid,lane,nonce);
  if(!late){
    WAITV(0);
    arrive_publish(cnt+C_O0,4,flagp(ws,b,FK_O0),nonce,lane);
    if(wid==3){
      o_touch(ws,lds0+L_JUNK,R2,j,0,lane,nonce);
      wait_flags(flagp(ws,blk_of(R2,lane&15),FK_O0),nonce);
      arrive(cnt+C_EF);
    } else { lds_wait(cnt+C_E,4); lds_wait(cnt+C_P1L,4);
#pragma unroll 1
      for(int i=WO_P1+wid;i<32;i+=3) wo_block(i);
      lds_wait(cnt+C_EF,1); }
    const int T2=R2*4+wid;
    const unsigned ob=WS_O+(unsigned)T2*65536u+lane*16u;
    u32x4 of[8][2];
#define G2_LDO(i,n) do{ const unsigned so_=(unsigned)((((n)+KO)&15)*4096); of[i][0]=__builtin_amdgcn_raw_buffer_load_b128(wsr,obv,so_,OAUX); of[i][1]=__builtin_amdgcn_raw_buffer_load_b128(wsr,obv+1024,so_,OAUX); }while(0)
    unsigned obv=ob;
#pragma unroll
    for(int i=0;i<8;i++){ G2_LDO(i,i); if(i==RAMP){ WAITV(0); } }
    const int h=lane>>5,blk=(lane>>4)&1,q=(lane&15)>>2,p=lane&3;
    lptr wa[2];
#pragma unroll
    for(int ct=0;ct<2;ct++){ int c=(ct*4+2*blk+(p>>1))^(((q>>1)&1)<<2); wa[ct]=(lptr)lds+L_WO+23*4096+(4*h+q)*128+c*16+8*(p&1); }
    f32x16 acc[2]; acc[0]=f32x16{}; acc[1]=f32x16{};
    s16x4 wf[2][8];
#define G2_LDW(i) do{ _Pragma("unroll") for(int s_=0;s_<2;s_++) _Pragma("unroll") for(int ct_=0;ct_<2;ct_++){ lptr a_=wa[ct_]+(8-(i))*4096+s_*2048; \
      wf[(i)&1][4*s_+2*ct_]=ldstr(a_); wf[(i)&1][4*s_+2*ct_+1]=ldstr(a_+1024); } }while(0)
    lds_wait(cnt+C_W1,4);
    G2_LDW(0);
    u64 f1=0;
#pragma unroll 1
    for(int it=0;it<4;it++){
      if(it==1){
        arrive(cnt+C_W2);
        if(wid==3){ if(!allof(f1==nonce)) wait_flags(flagp(ws,blk_of(R2,lane&15),FK_O1),nonce); MEMFENCE(); arrive(cnt+C_LF); }
        else lds_wait(cnt+C_LF,1);
        __builtin_amdgcn_s_setprio(2);
      }
      if(it==2) lds_wait(cnt+C_W2,4);
      PIN(wa[0]); PIN(wa[1]);
      const int nb=8*it+8;
      obv=ob+((it==0)?0u:((it==3)?0x40000000u:2048u)); PIN(obv);
#pragma unroll
      for(int i=0;i<8;i++){
        SBAR();
        if(i==4){ if(wid==3 && it==0) f1=__hip_atomic_load(flagp(ws,blk_of(R2,lane&15),FK_O1),RLX_AGENT); }
        G2_LDW(i+1);
#pragma unroll
        for(int s=0;s<2;s++)
#pragma unroll
          for(int ct=0;ct<2;ct++) acc[ct]=MFMA32(cat(wf[i&1][4*s+2*ct],wf[i&1][4*s+2*ct+1]),B8(of[i][s]),acc[ct]);
#pragma unroll
        for(int g_=0;g_<4;g_++){ __builtin_amdgcn_sched_group_barrier(0x100,2,0); __builtin_amdgcn_sched_group_barrier(0x8,1,0); }
        PIN(acc[0]); PIN(acc[1]);
        SBAR();
        G2_LDO(i,nb+i);
      }
      wa[0]-=32768; wa[1]-=32768;
    }
    lwptr stg=(lwptr)lds+L_WO+(31-wid)*4096;
#pragma unroll
    for(int ct=0;ct<2;ct++){
#pragma unroll
      for(int gp=0;gp<2;gp++){
        unsigned a0=pk(acc[ct][8*gp],acc[ct][8*gp+1]), a1=pk(acc[ct][8*gp+2],acc[ct][8*gp+3]);
        unsigned b0=pk(acc[ct][8*gp+4],acc[ct][8*gp+5]), b1=pk(acc[ct][8*gp+6],acc[ct][8*gp+7]);
        auto r0=__builtin_amdgcn_permlane32_swap(a0,b0,false,false); auto r1=__builtin_amdgcn_permlane32_swap(a1,b1,false,false);
        u32x4 vv={r0[0],r1[0],r0[1],r1[1]};
        *(LAS u32x4*)(stg+(lane&31)*128+(((ct*4+gp*2+h)^(lane&7))*16))=vv;
      }
    }
#pragma unroll
    for(int i=0;i<4;i++){
      const int row=8*i+(lane>>3);
      const u32x4 vv=*(const LAS u32x4*)(stg+row*128+(((lane&7)^(row&7))*16));
      __builtin_amdgcn_raw_buffer_store_b128(vv,outr,(unsigned)(T2*32+row)*4096u+2048u+C2*128u+(lane&7)*16u,0,SC1);
    }
  } else {
    const int v=wid-4;
    const int chb=xcd*128+(v&1)*64, rb=512*(j>>3)+8*(j&7)+4*(v>>1)+64*(lane>>3);
    u32x4 qq[4];
    const bool yr=__builtin_amdgcn_readfirstlane(*(volatile LAS unsigned*)(cnt+C_YF))!=0;
    MEMFENCE();
#pragma unroll
    for(int i=0;i<4;i++) qq[i]=*(const u32x4*)(Aq+(size_t)(rb+i)*1024+chb+(lane&7)*8);
    WAITV(4);
    arrive_publish(cnt+C_O1,4,flagp(ws,b,FK_O1),nonce,lane);
    __builtin_amdgcn_s_setprio(0);
    lds_wait(cnt+C_L,4);
    const unsigned yo=(unsigned)((lane>>2)*4096+(lane&3)*16);
    if(!yr) wait_flags<8>(flagp(ws,(lane&31)*8+xcd,FK_Y),nonce);
    dma4v_sc1(yo,yo+(65536-1024),yo+2*(65536-1024),yo+3*(65536-1024),ws+WS_Y+(size_t)chb*4096+j*128+(v>>1)*64,lds0+v*4096);
    if(v==0) o_touch(ws,lds0+L_JUNK,R2,j,1,lane,nonce);
    WAITV(0);
    lwptr stg=(lwptr)lds+v*4096;
    { const int g=lane>>4,q=(lane&15)>>2,p=lane&3;
      bf16x8 tt[4];
#pragma unroll
      for(int n=0;n<4;n++){ lptr a=(lptr)stg+(8*(2*n+(g>>1))+q)*64+(g&1)*32+8*p; tt[n]=cat(ldstr(a),ldstr(a+256)); }
      MEMFENCE();
      const int srow=16*(g&1)+(lane&15);
#pragma unroll
      for(int n=0;n<4;n++) *(LAS bf16x8*)(stg+srow*128+(((2*n+(g>>1))^(srow&7))*16))=tt[n];
    }
#pragma unroll
    for(int i=0;i<4;i++){
      const int row=8*i+(lane>>3);
      const u32x4 yy=*(const LAS u32x4*)(stg+row*128+(((lane&7)^(row&7))*16));
      u32x4 r;
#pragma unroll
      for(int d=0;d<4;d++) r[d]=pk(lo16(qq[i][d])*lo16(yy[d]),hi16(qq[i][d])*hi16(yy[d]));
      __builtin_amdgcn_raw_buffer_store_b128(r,outr,(unsigned)(rb+i)*4096u+chb*2u+(lane&7)*16u,0,SC1);
    }
    if(b==0 && wid==7){
#pragma unroll 1
      for(int i=0;i<4;i++) wait_flags<16>(flagp(ws,i*64+lane,FK_O1),nonce);
      if(lane==0) __hip_atomic_store((gu64*)(ws+F_E),nonce,RLX_AGENT);
    }
  }
}

extern "C" void kernel_launch(void* const* d_in, const int* in_sizes, int n_in, void* d_out, int out_size, void* d_ws, size_t ws_size, hipStream_t stream){
  (void)hipFuncSetAttribute((const void*)fused,hipFuncAttributeMaxDynamicSharedMemorySize,LDS_BYTES);
  fused<<<256,512,LDS_BYTES,stream>>>((const u16*)d_in[0],(const u16*)d_in[1],(const u16*)d_in[2],(const u16*)d_in[3],(const u16*)d_in[4],(const u16*)d_in[5],
                                      (const u16*)d_in[6],(const u16*)d_in[7],(const u16*)d_in[8],(u16*)d_out,(char*)d_ws);
}
```

```cpp
#include <hip/hip_runtime.h>
#include <hip/hip_bf16.h>
#include <stdint.h>
#if defined(__HIP_DEVICE_COMPILE__)
#define NOPK __attribute__((target("no-packed-fp32-ops")))
#else
#define NOPK
#endif
typedef unsigned short u16;
typedef unsigned long long u64;
typedef short bf16x8 __attribute__((ext_vector_type(8)));
typedef short s16x4 __attribute__((ext_vector_type(4)));
typedef float f32x16 __attribute__((ext_vector_type(16)));
typedef float f32x4 __attribute__((ext_vector_type(4)));
typedef float f32x2 __attribute__((ext_vector_type(2)));
typedef __bf16 bf2 __attribute__((ext_vector_type(2)));
typedef unsigned u32x4 __attribute__((ext_vector_type(4)));
typedef unsigned u32x2 __attribute__((ext_vector_type(2)));
#define LAS __attribute__((address_space(3)))
#define GAS __attribute__((address_space(1)))
typedef LAS const char* lptr;
typedef LAS char* lwptr;
typedef GAS u64 gu64;
#define DI __device__ __forceinline__ NOPK
#define MFMA32(a,b,c) __builtin_amdgcn_mfma_f32_32x32x16_bf16((a),(b),(c),0,0,0)
#define MFMA16(a,b,c) __builtin_amdgcn_mfma_f32_16x16x32_bf16((a),(b),(c),0,0,0)
#define SBAR() __builtin_amdgcn_sched_barrier(0)
#define PIN(x) asm volatile("":"+v"(x))
#define MEMFENCE() asm volatile("":::"memory")
#define BAR() asm volatile("s_barrier":::"memory")
#define WAITV(n) asm volatile("s_waitcnt vmcnt(" #n ")":::"memory")
#define WAITL() asm volatile("s_waitcnt lgkmcnt(0)":::"memory")
#define RLX_AGENT __ATOMIC_RELAXED, __HIP_MEMORY_SCOPE_AGENT
#define RLX_WG __ATOMIC_RELAXED, __HIP_MEMORY_SCOPE_WORKGROUP
#define SC1 16
#define XT_DIRECT
#define RAMP 99
#define NT 0
#define OAUX 16
#define B8(x) __builtin_bit_cast(bf16x8,(x))

#define PREPSZ (4*MB)
constexpr unsigned MB=1u<<20;
constexpr unsigned WS_KV=0, WS_O=8*MB, WS_XT=12*MB, WS_Y=16*MB, WS_F=20*MB, WS_END=21*MB, WS_ST=24*MB;
constexpr unsigned F_E=WS_F, F_W=WS_F+4096;
enum { FK_KV0=0, FK_KV1, FK_O0, FK_O1, FK_X, FK_Y };
constexpr int G1_SLOT=40960;
constexpr int HR=57344;
constexpr int L_CV=126976, CV_W=8480, CV_H=128, CV_X=128+4096+160;
constexpr int L_MISC=L_CV+4*CV_W;
constexpr int L_WO=L_MISC-131072;
constexpr int WO_P1=19;
static_assert(L_WO+(32-WO_P1)*4096>=HR+24576 && L_WO>=24576,"wo image");
constexpr int L_CNT=L_MISC;
constexpr int L_JUNK=L_CNT+128;
constexpr int L_MASK=L_JUNK+256;
constexpr int LDS_BYTES=L_MASK+2048;
static_assert(LDS_BYTES<=163840,"lds");
enum { C_RD=0, C_KV0=6, C_KV1, C_H0, C_H1, C_CV, C_E, C_L, C_W1, C_W2, C_W3, C_O0, C_O1, C_EF, C_LF, C_Y, C_X, C_XF, C_XR, C_YF, C_G2, C_HI, C_H0K=1, C_H0V=2, C_P1E=3, C_P1L=4 };
#define PREP_BAR(c) ((c)==1||(c)==3||(c)==4||(c)==6||(c)==8)
#define KV_PER 11
#define PFLAG ""
#define STAG 0
#define PREP0 4
#define CONV_START 30
#define CODE_LINES 256
constexpr unsigned SPIN_CAP=1u<<16, LSPIN_CAP=1u<<20;

DI unsigned pk(float a,float b){ f32x2 v={a,b}; bf2 r=__builtin_convertvector(v,bf2); return __builtin_bit_cast(unsigned,r); }
DI float asf(unsigned u){ return __builtin_bit_cast(float,u); }
DI unsigned asu(float f){ return __builtin_bit_cast(unsigned,f); }
DI bool allof(bool p){ return __builtin_amdgcn_ballot_w64(p)==~0ull; }
DI float lo16(unsigned u){ return asf(u<<16); }
DI float hi16(unsigned u){ return asf(u&0xffff0000u); }
DI bf16x8 ldsr128(lptr p){ return *(const LAS bf16x8*)p; }
DI s16x4 ldstr(lptr p){ return __builtin_amdgcn_ds_read_tr16_b64_v4i16((LAS s16x4*)p); }
DI bf16x8 cat(s16x4 a,s16x4 b){ return __builtin_shufflevector(a,b,0,1,2,3,4,5,6,7); }
DI u32x4 pack8(const f32x16& x,int s,float sc){ u32x4 r; r[0]=pk(x[8*s]*sc,x[8*s+1]*sc); r[1]=pk(x[8*s+2]*sc,x[8*s+3]*sc); r[2]=pk(x[8*s+4]*sc,x[8*s+5]*sc); r[3]=pk(x[8*s+6]*sc,x[8*s+7]*sc); return r; }
DI u32x4 pack8(const f32x16& x,int s){ u32x4 r; r[0]=pk(x[8*s],x[8*s+1]); r[1]=pk(x[8*s+2],x[8*s+3]); r[2]=pk(x[8*s+4],x[8*s+5]); r[3]=pk(x[8*s+6],x[8*s+7]); return r; }
#define DMA4_BODY(FL) "s_mov_b32 m0, %2\n\ts_nop 0\n\t" \
   "global_load_lds_dwordx4 %0, %1" FL "\n\tglobal_load_lds_dwordx4 %0, %1 offset:1024" FL "\n\tglobal_load_lds_dwordx4 %0, %1 offset:2048" FL "\n\tglobal_load_lds_dwordx4 %0, %1 offset:3072" FL
DI void dma4(unsigned voff,const void* sbase,unsigned ldsaddr){ asm volatile(DMA4_BODY(" nt") :: "v"(voff), "s"(sbase), "s"(ldsaddr) : "memory"); }
DI void dma4_sc1(unsigned voff,const void* sbase,unsigned ldsaddr){ asm volatile(DMA4_BODY(" sc1") :: "v"(voff), "s"(sbase), "s"(ldsaddr) : "memory"); }
DI void dma4v(unsigned v0,unsigned v1,unsigned v2,unsigned v3,const void* sbase,unsigned ldsaddr){
  asm volatile("s_mov_b32 m0, %5\n\ts_nop 0\n\t"
    "global_load_lds_dwordx4 %0, %4\n\tglobal_load_lds_dwordx4 %1, %4 offset:1024\n\tglobal_load_lds_dwordx4 %2, %4 offset:2048\n\tglobal_load_lds_dwordx4 %3, %4 offset:3072"
    :: "v"(v0),"v"(v1),"v"(v2),"v"(v3), "s"(sbase), "s"(ldsaddr) : "memory");
}
DI void dma4v_sc1(unsigned v0,unsigned v1,unsigned v2,unsigned v3,const void* sbase,unsigned ldsaddr){
  asm volatile("s_mov_b32 m0, %5\n\ts_nop 0\n\t"
    "global_load_lds_dwordx4 %0, %4 sc1\n\tglobal_load_lds_dwordx4 %1, %4 offset:1024 sc1\n\tglobal_load_lds_dwordx4 %2, %4 offset:2048 sc1\n\tglobal_load_lds_dwordx4 %3, %4 offset:3072 sc1"
    :: "v"(v0),"v"(v1),"v"(v2),"v"(v3), "s"(sbase), "s"(ldsaddr) : "memory");
}
DI void touch(unsigned voff,const void* sbase,unsigned junk){
  asm volatile("s_mov_b32 m0, %2\n\ts_nop 0\n\tglobal_load_lds_dword %0, %1" :: "v"(voff), "s"(sbase), "s"(junk) : "memory");
}
DI void touch64(const void* a,unsigned junk){
  asm volatile("s_mov_b32 m0, %1\n\ts_nop 0\n\tglobal_load_lds_dword %0, off" :: "v"(a), "s"(junk) : "memory");
}
DI bool flags_ready(const gu64* p,u64 nonce){ u64 v=__hip_atomic_load(p,RLX_AGENT); bool r=allof(v==nonce); MEMFENCE(); return r; }
template<int SL=2>
DI void wait_flags(const gu64* p,u64 nonce){
#pragma unroll 1
  for(unsigned sp=0;sp<SPIN_CAP;sp++){
    u64 v=__hip_atomic_load(p,RLX_AGENT);
    if(allof(v==nonce)) break;
    __builtin_amdgcn_s_sleep(SL);
  }
  MEMFENCE();
}
DI void lds_wait(LAS unsigned* p,unsigned n){
#pragma unroll 1
  for(unsigned sp=0;sp<LSPIN_CAP;sp++){
    unsigned v=__hip_atomic_load(p,RLX_WG);
    if(__builtin_amdgcn_readfirstlane(v)>=n) break;
    __builtin_amdgcn_s_sleep(1);
  }
  MEMFENCE();
}
DI void arrive(LAS unsigned* cnt){
  unsigned one=1u;
  asm volatile("s_mov_b64 exec, 1\n\tds_add_u32 %0, %1\n\ts_mov_b64 exec, -1" :: "v"(cnt), "v"(one) : "memory");
}
DI void arrive_publish(LAS unsigned* cnt,unsigned n,gu64* flag,u64 nonce,int lane){
  unsigned one=1u, old;
  asm volatile("s_mov_b64 exec, 1\n\tds_add_rtn_u32 %0, %1, %2\n\ts_mov_b64 exec, -1\n\ts_waitcnt lgkmcnt(0)" : "=&v"(old) : "v"(cnt), "v"(one) : "memory");
  if(__builtin_amdgcn_readfirstlane(old)==n-1){ if(lane==0) __hip_atomic_store(flag,nonce,RLX_AGENT); }
  MEMFENCE();
}
DI gu64* flagp(char* ws,int blk,int k){ return (gu64*)(ws+F_W+(unsigned)(blk*8+k)*128u); }
DI int blk_of(int R,int C){ return (((R&7)*4+(C&3))<<3)|((R>>3)*4+(C>>2)); }
#define STARG ,u64& dg_t
#define STPASS ,dg_t
#define ST(i) do{ if((i)==99){ SBAR(); asm volatile("s_memrealtime %0\n\ts_waitcnt lgkmcnt(0)" : "=s"(dg_t) :: "memory"); SBAR(); } }while(0)
#define STAMP_BEGIN() u64 dg_0; asm volatile("s_memrealtime %0\n\ts_waitcnt lgkmcnt(0)" : "=s"(dg_0) :: "memory"); u64 dg_t=dg_0
#define STAMP_END(ws) do{ ST(14); asm volatile("s_waitcnt vmcnt(0)":::"memory"); ST(15); if(wid==0){ const u64 tgt=dg_0+6000+(dg_t-dg_0); while(__builtin_amdgcn_s_memrealtime()<tgt) __builtin_amdgcn_s_sleep(2); } }while(0)

DI float fmax3(float a,float b,float c){ return __builtin_fmaxf(__builtin_fmaxf(a,b),c); }
DI float xhalf_max(float m){ auto rr=__builtin_amdgcn_permlane32_swap(asu(m),asu(m),false,false); return __builtin_fmaxf(asf(rr[0]),asf(rr[1])); }
struct G1F { bf16x8 x0,x1; s16x4 w[6]; };

DI const gu64* kvflag(char* ws,int R,int C,int kind,int head,int lane){
  const int Cs=(C&12)|((C-1-kind)&3); const int rr=(lane<3)? lane : 2; const int Rl=(R-2+rr<0)? R : R-2+rr;
  return flagp(ws,blk_of(Rl,Cs),FK_KV0+head);
}
template<bool BOTH>
DI void kv_fetch(char* ws,unsigned lds0,int R,int C,int kind,int head,int half,int lane){
  const int Cs=(C&12)|((C-1-kind)&3);
#pragma unroll 1
  for(int i=(R<2)?2-R:0;i<3;i++){
    const char* src=ws+WS_KV+(size_t)blk_of(R-2+i,Cs)*32768+head*16384+kind*8192+half*4096;
    const unsigned dst=lds0+(1-head)*HR+(1-kind)*24576+i*8192+half*4096;
    dma4_sc1(lane*16,src,dst); if(BOTH) dma4_sc1(lane*16,src+4096,dst+4096);
  }
}
DI void o_touch(char* ws,unsigned junk,int R2,int j,int head,int lane,u64 nonce){
  wait_flags<4>(flagp(ws,blk_of(R2,2*(j&7)+(lane&1)),FK_O0+head),nonce);
#pragma unroll
  for(int k=0;k<2;k++){
    const unsigned off=WS_O+(unsigned)(R2*4+(lane>>4))*65536u+(unsigned)((2*(j&7)+k)*4096+head*2048+(lane&15)*128);
    asm volatile("s_mov_b32 m0, %2\n\ts_nop 0\n\tglobal_load_lds_dword %0, %1 sc1" :: "v"(off), "s"(ws), "s"(junk) : "memory");
  }
}
template<bool CHECK>
DI void attention(char* lds,LAS unsigned* cnt,char* ws,__amdgpu_buffer_rsrc_t wsr,int b,int R,int C,int wid,int lane,u64 nonce STARG){
  const int late=wid>>2, jq=wid&3;
  const int T=4*R+jq;
  lptr VL=(lptr)lds+(1-late)*HR+jq*2048+lane*16;
  lptr KL=VL+24576;
  lptr QL=(lptr)lds+(1-late)*HR+49152+jq*2048+lane*16;
  const f32x16 zero=f32x16{};
  const float ninf=-__builtin_inff();
  const int dl=(lane&31)-4*(lane>>5);
  bf16x8 kf[9][2];
  const bf16x8 q0=ldsr128(QL), q1=ldsr128(QL+1024);
  kf[8][0]=ldsr128(KL+8*2048); kf[8][1]=ldsr128(KL+8*2048+1024);
#pragma unroll
  for(int u=0;u<4;u++){ kf[u][0]=ldsr128(KL+u*2048); kf[u][1]=ldsr128(KL+u*2048+1024); }
  f32x16 sd,sm,s[3];
  float m;
#pragma unroll
  for(int n=0;n<11;n++){
    SBAR();
    if(n==0){ sd=MFMA32(kf[8][0],q0,zero); sd=MFMA32(kf[8][1],q1,sd); PIN(sd); }
    else if(n==1){ sm=MFMA32(kf[0][0],q0,zero); sm=MFMA32(kf[0][1],q1,sm); PIN(sm); }
    else if(n<9){ const int u=n-1; s[u%3]=MFMA32(kf[u][0],q0,zero); s[u%3]=MFMA32(kf[u][1],q1,s[u%3]); PIN(s[u%3]); }
    if(n<4){ const int u=n+4; kf[u][0]=ldsr128(KL+u*2048); kf[u][1]=ldsr128(KL+u*2048+1024); }
    if(n==3){
#pragma unroll
      for(int i=0;i<16;i++){ const int a=(i&3)+8*(i>>2); sm[i]=(a<=dl)?sd[i]:((CHECK&&T<8)?ninf:sm[i]); }
    }
    if(n>=3){
      const int t=n-3; const f32x16& x=(t==0)?sm:s[t%3];
      float a=fmax3(x[0],x[1],x[2]), c=fmax3(x[3],x[4],x[5]);
      a=fmax3(a,x[6],x[7]); c=fmax3(c,x[8],x[9]); a=fmax3(a,x[10],x[11]); c=fmax3(c,x[12],x[13]); a=fmax3(a,x[14],x[15]);
      if(t==0) m=__builtin_fmaxf(a,c);
      else { const float mm=fmax3(m,a,c); m=(!CHECK||T-8+t>=0)?mm:m; }
      PIN(m);
    }
  }
  SBAR();
  ST(12);
  arrive(cnt+(late?C_P1L:C_P1E));
  if(late){
    if(jq==0){ if(flags_ready(flagp(ws,(lane&31)*8+(b&7),FK_Y),nonce)) arrive(cnt+C_YF); }
    WAITV(0); arrive(cnt+C_W1); lds_wait(cnt+C_E,4);
  }
  ST(13);
  bf16x8 vfr[2][2];
  vfr[0][0]=ldsr128(VL+8*2048); vfr[0][1]=ldsr128(VL+8*2048+1024);
  vfr[1][0]=ldsr128(VL); vfr[1][1]=ldsr128(VL+1024);
  const u32x4 mk0=*(const LAS u32x4*)((lptr)lds+L_MASK+lane*16), mk1=*(const LAS u32x4*)((lptr)lds+L_MASK+1024+lane*16);
  m=xhalf_max(m);
  f32x16 negm;
#pragma unroll
  for(int i=0;i<16;i++) negm[i]=-m;
  PIN(negm);
  const unsigned one2=((((lane>>2)^(lane>>4))&1)==0)?0x3f803f80u:0u;
  const u32x4 ones={one2,one2,one2,one2};
  f32x16 o; f32x4 ls;
  u32x4 p0,p1;
#pragma unroll
  for(int u=0;u<=8;u++){
    SBAR();
    u32x4 n0,n1;
    if(u+1<8){ const int t=u+1; s[t&1]=MFMA32(kf[t][0],q0,negm); s[t&1]=MFMA32(kf[t][1],q1,s[t&1]); }
    if(u<8){
      f32x16& x=(u==0)?sm:s[u&1];
#pragma unroll
      for(int i=0;i<16;i++) x[i]=__builtin_amdgcn_exp2f((u==0)?x[i]-m:x[i]);
      n0=pack8(x,0); n1=pack8(x,1);
      if(CHECK && u>0){ if(T-8+u<0){ n0=u32x4{0,0,0,0}; n1=u32x4{0,0,0,0}; } }
    }
    if(u==1){
      const u32x4 d0=p0&mk0, d1=p1&mk1;
      o=MFMA32(vfr[0][0],B8(d0),zero); o=MFMA32(vfr[0][1],B8(d1),o);
      o=MFMA32(vfr[1][0],B8(p0^d0),o); o=MFMA32(vfr[1][1],B8(p1^d1),o);
      vfr[1][0]=ldsr128(VL+1*2048); vfr[1][1]=ldsr128(VL+1*2048+1024);
      vfr[0][0]=ldsr128(VL+2*2048); vfr[0][1]=ldsr128(VL+2*2048+1024);
      ls=MFMA16(B8(ones),B8(p0),(f32x4{0,0,0,0})); ls=MFMA16(B8(ones),B8(p1),ls);
    } else if(u>1){
      const int t=u-1;
      o=MFMA32(vfr[t&1][0],B8(p0),o); o=MFMA32(vfr[t&1][1],B8(p1),o);
      if(t+2<8){ vfr[t&1][0]=ldsr128(VL+(t+2)*2048); vfr[t&1][1]=ldsr128(VL+(t+2)*2048+1024); }
      ls=MFMA16(B8(ones),B8(p0),ls); ls=MFMA16(B8(ones),B8(p1),ls);
    }
    if(u>1 && u<7 && !CHECK){
#pragma unroll
      for(int g_=0;g_<6;g_++){ __builtin_amdgcn_sched_group_barrier(0x8,1,0); __builtin_amdgcn_sched_group_barrier(0x402,4,0); }
    }
    if(u+1<8) PIN(s[(u+1)&1]);
    if(u>0){ PIN(ls); PIN(o); }
    if(u<8){ PIN(n0); PIN(n1); p0=n0; p1=n1; }
  }
  SBAR();
  arrive(cnt+(late?C_L:C_E));
  const float il=__builtin_amdgcn_rcpf(ls[0]);
  const unsigned oo=WS_O+(unsigned)(T*64+2*(2*C+late))*1024u+lane*16u;
  __builtin_amdgcn_raw_buffer_store_b128(pack8(o,0,il),wsr,oo,0,SC1);
  __builtin_amdgcn_raw_buffer_store_b128(pack8(o,1,il),wsr,oo+1024,0,SC1);
}

__global__ __launch_bounds__(512,2) NOPK void fused(const u16* Aq,const u16* Ak,const u16* Av,const u16* Ask,const u16* Ah,const u16* AD,
                                               const u16* X,const u16* W,const u16* WO,u16* out,char* ws){
  extern __shared__ __attribute__((aligned(16))) char lds[];
  const char* kpc; asm volatile("s_getpc_b64 %0":"=s"(kpc));
  asm volatile(""::"s"(X),"s"(W),"s"(ws),"s"(WO),"s"(Ak));
  STAMP_BEGIN();
  const int tid=threadIdx.x, lane=tid&63; const int wid=__builtin_amdgcn_readfirstlane(tid>>6);
  const int b=blockIdx.x, xcd=b&7, j=b>>3;
  const int R=(xcd>>2)*8+(j>>2), C=(xcd&3)*4+(j&3);
  const int wgi=xcd*32+j;
  const int R2=(xcd>>1)*4+(j>>3), C2=(xcd&1)*8+(j&7), KO=2*(j&7)+4*(j>>3);
  const unsigned lds0=(unsigned)(uintptr_t)lds;
  LAS unsigned* cnt=(LAS unsigned*)((lwptr)lds+L_CNT);
  const __amdgpu_buffer_rsrc_t wsr=__builtin_amdgcn_make_buffer_rsrc(ws,0,WS_END,0x00020000);
  const __amdgpu_buffer_rsrc_t outr=__builtin_amdgcn_make_buffer_rsrc(out,0,8*MB,0x00020000);
  u64 nonce;
#define LOAD_NONCE() do{ asm volatile("s_load_dwordx2 %0, %1, 0x0\n\ts_waitcnt lgkmcnt(0)" : "=s"(nonce) : "s"(ws+F_E) : "memory"); \
    nonce=nonce*6364136223846793005ull+1442695040888963407ull; }while(0)
  auto wo_block=[&](int i) NOPK __attribute__((always_inline)) {
    const int hd=2*((i+KO)&15)+(i>>4);
    const unsigned vo=(unsigned)((lane>>3)*2048+C2*128+(((lane&7)^(((lane>>4)&1)<<2))*16));
    dma4v(vo,vo+16384-1024,vo+2*(16384-1024),vo+3*(16384-1024),(const char*)WO+(size_t)hd*65536,lds0+L_WO+(31-i)*4096);
  };
  if(__builtin_expect(wid>=4,1)){
    const int v=wid-4;
    const unsigned junk=lds0+L_JUNK;
    if(__builtin_expect(wid==4,1)){
      const unsigned a0=(unsigned)((lane>>3)*2048+(((lane&7)^(lane>>4))*16)), a1=(a0^64u)+(16384-1024);
      const unsigned b0=(unsigned)((lane>>3)*6144+(((lane&7)^(((lane>>4)&1)<<2))*16));
      const char* xb=(const char*)X+(size_t)R*128*2048; const char* wb=(const char*)W+(C&12)*128;
      const int cs[3]={(C&3)*128,2048+((C+1)&3)*128,4096+((C+2)&3)*128};
      unsigned la=lds0;
      ST(1);
#pragma unroll 1
      for(int k=-2;k<14;k++){
        if(k>=0){ WAITV(40); BAR(); }
#pragma unroll
        for(int g=0;g<4;g++){
          dma4v(a0,a1,a0+2*(16384-1024),a1+2*(16384-1024),xb+g*65536,la+g*4096);
          if(g<3){
            dma4v(b0,b0+(49152-1024),b0+2*(49152-1024),b0+3*(49152-1024),wb+cs[g],la+16384+g*8192);
            dma4v(b0,b0+(49152-1024),b0+2*(49152-1024),b0+3*(49152-1024),wb+cs[g]+32*6144,la+16384+g*8192+4096);
          }
        }
        xb+=128; wb+=64*6144; la+=G1_SLOT; if(la==lds0+3*G1_SLOT) la=lds0;
      }
      LOAD_NONCE();
      WAITV(40); BAR();
    } else if(wid==5){
      __builtin_amdgcn_s_setprio(3);
      const char* ta; unsigned tstep;
      { const int sg=lane/20, i=lane-sg*20; const bool isx=i<8; const int rem=i-8, kr=(xcd>>2)*32+j;
        tstep=isx?128u:64u*6144u;
        ta=(isx? (const char*)X+(size_t)((xcd>>2)*1024+((xcd&3)*32+j)*8+i)*2048 : (const char*)W+(kr*6144+(rem>>2)*2048+((xcd&3)*4+(rem&3))*128))+(size_t)tstep*(1+sg);
        tstep*=3; }
#define STOUCH() do{ if(lane<60) touch64(ta,junk); ta+=tstep; }while(0)
      __builtin_amdgcn_s_sleep(10);
      STOUCH();
      if(lane<CODE_LINES/32) touch((unsigned)((j*(CODE_LINES/32)+lane)*128),kpc,junk);
      if(lane<6) touch((unsigned)(F_W+(b*8+lane)*128),ws,junk);
      if(j==0) *(LAS u32x4*)((lwptr)lds+L_CV+lane*16)=u32x4{0,0,0,0};
      if(lane<28) cnt[lane]=0u;
      { const int bq=lane&31, hi=lane>>5; u32x4 mk[2];
#pragma unroll
        for(int d=0;d<8;d++){ const int i=2*d; const int a=(i&3)+8*(i>>2)+4*hi; mk[d>>2][d&3]=((a<=bq)?0xffffu:0u)|((a+1<=bq)?0xffff0000u:0u); }
        *(LAS u32x4*)((lwptr)lds+L_MASK+lane*16)=mk[0]; *(LAS u32x4*)((lwptr)lds+L_MASK+1024+lane*16)=mk[1]; }
      LOAD_NONCE();
      BAR();
      int pc=(j==0)?1:0;
#pragma unroll 1
      for(int i=0;i<12;i++){
        if(i<4) STOUCH();
#pragma unroll 1
        for(;pc<33 && pc<(i+1)*KV_PER;pc++){
          const bool isk=pc<17; const int r0=isk? 4*pc-4 : 4*(pc-17);
          const unsigned vo=(unsigned)((lane>>4)*2048+(((lane&15)^(((r0>>3)&7)<<1))*16));
          const char* src=(const char*)(isk?Ak:Av)+(size_t)(j*64+r0)*2048+xcd*256;
          asm volatile("s_mov_b32 m0, %2\n\ts_nop 0\n\tglobal_load_lds_dwordx4 %0, %1" PFLAG :: "v"(vo), "s"(src), "s"(lds0+L_CV+pc*1024) : "memory");
        }
        if(i==PREP0-1) WAITV(1);
        BAR();
      }
      u64 fxv=__hip_atomic_load(flagp(ws,(lane&31)*8+xcd,FK_X),RLX_AGENT);
      BAR();
      SBAR(); PIN(fxv);
      if(allof(fxv==nonce)){ arrive(cnt+C_XF); WAITL(); }
      BAR();
      __builtin_amdgcn_s_setprio(0);
    } else {
      __builtin_amdgcn_s_setprio(1);
      const int hv=wid-6, rg=lane&7;
      u32x4 sk[4];
      LOAD_NONCE();
      BAR();
#pragma unroll
      for(int i=0;i<4;i++) sk[i]=*(const u32x4*)(Ask+(xcd*128+(hv*16+(i>>1)*8+(lane>>3))*4)*4+(i&1)*8);
#pragma unroll 1
      for(int i=0;i<PREP0;i++) BAR();
      SBAR();
      asm volatile("":"+v"(sk[0]),"+v"(sk[1]),"+v"(sk[2]),"+v"(sk[3]));
#pragma unroll 1
      for(int g=0;g<2;g++){
        const int c4=hv*16+g*8+(lane>>3);
        u32x2 kk[11], vv[8];
        lptr kb=(lptr)lds+L_CV+(8*rg+4)*256+(c4&1)*8;
#pragma unroll
        for(int i=0;i<3;i++) kk[i]=*(const LAS u32x2*)(kb+(i-3)*256+(((c4>>1)^(((rg-1)&7)<<1))*16));
#pragma unroll
        for(int i=0;i<8;i++){ kk[3+i]=*(const LAS u32x2*)(kb+i*256+(((c4>>1)^(rg<<1))*16)); vv[i]=*(const LAS u32x2*)(kb+16384+i*256+(((c4>>1)^(rg<<1))*16)); }
#pragma unroll
        for(int c=0;c<4;c++){
          const unsigned w01=sk[c>>1][(c&1)*2], w23=sk[c>>1][(c&1)*2+1];
          const float w0=lo16(w01),w1=hi16(w01),w2=lo16(w23),w3=hi16(w23);
          float kf[11];
#pragma unroll
          for(int i=0;i<11;i++) kf[i]=(c&1)?hi16(kk[i][c>>1]):lo16(kk[i][c>>1]);
          float o[8];
#pragma unroll
          for(int i=0;i<8;i++){ float vf=(c&1)?hi16(vv[i][c>>1]):lo16(vv[i][c>>1]); o[i]=(kf[i]*w3+kf[i+1]*w2+kf[i+2]*w1+kf[i+3]*w0)*vf; }
          u32x4 r; r[0]=pk(o[0],o[1]); r[1]=pk(o[2],o[3]); r[2]=pk(o[4],o[5]); r[3]=pk(o[6],o[7]);
          __builtin_amdgcn_raw_buffer_store_b128(r,wsr,WS_XT+(unsigned)(xcd*128+c4*4+c)*4096u+(unsigned)(j*128+rg*16),0,SC1);
          SBAR();
          if(c!=1){ BAR(); SBAR(); }
        }
        sk[0]=sk[2]; sk[1]=sk[3];
      }
      WAITV(0);
      BAR();
      if(hv==0 && lane==0) __hip_atomic_store(flagp(ws,b,FK_X),nonce,RLX_AGENT);
#pragma unroll 1
      for(int i=PREP0+8;i<=13;i++) BAR();
      __builtin_amdgcn_s_setprio(0);
      __builtin_amdgcn_s_sleep(8);
      dma4(lane*16,(const char*)Ah+(size_t)(wgi*4+2*hv)*4096,lds0+L_CV+(2*hv)*CV_W+CV_H);
      dma4(lane*16,(const char*)Ah+(size_t)(wgi*4+2*hv+1)*4096,lds0+L_CV+(2*hv+1)*CV_W+CV_H);
      BAR();
    }
    const int ch=wgi*4+v;
    const float Dc=lo16(AD[ch]);
    if(lane<32) *(LAS unsigned*)((lwptr)lds+L_CV+v*CV_W+lane*4)=0u;
    if(lane<40) *(LAS unsigned*)((lwptr)lds+L_CV+v*CV_W+CV_H+4096+lane*4)=0u;
    const bool xr=__builtin_amdgcn_readfirstlane(cnt[C_XF])!=0;
    if(xr){ dma4_sc1(lane*16,ws+WS_XT+(size_t)ch*4096,lds0+L_CV+v*CV_W+CV_X); WAITV(4); }
    else WAITV(0);
    __builtin_amdgcn_s_setprio(1);
    BAR();
    ST(3);
    if(!xr){ wait_flags<4>(flagp(ws,(lane&31)*8+xcd,FK_X),nonce); dma4_sc1(lane*16,ws+WS_XT+(size_t)ch*4096,lds0+L_CV+v*CV_W+CV_X); }
    bool h1;
    {
      lwptr LW=(lwptr)lds+L_CV+v*CV_W;
      const int g=lane>>4,q=(lane&15)>>2,p=lane&3;
      lptr ha=(lptr)LW+CV_H+8*(4*p+8*g+q-12);
      lptr xa=(lptr)LW+CV_X+8*(p+8-8*g-q);
      PIN(ha); PIN(xa);
      bf16x8 A[16],B0[16],B1[16];
#pragma unroll
      for(int d=0;d<16;d++) A[d]=cat(ldstr(ha+256*d),ldstr(ha+256*d+32));
      WAITV(0);
      ST(4);
      float zf=0.f; PIN(zf);
#define MM0(z,a,b) asm volatile("v_mfma_f32_16x16x32_bf16 %0, %1, %2, 0" : "=&v"(z) : "v"(a), "v"(b))
#define MMA(z,a,b) asm volatile("v_mfma_f32_16x16x32_bf16 %0, %1, %2, %0" : "+v"(z) : "v"(a), "v"(b))
#define LDB(P,e) B##P[e]=cat(ldstr(xa+256*(e)+128*(P)+32),ldstr(xa+256*(e)+128*(P)))
#define ROR(d,s,z) asm volatile("v_add_f32_dpp %0, %1, %2 wave_ror:1 row_mask:0xf bank_mask:0xf" : "=&v"(d) : "v"(s), "v"(z))
#define SHR(d,s,z) asm volatile("v_add_f32_dpp %0, %1, %2 wave_shr:1 row_mask:0xf bank_mask:0xf" : "+v"(d) : "v"(s), "v"(z))
#define ROR2(d,s,z,e,t,y) asm volatile("v_add_f32_dpp %0, %2, %3 wave_ror:1 row_mask:0xf bank_mask:0xf\n\tv_add_f32_dpp %1, %4, %5 wave_ror:1 row_mask:0xf bank_mask:0xf" : "=&v"(d), "=&v"(e) : "v"(s), "v"(z), "v"(t), "v"(y))
#define SHR2(d,s,z,e,t,y) asm volatile("v_add_f32_dpp %0, %2, %3 wave_shr:1 row_mask:0xf bank_mask:0xf\n\tv_add_f32_dpp %1, %4, %5 wave_shr:1 row_mask:0xf bank_mask:0xf" : "+v"(d), "+v"(e) : "v"(s), "v"(z), "v"(t), "v"(y))
      const unsigned yo=WS_Y+(unsigned)ch*4096u+lane*16u;
#define STY(C) __builtin_amdgcn_raw_buffer_store_b128(yw,wsr,yo+(C)*1024,0,SC1)
#define NOP1() asm volatile("s_nop 0")
#define VNOP() __builtin_amdgcn_s_setprio(1)
      unsigned xa_,xb_;
      lptr xe=(lptr)LW+CV_X+2*lane; PIN(xe);
#define LDX(c) do{ xa_=*(const LAS u16*)(xe+256*(c)); xb_=*(const LAS u16*)(xe+256*(c)+128); }while(0)
#define SHX() do{ xa_<<=16; xb_<<=16; PIN(xa_); PIN(xb_); }while(0)
#define FMD(z,x) do{ z[0]=__builtin_fmaf(asf(x),Dc,z[0]); PIN(z); }while(0)
      const gu64* kf1=kvflag(ws,R,C,v>>1,1,lane);
      u64 f1v; unsigned c1v;
#define HOOK1() do{ f1v=__hip_atomic_load(kf1,RLX_AGENT); c1v=*(volatile LAS unsigned*)(cnt+C_KV1); }while(0)
#define HOOK2() do{ h1=allof(f1v==nonce) && __builtin_amdgcn_readfirstlane(c1v)>=2; MEMFENCE(); if(h1) kv_fetch<false>(ws,lds0,R,C,v>>1,1,v&1,lane); }while(0)
      SBAR();
f32x4 z0,z1,z2,z3,z4,z5,z6,z7,z8,z9,z10,z11,z12,z13,z14,z15,z16,z17,z18,z19,z20,z21,z22,z23,z24,z25,z26,z27,z28,z29,z30,z31;
float t2_0,t1_0,u_0,t2_1,t1_1,u_1,t2_2,t1_2,u_2,t2_3,t1_3,u_3,t2_4,t1_4,u_4,t2_5,t1_5,u_5,t2_6,t1_6,u_6,t2_7,t1_7,u_7,t2_8,t1_8,u_8,t2_9,t1_9,u_9,t2_10,t1_10,u_10,t2_11,t1_11,u_11,t2_12,t1_12,u_12,t2_13,t1_13,u_13,t2_14,t1_14,u_14,t2_15,t1_15,u_15,t2_16,t1_16,u_16,t2_17,t1_17,u_17,t2_18,t1_18,u_18,t2_19,t1_19,u_19,t2_20,t1_20,u_20,t2_21,t1_21,u_21,t2_22,t1_22,u_22,t2_23,t1_23,u_23,t2_24,t1_24,u_24,t2_25,t1_25,u_25,t2_26,t1_26,u_26,t2_27,t1_27,u_27,t2_28,t1_28,u_28,t2_29,t1_29,u_29,t2_30,t1_30,u_30,t2_31,t1_31,u_31;
u32x4 yw;

LDB(0,0); LDB(1,0);
LDB(0,1); LDB(1,1);
LDB(0,2); LDB(1,2);
LDX(0);
SBAR();
MM0(z0,A[0],B0[0]); SBAR(); LDB(0,3); SBAR();
MM0(z1,A[0],B1[0]); SBAR(); LDB(1,3); SBAR();
MM0(z2,A[1],B0[0]); SBAR(); LDB(0,4); SBAR();
MM0(z3,A[1],B1[0]); SBAR(); LDB(1,4); SBAR();
MMA(z2,A[0],B0[1]); SBAR();  SBAR();
MMA(z3,A[0],B1[1]); SBAR();  SBAR();
MM0(z4,A[2],B0[0]); SBAR();  SBAR();
MM0(z5,A[2],B1[0]); SBAR();  SBAR();
MMA(z4,A[1],B0[1]); SBAR();  SBAR();
MMA(z5,A[1],B1[1]); SBAR();  SBAR();
MMA(z4,A[0],B0[2]); SBAR(); ROR2(t2_0,zf,z0[2],t2_1,z0[3],z1[2]); SBAR();
MMA(z5,A[0],B1[2]); SBAR(); LDB(0,5); SBAR();
MM0(z6,A[3],B0[0]); SBAR(); SHR2(t2_0,z0[3],z0[2],t2_1,z1[3],z1[2]); SBAR();
MM0(z7,A[3],B1[0]); SBAR(); LDB(1,5); SBAR();
MMA(z6,A[2],B0[1]); SBAR(); ROR2(t1_0,zf,z0[1],t1_1,t2_0,z1[1]); SBAR();
MMA(z7,A[2],B1[1]); SBAR(); SHX(); SBAR();
MMA(z6,A[1],B0[2]); SBAR(); SHR2(t1_0,t2_0,z0[1],t1_1,t2_1,z1[1]); SBAR();
MMA(z7,A[1],B1[2]); SBAR(); FMD(z0,xa_); FMD(z1,xb_); SBAR();
MMA(z6,A[0],B0[3]); SBAR(); ROR2(u_0,zf,z0[0],u_1,t1_0,z1[0]); SBAR();
MMA(z7,A[0],B1[3]); SBAR(); LDX(1); SBAR();
MM0(z8,A[4],B0[0]); SBAR(); SHR2(u_0,t1_0,z0[0],u_1,t1_1,z1[0]); SBAR();
MM0(z9,A[4],B1[0]); SBAR(); yw[0]=pk(u_0,u_1); PIN(yw); SBAR();
MMA(z8,A[3],B0[1]); SBAR(); ROR2(t2_2,z1[3],z2[2],t2_3,z2[3],z3[2]); SBAR();
MMA(z9,A[3],B1[1]); SBAR(); LDB(0,6); SBAR();
MMA(z8,A[2],B0[2]); SBAR(); SHR2(t2_2,z2[3],z2[2],t2_3,z3[3],z3[2]); SBAR();
MMA(z9,A[2],B1[2]); SBAR(); LDB(1,6); SBAR();
MMA(z8,A[1],B0[3]); SBAR(); ROR2(t1_2,t2_1,z2[1],t1_3,t2_2,z3[1]); SBAR();
MMA(z9,A[1],B1[3]); SBAR(); SHX(); SBAR();
MMA(z8,A[0],B0[4]); SBAR(); SHR2(t1_2,t2_2,z2[1],t1_3,t2_3,z3[1]); SBAR();
MMA(z9,A[0],B1[4]); SBAR(); FMD(z2,xa_); FMD(z3,xb_); SBAR();
MM0(z10,A[5],B0[0]); SBAR(); ROR2(u_2,t1_1,z2[0],u_3,t1_2,z3[0]); SBAR();
MM0(z11,A[5],B1[0]); SBAR(); LDX(2); SBAR();
MMA(z10,A[4],B0[1]); SBAR(); SHR2(u_2,t1_2,z2[0],u_3,t1_3,z3[0]); SBAR();
MMA(z11,A[4],B1[1]); SBAR(); yw[1]=pk(u_2,u_3); PIN(yw); SBAR();
MMA(z10,A[3],B0[2]); SBAR(); ROR2(t2_4,z3[3],z4[2],t2_5,z4[3],z5[2]); SBAR();
MMA(z11,A[3],B1[2]); SBAR(); LDB(0,7); SBAR();
MMA(z10,A[2],B0[3]); SBAR(); SHR2(t2_4,z4[3],z4[2],t2_5,z5[3],z5[2]); SBAR();
MMA(z11,A[2],B1[3]); SBAR(); LDB(1,7); SBAR();
MMA(z10,A[1],B0[4]); SBAR(); ROR2(t1_4,t2_3,z4[1],t1_5,t2_4,z5[1]); SBAR();
MMA(z11,A[1],B1[4]); SBAR(); SHX(); SBAR();
MMA(z10,A[0],B0[5]); SBAR(); SHR2(t1_4,t2_4,z4[1],t1_5,t2_5,z5[1]); SBAR();
MMA(z11,A[0],B1[5]); SBAR(); FMD(z4,xa_); FMD(z5,xb_); SBAR();
MM0(z12,A[6],B0[0]); SBAR(); ROR2(u_4,t1_3,z4[0],u_5,t1_4,z5[0]); SBAR();
MM0(z13,A[6],B1[0]); SBAR(); LDX(3); SBAR();
MMA(z12,A[5],B0[1]); SBAR(); SHR2(u_4,t1_4,z4[0],u_5,t1_5,z5[0]); SBAR();
MMA(z13,A[5],B1[1]); SBAR(); yw[2]=pk(u_4,u_5); PIN(yw); SBAR();
MMA(z12,A[4],B0[2]); SBAR(); ROR2(t2_6,z5[3],z6[2],t2_7,z6[3],z7[2]); SBAR();
MMA(z13,A[4],B1[2]); SBAR(); LDB(0,8); SBAR();
MMA(z12,A[3],B0[3]); SBAR(); SHR2(t2_6,z6[3],z6[2],t2_7,z7[3],z7[2]); SBAR();
MMA(z13,A[3],B1[3]); SBAR(); LDB(1,8); SBAR();
MMA(z12,A[2],B0[4]); SBAR(); ROR2(t1_6,t2_5,z6[1],t1_7,t2_6,z7[1]); SBAR();
MMA(z13,A[2],B1[4]); SBAR(); SHX(); SBAR();
MMA(z12,A[1],B0[5]); SBAR(); SHR2(t1_6,t2_6,z6[1],t1_7,t2_7,z7[1]); SBAR();
MMA(z13,A[1],B1[5]); SBAR(); FMD(z6,xa_); FMD(z7,xb_); SBAR();
MMA(z12,A[0],B0[6]); SBAR(); ROR2(u_6,t1_5,z6[0],u_7,t1_6,z7[0]); SBAR();
MMA(z13,A[0],B1[6]); SBAR(); LDX(4); SBAR();
MM0(z14,A[7],B0[0]); SBAR(); SHR2(u_6,t1_6,z6[0],u_7,t1_7,z7[0]); SBAR();
MM0(z15,A[7],B1[0]); SBAR(); yw[3]=pk(u_6,u_7); PIN(yw); STY(0); SBAR();
MMA(z14,A[6],B0[1]); SBAR(); ROR2(t2_8,z7[3],z8[2],t2_9,z8[3],z9[2]); SBAR();
MMA(z15,A[6],B1[1]); SBAR(); LDB(0,9); SBAR();
MMA(z14,A[5],B0[2]); SBAR(); SHR2(t2_8,z8[3],z8[2],t2_9,z9[3],z9[2]); SBAR();
MMA(z15,A[5],B1[2]); SBAR(); LDB(1,9); SBAR();
MMA(z14,A[4],B0[3]); SBAR(); ROR2(t1_8,t2_7,z8[1],t1_9,t2_8,z9[1]); SBAR();
MMA(z15,A[4],B1[3]); SBAR(); SHX(); SBAR();
MMA(z14,A[3],B0[4]); SBAR(); SHR2(t1_8,t2_8,z8[1],t1_9,t2_9,z9[1]); SBAR();
MMA(z15,A[3],B1[4]); SBAR(); FMD(z8,xa_); FMD(z9,xb_); SBAR();
MMA(z14,A[2],B0[5]); SBAR(); ROR2(u_8,t1_7,z8[0],u_9,t1_8,z9[0]); SBAR();
MMA(z15,A[2],B1[5]); SBAR(); LDX(5); SBAR();
MMA(z14,A[1],B0[6]); SBAR(); SHR2(u_8,t1_8,z8[0],u_9,t1_9,z9[0]); SBAR();
MMA(z15,A[1],B1[6]); SBAR(); yw[0]=pk(u_8,u_9); PIN(yw); SBAR();
MMA(z14,A[0],B0[7]); SBAR(); ROR2(t2_10,z9[3],z10[2],t2_11,z10[3],z11[2]); SBAR();
MMA(z15,A[0],B1[7]); SBAR(); LDB(0,10); SBAR();
MM0(z16,A[8],B0[0]); SBAR(); SHR2(t2_10,z10[3],z10[2],t2_11,z11[3],z11[2]); SBAR();
MM0(z17,A[8],B1[0]); SBAR(); LDB(1,10); SBAR();
MMA(z16,A[7],B0[1]); SBAR(); ROR2(t1_10,t2_9,z10[1],t1_11,t2_10,z11[1]); SBAR();
MMA(z17,A[7],B1[1]); SBAR(); SHX(); SBAR();
MMA(z16,A[6],B0[2]); SBAR(); SHR2(t1_10,t2_10,z10[1],t1_11,t2_11,z11[1]); SBAR();
MMA(z17,A[6],B1[2]); SBAR(); FMD(z10,xa_); FMD(z11,xb_); SBAR();
MMA(z16,A[5],B0[3]); SBAR(); ROR2(u_10,t1_9,z10[0],u_11,t1_10,z11[0]); SBAR();
MMA(z17,A[5],B1[3]); SBAR(); LDX(6); SBAR();
MMA(z16,A[4],B0[4]); SBAR(); SHR2(u_10,t1_10,z10[0],u_11,t1_11,z11[0]); SBAR();
MMA(z17,A[4],B1[4]); SBAR(); yw[1]=pk(u_10,u_11); PIN(yw); SBAR();
MMA(z16,A[3],B0[5]); SBAR(); ROR2(t2_12,z11[3],z12[2],t2_13,z12[3],z13[2]); SBAR();
MMA(z17,A[3],B1[5]); SBAR(); LDB(0,11); SBAR();
MMA(z16,A[2],B0[6]); SBAR(); SHR2(t2_12,z12[3],z12[2],t2_13,z13[3],z13[2]); SBAR();
MMA(z17,A[2],B1[6]); SBAR(); LDB(1,11); SBAR();
MMA(z16,A[1],B0[7]); SBAR(); ROR2(t1_12,t2_11,z12[1],t1_13,t2_12,z13[1]); SBAR();
MMA(z17,A[1],B1[7]); SBAR(); SHX(); SBAR();
MMA(z16,A[0],B0[8]); SBAR(); SHR2(t1_12,t2_12,z12[1],t1_13,t2_13,z13[1]); SBAR();
MMA(z17,A[0],B1[8]); SBAR(); FMD(z12,xa_); FMD(z13,xb_); SBAR();
MM0(z18,A[9],B0[0]); SBAR(); ROR2(u_12,t1_11,z12[0],u_13,t1_12,z13[0]); SBAR();
MM0(z19,A[9],B1[0]); SBAR(); LDX(7); SBAR();
MMA(z18,A[8],B0[1]); SBAR(); SHR2(u_12,t1_12,z12[0],u_13,t1_13,z13[0]); SBAR();
MMA(z19,A[8],B1[1]); SBAR(); yw[2]=pk(u_12,u_13); PIN(yw); SBAR();
MMA(z18,A[7],B0[2]); SBAR(); ROR2(t2_14,z13[3],z14[2],t2_15,z14[3],z15[2]); SBAR();
MMA(z19,A[7],B1[2]); SBAR(); LDB(0,12); SBAR();
MMA(z18,A[6],B0[3]); SBAR(); SHR2(t2_14,z14[3],z14[2],t2_15,z15[3],z15[2]); SBAR();
MMA(z19,A[6],B1[3]); SBAR(); LDB(1,12); SBAR();
MMA(z18,A[5],B0[4]); SBAR(); ROR2(t1_14,t2_13,z14[1],t1_15,t2_14,z15[1]); SBAR();
MMA(z19,A[5],B1[4]); SBAR(); SHX(); SBAR();
MMA(z18,A[4],B0[5]); SBAR(); SHR2(t1_14,t2_14,z14[1],t1_15,t2_15,z15[1]); SBAR();
MMA(z19,A[4],B1[5]); SBAR(); FMD(z14,xa_); FMD(z15,xb_); SBAR();
MMA(z18,A[3],B0[6]); SBAR(); ROR2(u_14,t1_13,z14[0],u_15,t1_14,z15[0]); SBAR();
MMA(z19,A[3],B1[6]); SBAR(); LDX(8); SBAR();
MMA(z18,A[2],B0[7]); SBAR(); SHR2(u_14,t1_14,z14[0],u_15,t1_15,z15[0]); SBAR();
MMA(z19,A[2],B1[7]); SBAR(); yw[3]=pk(u_14,u_15); PIN(yw); STY(1); SBAR();
MMA(z18,A[1],B0[8]); SBAR(); ROR2(t2_16,z15[3],z16[2],t2_17,z16[3],z17[2]); SBAR();
MMA(z19,A[1],B1[8]); SBAR(); LDB(0,13); SBAR();
MMA(z18,A[0],B0[9]); SBAR(); SHR2(t2_16,z16[3],z16[2],t2_17,z17[3],z17[2]); SBAR();
MMA(z19,A[0],B1[9]); SBAR(); LDB(1,13); SBAR();
MM0(z20,A[10],B0[0]); SBAR(); ROR2(t1_16,t2_15,z16[1],t1_17,t2_16,z17[1]); SBAR();
MM0(z21,A[10],B1[0]); SBAR(); SHX(); SBAR();
MMA(z20,A[9],B0[1]); SBAR(); SHR2(t1_16,t2_16,z16[1],t1_17,t2_17,z17[1]); SBAR();
MMA(z21,A[9],B1[1]); SBAR(); FMD(z16,xa_); FMD(z17,xb_); SBAR();
MMA(z20,A[8],B0[2]); SBAR(); ROR2(u_16,t1_15,z16[0],u_17,t1_16,z17[0]); SBAR();
MMA(z21,A[8],B1[2]); SBAR(); LDX(9); SBAR();
MMA(z20,A[7],B0[3]); SBAR(); SHR2(u_16,t1_16,z16[0],u_17,t1_17,z17[0]); SBAR();
MMA(z21,A[7],B1[3]); SBAR(); yw[0]=pk(u_16,u_17); PIN(yw); SBAR();
MMA(z20,A[6],B0[4]); SBAR(); ROR2(t2_18,z17[3],z18[2],t2_19,z18[3],z19[2]); SBAR();
MMA(z21,A[6],B1[4]); SBAR(); LDB(0,14); SBAR();
MMA(z20,A[5],B0[5]); SBAR(); SHR2(t2_18,z18[3],z18[2],t2_19,z19[3],z19[2]); SBAR();
MMA(z21,A[5],B1[5]); SBAR(); LDB(1,14); SBAR();
MMA(z20,A[4],B0[6]); SBAR(); ROR2(t1_18,t2_17,z18[1],t1_19,t2_18,z19[1]); SBAR();
MMA(z21,A[4],B1[6]); SBAR(); SHX(); SBAR();
MMA(z20,A[3],B0[7]); SBAR(); SHR2(t1_18,t2_18,z18[1],t1_19,t2_19,z19[1]); SBAR();
MMA(z21,A[3],B1[7]); SBAR(); FMD(z18,xa_); FMD(z19,xb_); SBAR();
MMA(z20,A[2],B0[8]); SBAR(); ROR2(u_18,t1_17,z18[0],u_19,t1_18,z19[0]); SBAR();
MMA(z21,A[2],B1[8]); SBAR(); LDX(10); SBAR();
MMA(z20,A[1],B0[9]); SBAR(); SHR2(u_18,t1_18,z18[0],u_19,t1_19,z19[0]); SBAR();
MMA(z21,A[1],B1[9]); SBAR(); yw[1]=pk(u_18,u_19); PIN(yw); SBAR();
MMA(z20,A[0],B0[10]); SBAR();  SBAR();
MMA(z21,A[0],B1[10]); SBAR();  SBAR();
MM0(z22,A[11],B0[0]); SBAR();  SBAR();
MM0(z23,A[11],B1[0]); SBAR();  SBAR();
MMA(z22,A[10],B0[1]); SBAR();  SBAR();
MMA(z23,A[10],B1[1]); SBAR();  SBAR();
MMA(z22,A[9],B0[2]); SBAR();  SBAR();
MMA(z23,A[9],B1[2]); SBAR();  SBAR();
MMA(z22,A[8],B0[3]); SBAR();  SBAR();
MMA(z23,A[8],B1[3]); SBAR();  SBAR();
MMA(z22,A[7],B0[4]); SBAR(); ROR2(t2_20,z19[3],z20[2],t2_21,z20[3],z21[2]); SBAR();
MMA(z23,A[7],B1[4]); SBAR(); LDB(0,15); SBAR();
MMA(z22,A[6],B0[5]); SBAR(); SHR2(t2_20,z20[3],z20[2],t2_21,z21[3],z21[2]); SBAR();
MMA(z23,A[6],B1[5]); SBAR(); LDB(1,15); SBAR();
MMA(z22,A[5],B0[6]); SBAR(); ROR2(t1_20,t2_19,z20[1],t1_21,t2_20,z21[1]); SBAR();
MMA(z23,A[5],B1[6]); SBAR(); SHX(); SBAR();
MMA(z22,A[4],B0[7]); SBAR(); SHR2(t1_20,t2_20,z20[1],t1_21,t2_21,z21[1]); SBAR();
MMA(z23,A[4],B1[7]); SBAR(); FMD(z20,xa_); FMD(z21,xb_); SBAR();
MMA(z22,A[3],B0[8]); SBAR(); ROR2(u_20,t1_19,z20[0],u_21,t1_20,z21[0]); SBAR();
MMA(z23,A[3],B1[8]); SBAR(); LDX(11); SBAR();
MMA(z22,A[2],B0[9]); SBAR(); SHR2(u_20,t1_20,z20[0],u_21,t1_21,z21[0]); SBAR();
MMA(z23,A[2],B1[9]); SBAR(); yw[2]=pk(u_20,u_21); PIN(yw); SBAR();
MMA(z22,A[1],B0[10]); SBAR();  SBAR();
MMA(z23,A[1],B1[10]); SBAR();  SBAR();
MMA(z22,A[0],B0[11]); SBAR();  SBAR();
MMA(z23,A[0],B1[11]); SBAR();  SBAR();
MM0(z24,A[12],B0[0]); SBAR();  SBAR();
MM0(z25,A[12],B1[0]); SBAR();  SBAR();
MMA(z24,A[11],B0[1]); SBAR();  SBAR();
MMA(z25,A[11],B1[1]); SBAR();  SBAR();
MMA(z24,A[10],B0[2]); SBAR();  SBAR();
MMA(z25,A[10],B1[2]); SBAR();  SBAR();
MMA(z24,A[9],B0[3]); SBAR();  SBAR();
MMA(z25,A[9],B1[3]); SBAR();  SBAR();
MMA(z24,A[8],B0[4]); SBAR(); ROR2(t2_22,z21[3],z22[2],t2_23,z22[3],z23[2]); SBAR();
MMA(z25,A[8],B1[4]); SBAR(); VNOP(); SBAR();
MMA(z24,A[7],B0[5]); SBAR(); SHR2(t2_22,z22[3],z22[2],t2_23,z23[3],z23[2]); SBAR();
MMA(z25,A[7],B1[5]); SBAR(); VNOP(); SBAR();
MMA(z24,A[6],B0[6]); SBAR(); ROR2(t1_22,t2_21,z22[1],t1_23,t2_22,z23[1]); SBAR();
MMA(z25,A[6],B1[6]); SBAR(); SHX(); SBAR();
MMA(z24,A[5],B0[7]); SBAR(); SHR2(t1_22,t2_22,z22[1],t1_23,t2_23,z23[1]); SBAR();
MMA(z25,A[5],B1[7]); SBAR(); FMD(z22,xa_); FMD(z23,xb_); SBAR();
MMA(z24,A[4],B0[8]); SBAR(); ROR2(u_22,t1_21,z22[0],u_23,t1_22,z23[0]); SBAR();
MMA(z25,A[4],B1[8]); SBAR(); LDX(12); SBAR();
MMA(z24,A[3],B0[9]); SBAR(); SHR2(u_22,t1_22,z22[0],u_23,t1_23,z23[0]); SBAR();
MMA(z25,A[3],B1[9]); SBAR(); yw[3]=pk(u_22,u_23); PIN(yw); STY(2); SBAR();
MMA(z24,A[2],B0[10]); SBAR();  SBAR();
MMA(z25,A[2],B1[10]); SBAR();  SBAR();
MMA(z24,A[1],B0[11]); SBAR();  SBAR();
MMA(z25,A[1],B1[11]); SBAR();  SBAR();
MMA(z24,A[0],B0[12]); SBAR();  SBAR();
MMA(z25,A[0],B1[12]); SBAR();  SBAR();
MM0(z26,A[13],B0[0]); SBAR();  SBAR();
MM0(z27,A[13],B1[0]); SBAR();  SBAR();
MMA(z26,A[12],B0[1]); SBAR();  SBAR();
MMA(z27,A[12],B1[1]); SBAR();  SBAR();
MMA(z26,A[11],B0[2]); SBAR();  SBAR();
MMA(z27,A[11],B1[2]); SBAR();  SBAR();
MMA(z26,A[10],B0[3]); SBAR();  SBAR();
MMA(z27,A[10],B1[3]); SBAR();  SBAR();
MMA(z26,A[9],B0[4]); SBAR(); ROR2(t2_24,z23[3],z24[2],t2_25,z24[3],z25[2]); SBAR();
MMA(z27,A[9],B1[4]); SBAR(); VNOP(); SBAR();
MMA(z26,A[8],B0[5]); SBAR(); SHR2(t2_24,z24[3],z24[2],t2_25,z25[3],z25[2]); SBAR();
MMA(z27,A[8],B1[5]); SBAR(); VNOP(); SBAR();
MMA(z26,A[7],B0[6]); SBAR(); ROR2(t1_24,t2_23,z24[1],t1_25,t2_24,z25[1]); SBAR();
MMA(z27,A[7],B1[6]); SBAR(); SHX(); SBAR();
MMA(z26,A[6],B0[7]); SBAR(); SHR2(t1_24,t2_24,z24[1],t1_25,t2_25,z25[1]); SBAR();
MMA(z27,A[6],B1[7]); SBAR(); FMD(z24,xa_); FMD(z25,xb_); SBAR();
MMA(z26,A[5],B0[8]); SBAR(); ROR2(u_24,t1_23,z24[0],u_25,t1_24,z25[0]); SBAR();
MMA(z27,A[5],B1[8]); SBAR(); LDX(13); SBAR();
MMA(z26,A[4],B0[9]); SBAR(); SHR2(u_24,t1_24,z24[0],u_25,t1_25,z25[0]); SBAR();
MMA(z27,A[4],B1[9]); SBAR(); yw[0]=pk(u_24,u_25); PIN(yw); SBAR();
MMA(z26,A[3],B0[10]); SBAR();  SBAR();
MMA(z27,A[3],B1[10]); SBAR();  SBAR();
MMA(z26,A[2],B0[11]); SBAR();  SBAR();
MMA(z27,A[2],B1[11]); SBAR();  SBAR();
HOOK1(); SBAR();
MMA(z26,A[1],B0[12]); SBAR();  SBAR();
MMA(z27,A[1],B1[12]); SBAR();  SBAR();
MMA(z26,A[0],B0[13]); SBAR();  SBAR();
MMA(z27,A[0],B1[13]); SBAR();  SBAR();
MM0(z28,A[14],B0[0]); SBAR();  SBAR();
MM0(z29,A[14],B1[0]); SBAR();  SBAR();
MMA(z28,A[13],B0[1]); SBAR();  SBAR();
MMA(z29,A[13],B1[1]); SBAR();  SBAR();
MMA(z28,A[12],B0[2]); SBAR();  SBAR();
MMA(z29,A[12],B1[2]); SBAR();  SBAR();
MMA(z28,A[11],B0[3]); SBAR();  SBAR();
MMA(z29,A[11],B1[3]); SBAR();  SBAR();
MMA(z28,A[10],B0[4]); SBAR(); ROR2(t2_26,z25[3],z26[2],t2_27,z26[3],z27[2]); SBAR();
MMA(z29,A[10],B1[4]); SBAR(); VNOP(); SBAR();
MMA(z28,A[9],B0[5]); SBAR(); SHR2(t2_26,z26[3],z26[2],t2_27,z27[3],z27[2]); SBAR();
MMA(z29,A[9],B1[5]); SBAR(); VNOP(); SBAR();
MMA(z28,A[8],B0[6]); SBAR(); ROR2(t1_26,t2_25,z26[1],t1_27,t2_26,z27[1]); SBAR();
MMA(z29,A[8],B1[6]); SBAR(); SHX(); SBAR();
MMA(z28,A[7],B0[7]); SBAR(); SHR2(t1_26,t2_26,z26[1],t1_27,t2_27,z27[1]); SBAR();
MMA(z29,A[7],B1[7]); SBAR(); FMD(z26,xa_); FMD(z27,xb_); SBAR();
MMA(z28,A[6],B0[8]); SBAR(); ROR2(u_26,t1_25,z26[0],u_27,t1_26,z27[0]); SBAR();
MMA(z29,A[6],B1[8]); SBAR(); LDX(14); SBAR();
MMA(z28,A[5],B0[9]); SBAR(); SHR2(u_26,t1_26,z26[0],u_27,t1_27,z27[0]); SBAR();
MMA(z29,A[5],B1[9]); SBAR(); yw[1]=pk(u_26,u_27); PIN(yw); SBAR();
MMA(z28,A[4],B0[10]); SBAR();  SBAR();
MMA(z29,A[4],B1[10]); SBAR();  SBAR();
MMA(z28,A[3],B0[11]); SBAR();  SBAR();
MMA(z29,A[3],B1[11]); SBAR();  SBAR();
MMA(z28,A[2],B0[12]); SBAR();  SBAR();
MMA(z29,A[2],B1[12]); SBAR();  SBAR();
MMA(z28,A[1],B0[13]); SBAR();  SBAR();
MMA(z29,A[1],B1[13]); SBAR();  SBAR();
MMA(z28,A[0],B0[14]); SBAR();  SBAR();
MMA(z29,A[0],B1[14]); SBAR();  SBAR();
MM0(z30,A[15],B0[0]); SBAR();  SBAR();
MM0(z31,A[15],B1[0]); SBAR();  SBAR();
MMA(z30,A[14],B0[1]); SBAR();  SBAR();
MMA(z31,A[14],B1[1]); SBAR();  SBAR();
MMA(z30,A[13],B0[2]); SBAR();  SBAR();
MMA(z31,A[13],B1[2]); SBAR();  SBAR();
MMA(z30,A[12],B0[3]); SBAR();  SBAR();
MMA(z31,A[12],B1[3]); SBAR();  SBAR();
MMA(z30,A[11],B0[4]); SBAR(); ROR2(t2_28,z27[3],z28[2],t2_29,z28[3],z29[2]); SBAR();
MMA(z31,A[11],B1[4]); SBAR(); VNOP(); SBAR();
MMA(z30,A[10],B0[5]); SBAR(); SHR2(t2_28,z28[3],z28[2],t2_29,z29[3],z29[2]); SBAR();
HOOK2(); SBAR();
MMA(z31,A[10],B1[5]); SBAR(); VNOP(); SBAR();
MMA(z30,A[9],B0[6]); SBAR(); ROR2(t1_28,t2_27,z28[1],t1_29,t2_28,z29[1]); SBAR();
MMA(z31,A[9],B1[6]); SBAR(); SHX(); SBAR();
MMA(z30,A[8],B0[7]); SBAR(); SHR2(t1_28,t2_28,z28[1],t1_29,t2_29,z29[1]); SBAR();
MMA(z31,A[8],B1[7]); SBAR(); FMD(z28,xa_); FMD(z29,xb_); SBAR();
MMA(z30,A[7],B0[8]); SBAR(); ROR2(u_28,t1_27,z28[0],u_29,t1_28,z29[0]); SBAR();
MMA(z31,A[7],B1[8]); SBAR(); LDX(15); SBAR();
MMA(z30,A[6],B0[9]); SBAR(); SHR2(u_28,t1_28,z28[0],u_29,t1_29,z29[0]); SBAR();
MMA(z31,A[6],B1[9]); SBAR(); yw[2]=pk(u_28,u_29); PIN(yw); SBAR();
MMA(z30,A[5],B0[10]); SBAR();  SBAR();
MMA(z31,A[5],B1[10]); SBAR();  SBAR();
MMA(z30,A[4],B0[11]); SBAR();  SBAR();
MMA(z31,A[4],B1[11]); SBAR();  SBAR();
MMA(z30,A[3],B0[12]); SBAR();  SBAR();
MMA(z31,A[3],B1[12]); SBAR();  SBAR();
MMA(z30,A[2],B0[13]); SBAR();  SBAR();
MMA(z31,A[2],B1[13]); SBAR();  SBAR();
MMA(z30,A[1],B0[14]); SBAR();  SBAR();
MMA(z31,A[1],B1[14]); SBAR();  SBAR();
MMA(z30,A[0],B0[15]); SBAR();  SBAR();
MMA(z31,A[0],B1[15]); SBAR();  SBAR();
NOP1(); SBAR();  SBAR();
NOP1(); SBAR();  SBAR();
NOP1(); SBAR();  SBAR();
NOP1(); SBAR();  SBAR();
NOP1(); SBAR();  SBAR();
NOP1(); SBAR();  SBAR();
NOP1(); SBAR();  SBAR();
NOP1(); SBAR();  SBAR();
NOP1(); SBAR(); ROR2(t2_30,z29[3],z30[2],t2_31,z30[3],z31[2]); SBAR();
NOP1(); SBAR(); VNOP(); SBAR();
NOP1(); SBAR(); SHR2(t2_30,z30[3],z30[2],t2_31,z31[3],z31[2]); SBAR();
NOP1(); SBAR(); VNOP(); SBAR();
NOP1(); SBAR(); ROR2(t1_30,t2_29,z30[1],t1_31,t2_30,z31[1]); SBAR();
NOP1(); SBAR(); SHX(); SBAR();
NOP1(); SBAR(); SHR2(t1_30,t2_30,z30[1],t1_31,t2_31,z31[1]); SBAR();
NOP1(); SBAR(); FMD(z30,xa_); FMD(z31,xb_); SBAR();
NOP1(); SBAR(); ROR2(u_30,t1_29,z30[0],u_31,t1_30,z31[0]); SBAR();
NOP1(); SBAR(); VNOP(); SBAR();
NOP1(); SBAR(); SHR2(u_30,t1_30,z30[0],u_31,t1_31,z31[0]); SBAR();
NOP1(); SBAR(); yw[3]=pk(u_30,u_31); PIN(yw); STY(3); SBAR();
      ST(5);
    }
    if(!h1){
      wait_flags<4>(kvflag(ws,R,C,v>>1,1,lane),nonce);
      lds_wait(cnt+C_KV1,2);
      kv_fetch<false>(ws,lds0,R,C,v>>1,1,v&1,lane);
    }
    WAITV(0);
    arrive_publish(cnt+C_H1,4,flagp(ws,b,FK_Y),nonce,lane);
    lds_wait(cnt+C_H1,4); lds_wait(cnt+C_P1E,4);
#pragma unroll 1
    for(int i=v;i<WO_P1;i+=4) wo_block(i);
    if(v<2) touch((unsigned)((512*(j>>3)+8*(j&7)+64*(lane>>3)+(lane&7))*2048+(xcd*2+v)*128),Aq,junk);
    ST(6);
  } else {
    const int wr=wid>>1, wc=wid&1;
    const int r=lane&31, h=lane>>5;
    lptr L=(lptr)lds;
    lptr xa[4];
    { int row=wr*64+r; int sw=(row>>1)&7;
#pragma unroll
      for(int ks=0;ks<4;ks++) xa[ks]=L+row*128+(((2*ks+h)^sw)*16); }
    lptr wa;
    { int blk=(lane>>4)&1,q=(lane&15)>>2,p=lane&3; int c=(wc*4+2*blk+(p>>1))^(((q>>1)&1)<<2); wa=L+16384+(8*h+q)*128+c*16+8*(p&1); }
    f32x16 aq[2],ak[2],av[2];
#pragma unroll
    for(int i=0;i<2;i++){ aq[i]=f32x16{}; ak[i]=f32x16{}; av[i]=f32x16{}; }
    G1F F0,F1;
#define G1_RX(F,ks) do{ F.x0=ldsr128(xa[ks]+so); F.x1=ldsr128(xa[ks]+so+4096); }while(0)
#define G1_RW(F,ks,sg) do{ F.w[2*(sg)]=ldstr(wa+so+(sg)*8192+(16*(ks))*128); F.w[2*(sg)+1]=ldstr(wa+so+(sg)*8192+(16*(ks)+4)*128); }while(0)
#define G1_W(F,sg) cat(F.w[2*(sg)],F.w[2*(sg)+1])
#define G1_STEP(F,N,ks,LD) do{ \
    ak[0]=MFMA32(G1_W(F,1),F.x0,ak[0]); PIN(ak[0]); if(LD) G1_RX(N,ks); SBAR(); \
    ak[1]=MFMA32(G1_W(F,1),F.x1,ak[1]); PIN(ak[1]); if(LD) G1_RW(N,ks,1); SBAR(); \
    av[0]=MFMA32(F.x0,G1_W(F,2),av[0]); PIN(av[0]); if(LD) G1_RW(N,ks,2); SBAR(); \
    av[1]=MFMA32(F.x1,G1_W(F,2),av[1]); PIN(av[1]); if(LD) G1_RW(N,ks,0); SBAR(); \
    aq[0]=MFMA32(G1_W(F,0),F.x0,aq[0]); PIN(aq[0]); SBAR(); \
    aq[1]=MFMA32(G1_W(F,0),F.x1,aq[1]); PIN(aq[1]); SBAR(); }while(0)
    __builtin_amdgcn_s_setprio(2);
    asm volatile("":"+v"(aq[0]),"+v"(aq[1]),"+v"(ak[0]),"+v"(ak[1]),"+v"(av[0]),"+v"(av[1]));
    LOAD_NONCE();
    BAR();
    ST(1);
    int so=0;
    G1_RX(F0,0); G1_RW(F0,0,1); G1_RW(F0,0,2); G1_RW(F0,0,0); SBAR();
#pragma unroll 1
    for(int t=0;t<15;t++){
      G1_STEP(F0,F1,1,true);
      G1_STEP(F1,F0,2,true);
      G1_STEP(F0,F1,3,true);
      so=(so==2*G1_SLOT)?0:so+G1_SLOT;
      WAITL(); BAR(); SBAR();
      G1_STEP(F1,F0,0,true);
    }
    G1_STEP(F0,F1,1,true);
    G1_STEP(F1,F0,2,true);
    G1_STEP(F0,F1,3,true);
    arrive(cnt+C_RD);
    SBAR();
    G1_STEP(F1,F0,0,false);
    ST(2);
    const float qs=0.17677669529663687f*1.4426950408889634f;
    u32x4 kf[2][2],vf[2][2];
    const unsigned go=WS_KV+(unsigned)b*32768u+wc*16384u+(2*wr)*2048u+lane*16u;
#define KV_STORE() _Pragma("unroll") for(int rt=0;rt<2;rt++) _Pragma("unroll") for(int s=0;s<2;s++) __builtin_amdgcn_raw_buffer_store_b128(kf[rt][s],wsr,go+rt*2048+s*1024,0,SC1); \
      _Pragma("unroll") for(int rt=0;rt<2;rt++) _Pragma("unroll") for(int s=0;s<2;s++) __builtin_amdgcn_raw_buffer_store_b128(vf[rt][s],wsr,go+8192+rt*2048+s*1024,0,SC1)
#pragma unroll
    for(int rt=0;rt<2;rt++)
#pragma unroll
      for(int s=0;s<2;s++){ kf[rt][s]=pack8(ak[rt],s); vf[rt][s]=pack8(av[rt],s); }
    if(wc==0){ KV_STORE(); }
    lds_wait(cnt+C_RD,4);
#pragma unroll
    for(int rt=0;rt<2;rt++){
      lwptr lq=(lwptr)lds+(1-wc)*HR+49152+(2*wr+rt)*2048+lane*16;
#pragma unroll
      for(int s=0;s<2;s++) *(LAS u32x4*)(lq+s*1024)=pack8(aq[rt],s,qs);
    }
    if(R<2){
#pragma unroll 1
      for(int i=0;i<8*(2-R);i++) *(LAS u32x4*)((lwptr)lds+(1-wc)*HR+wr*24576+i*1024+lane*16)=u32x4{0,0,0,0};
    }
    if(wc==1){ lds_wait(cnt+C_KV0,2); KV_STORE(); }
    ST(3);
    WAITV(0);
    arrive_publish(cnt+C_KV0+wc,2,flagp(ws,b,FK_KV0+wc),nonce,lane);
    ST(4);
    if(wid==0){
      lds_wait(cnt+C_KV0,2);
      const int kind=(lane>>2)&1, ri=lane&3;
      const gu64* fp=kvflag(ws,R,C,kind,0,ri);
      unsigned todo=(R>=2)?0x77u:((R==1)?0x66u:0x44u);
#pragma unroll 1
      for(unsigned sp=0;sp<SPIN_CAP && todo;sp++){
        u64 fv=0;
        if(lane<8 && ((todo>>lane)&1)) fv=__hip_atomic_load(fp,RLX_AGENT);
        unsigned rdy=(unsigned)__builtin_amdgcn_ballot_w64(fv==nonce)&todo;
        MEMFENCE();
        todo&=~rdy;
        if(!rdy) __builtin_amdgcn_s_sleep(2);
#pragma unroll 1
        while(rdy){
          const int bit=__builtin_ctz(rdy); rdy&=rdy-1;
          const int kd=bit>>2, i=bit&3;
          const char* src=ws+WS_KV+(size_t)blk_of(R-2+i,(C&12)|((C-1-kd)&3))*32768+kd*8192;
          const unsigned dst=lds0+HR+(1-kd)*24576+i*8192;
          dma4_sc1(lane*16,src,dst); dma4_sc1(lane*16,src+4096,dst+4096);
        }
      }
      WAITV(0);
      arrive(cnt+C_H0K);
    } else lds_wait(cnt+C_H0K,1);
    __builtin_amdgcn_s_setprio(0);
    ST(6);
  }
  const int late=wid>>2;
  if(R>=2) attention<false>(lds,cnt,ws,wsr,b,R,C,wid,lane,nonce STPASS);
  else     attention<true >(lds,cnt,ws,wsr,b,R,C,wid,lane,nonce STPASS);
  ST(7);
  if(!late){
    WAITV(0);
    arrive_publish(cnt+C_O0,4,flagp(ws,b,FK_O0),nonce,lane);
    ST(8);
    if(wid==3){
      o_touch(ws,lds0+L_JUNK,R2,j,0,lane,nonce);
      ST(1);
      wait_flags(flagp(ws,blk_of(R2,lane&15),FK_O0),nonce);
      arrive(cnt+C_EF);
    } else { lds_wait(cnt+C_E,4); lds_wait(cnt+C_P1L,4);
#pragma unroll 1
      for(int i=WO_P1+wid;i<32;i+=3) wo_block(i);
      ST(1);
      lds_wait(cnt+C_EF,1); }
    ST(9);
    const int T2=R2*4+wid;
    const unsigned ob=WS_O+(unsigned)T2*65536u+lane*16u;
    u32x4 of[8][2];
#define G2_LDO(i,n) do{ const unsigned so_=(unsigned)((((n)+KO)&15)*4096); of[i][0]=__builtin_amdgcn_raw_buffer_load_b128(wsr,obv,so_,OAUX); of[i][1]=__builtin_amdgcn_raw_buffer_load_b128(wsr,obv+1024,so_,OAUX); }while(0)
    unsigned obv=ob;
#pragma unroll
    for(int i=0;i<8;i++){ G2_LDO(i,i); if(i==RAMP){ WAITV(0); } }
    const int h=lane>>5,blk=(lane>>4)&1,q=(lane&15)>>2,p=lane&3;
    lptr wa[2];
#pragma unroll
    for(int ct=0;ct<2;ct++){ int c=(ct*4+2*blk+(p>>1))^(((q>>1)&1)<<2); wa[ct]=(lptr)lds+L_WO+23*4096+(4*h+q)*128+c*16+8*(p&1); }
    f32x16 acc[2]; acc[0]=f32x16{}; acc[1]=f32x16{};
    s16x4 wf[2][8];
#define G2_LDW(i) do{ _Pragma("unroll") for(int s_=0;s_<2;s_++) _Pragma("unroll") for(int ct_=0;ct_<2;ct_++){ lptr a_=wa[ct_]+(8-(i))*4096+s_*2048; \
      wf[(i)&1][4*s_+2*ct_]=ldstr(a_); wf[(i)&1][4*s_+2*ct_+1]=ldstr(a_+1024); } }while(0)
    lds_wait(cnt+C_W1,4);
    G2_LDW(0);
    u64 f1=0;
#pragma unroll 1
    for(int it=0;it<4;it++){
      if(it==1){
        ST(11);
        arrive(cnt+C_W2);
        if(wid==3){ if(!allof(f1==nonce)) wait_flags(flagp(ws,blk_of(R2,lane&15),FK_O1),nonce); MEMFENCE(); arrive(cnt+C_LF); }
        else lds_wait(cnt+C_LF,1);
        __builtin_amdgcn_s_setprio(2);
        ST(5);
      }
      if(it==2) lds_wait(cnt+C_W2,4);
      PIN(wa[0]); PIN(wa[1]);
      const int nb=8*it+8;
      obv=ob+((it==0)?0u:((it==3)?0x40000000u:2048u)); PIN(obv);
#pragma unroll
      for(int i=0;i<8;i++){
        SBAR();
        if(i==4){ if(wid==3 && it==0) f1=__hip_atomic_load(flagp(ws,blk_of(R2,lane&15),FK_O1),RLX_AGENT); }
        G2_LDW(i+1);
#pragma unroll
        for(int s=0;s<2;s++)
#pragma unroll
          for(int ct=0;ct<2;ct++) acc[ct]=MFMA32(cat(wf[i&1][4*s+2*ct],wf[i&1][4*s+2*ct+1]),B8(of[i][s]),acc[ct]);
#pragma unroll
        for(int g_=0;g_<4;g_++){ __builtin_amdgcn_sched_group_barrier(0x100,2,0); __builtin_amdgcn_sched_group_barrier(0x8,1,0); }
        PIN(acc[0]); PIN(acc[1]);
        SBAR();
        G2_LDO(i,nb+i);
      }
      wa[0]-=32768; wa[1]-=32768;
    }
    ST(10);
    lwptr stg=(lwptr)lds+L_WO+(31-wid)*4096;
#pragma unroll
    for(int ct=0;ct<2;ct++){
#pragma unroll
      for(int gp=0;gp<2;gp++){
        unsigned a0=pk(acc[ct][8*gp],acc[ct][8*gp+1]), a1=pk(acc[ct][8*gp+2],acc[ct][8*gp+3]);
        unsigned b0=pk(acc[ct][8*gp+4],acc[ct][8*gp+5]), b1=pk(acc[ct][8*gp+6],acc[ct][8*gp+7]);
        auto r0=__builtin_amdgcn_permlane32_swap(a0,b0,false,false); auto r1=__builtin_amdgcn_permlane32_swap(a1,b1,false,false);
        u32x4 vv={r0[0],r1[0],r0[1],r1[1]};
        *(LAS u32x4*)(stg+(lane&31)*128+(((ct*4+gp*2+h)^(lane&7))*16))=vv;
      }
    }
#pragma unroll
    for(int i=0;i<4;i++){
      const int row=8*i+(lane>>3);
      const u32x4 vv=*(const LAS u32x4*)(stg+row*128+(((lane&7)^(row&7))*16));
      __builtin_amdgcn_raw_buffer_store_b128(vv,outr,(unsigned)(T2*32+row)*4096u+2048u+C2*128u+(lane&7)*16u,0,SC1);
    }
  } else {
    const int v=wid-4;
    const int chb=xcd*128+(v&1)*64, rb=512*(j>>3)+8*(j&7)+4*(v>>1)+64*(lane>>3);
    u32x4 qq[4];
    const bool yr=__builtin_amdgcn_readfirstlane(*(volatile LAS unsigned*)(cnt+C_YF))!=0;
    MEMFENCE();
#pragma unroll
    for(int i=0;i<4;i++) qq[i]=*(const u32x4*)(Aq+(size_t)(rb+i)*1024+chb+(lane&7)*8);
    WAITV(4);
    arrive_publish(cnt+C_O1,4,flagp(ws,b,FK_O1),nonce,lane);
    ST(8);
    __builtin_amdgcn_s_setprio(0);
    lds_wait(cnt+C_L,4);
    const unsigned yo=(unsigned)((lane>>2)*4096+(lane&3)*16);
    if(!yr) wait_flags<8>(flagp(ws,(lane&31)*8+xcd,FK_Y),nonce);
    dma4v_sc1(yo,yo+(65536-1024),yo+2*(65536-1024),yo+3*(65536-1024),ws+WS_Y+(size_t)chb*4096+j*128+(v>>1)*64,lds0+v*4096);
    ST(9);
    if(v==0) o_touch(ws,lds0+L_JUNK,R2,j,1,lane,nonce);
    WAITV(0);
    ST(10);
    lwptr stg=(lwptr)lds+v*4096;
    { const int g=lane>>4,q=(lane&15)>>2,p=lane&3;
      bf16x8 tt[4];
#pragma unroll
      for(int n=0;n<4;n++){ lptr a=(lptr)stg+(8*(2*n+(g>>1))+q)*64+(g&1)*32+8*p; tt[n]=cat(ldstr(a),ldstr(a+256)); }
      MEMFENCE();
      const int srow=16*(g&1)+(lane&15);
#pragma unroll
      for(int n=0;n<4;n++) *(LAS bf16x8*)(stg+srow*128+(((2*n+(g>>1))^(srow&7))*16))=tt[n];
    }
#pragma unroll
    for(int i=0;i<4;i++){
      const int row=8*i+(lane>>3);
      const u32x4 yy=*(const LAS u32x4*)(stg+row*128+(((lane&7)^(row&7))*16));
      u32x4 r;
#pragma unroll
      for(int d=0;d<4;d++) r[d]=pk(lo16(qq[i][d])*lo16(yy[d]),hi16(qq[i][d])*hi16(yy[d]));
      __builtin_amdgcn_raw_buffer_store_b128(r,outr,(unsigned)(rb+i)*4096u+chb*2u+(lane&7)*16u,0,SC1);
    }
    if(b==0 && wid==7){
#pragma unroll 1
      for(int i=0;i<4;i++) wait_flags<16>(flagp(ws,i*64+lane,FK_O1),nonce);
      if(lane==0) __hip_atomic_store((gu64*)(ws+F_E),nonce,RLX_AGENT);
    }
  }
  STAMP_END(ws);
}

extern "C" void kernel_launch(void* const* d_in, const int* in_sizes, int n_in, void* d_out, int out_size, void* d_ws, size_t ws_size, hipStream_t stream){
  (void)hipFuncSetAttribute((const void*)fused,hipFuncAttributeMaxDynamicSharedMemorySize,LDS_BYTES);
  fused<<<256,512,LDS_BYTES,stream>>>((const u16*)d_in[0],(const u16*)d_in[1],(const u16*)d_in[2],(const u16*)d_in[3],(const u16*)d_in[4],(const u16*)d_in[5],
                                      (const u16*)d_in[6],(const u16*)d_in[7],(const u16*)d_in[8],(u16*)d_out,(char*)d_ws);
}
```

```cpp
#include <hip/hip_runtime.h>
#include <hip/hip_bf16.h>
#include <stdint.h>
#if defined(__HIP_DEVICE_COMPILE__)
#define NOPK __attribute__((target("no-packed-fp32-ops")))
#else
#define NOPK
#endif
typedef unsigned short u16;
typedef unsigned long long u64;
typedef short bf16x8 __attribute__((ext_vector_type(8)));
typedef short s16x4 __attribute__((ext_vector_type(4)));
typedef float f32x16 __attribute__((ext_vector_type(16)));
typedef float f32x4 __attribute__((ext_vector_type(4)));
typedef float f32x2 __attribute__((ext_vector_type(2)));
typedef __bf16 bf2 __attribute__((ext_vector_type(2)));
typedef unsigned u32x4 __attribute__((ext_vector_type(4)));
typedef unsigned u32x2 __attribute__((ext_vector_type(2)));
#define LAS __attribute__((address_space(3)))
#define GAS __attribute__((address_space(1)))
typedef LAS const char* lptr;
typedef LAS char* lwptr;
typedef GAS u64 gu64;
#define DI __device__ __forceinline__ NOPK
#define MFMA32(a,b,c) __builtin_amdgcn_mfma_f32_32x32x16_bf16((a),(b),(c),0,0,0)
#define MFMA16(a,b,c) __builtin_amdgcn_mfma_f32_16x16x32_bf16((a),(b),(c),0,0,0)
#define SBAR() __builtin_amdgcn_sched_barrier(0)
#define PIN(x) asm volatile("":"+v"(x))
#define MEMFENCE() asm volatile("":::"memory")
#define BAR() asm volatile("s_barrier":::"memory")
#define WAITV(n) asm volatile("s_waitcnt vmcnt(" #n ")":::"memory")
#define WAITL() asm volatile("s_waitcnt lgkmcnt(0)":::"memory")
#define RLX_AGENT __ATOMIC_RELAXED, __HIP_MEMORY_SCOPE_AGENT
#define RLX_WG __ATOMIC_RELAXED, __HIP_MEMORY_SCOPE_WORKGROUP
#define SC1 16
#define XT_DIRECT
#define RAMP 99
#define NT 0
#define OAUX 16
#define B8(x) __builtin_bit_cast(bf16x8,(x))

#define PREPSZ (4*MB)
constexpr unsigned MB=1u<<20;
constexpr unsigned WS_KV=0, WS_O=8*MB, WS_XT=12*MB, WS_Y=16*MB, WS_F=20*MB, WS_END=21*MB, WS_ST=24*MB;
constexpr unsigned F_E=WS_F, F_W=WS_F+4096;
enum { FK_KV0=0, FK_KV1, FK_O0, FK_O1, FK_X, FK_Y };
constexpr int G1_SLOT=40960;
constexpr int HR=57344;
constexpr int L_CV=126976, CV_W=8480, CV_H=128, CV_X=128+4096+160;
constexpr int L_MISC=L_CV+4*CV_W;
constexpr int L_WO=L_MISC-131072;
constexpr int WO_P1=19;
static_assert(L_WO+(32-WO_P1)*4096>=HR+24576 && L_WO>=24576,"wo image");
constexpr int L_CNT=L_MISC;
constexpr int L_JUNK=L_CNT+128;
constexpr int L_MASK=L_JUNK+256;
constexpr int LDS_BYTES=L_MASK+2048;
static_assert(LDS_BYTES<=163840,"lds");
enum { C_RD=0, C_KV0=6, C_KV1, C_H0, C_H1, C_CV, C_E, C_L, C_W1, C_W2, C_W3, C_O0, C_O1, C_EF, C_LF, C_Y, C_X, C_XF, C_XR, C_YF, C_G2, C_HI, C_H0K=1, C_H0V=2, C_P1E=3, C_P1L=4 };
#define PREP_BAR(c) ((c)==1||(c)==3||(c)==4||(c)==6||(c)==8)
#define WSOFF 0
#define LP1 0
#define KV_G 2
#define KVA 0
#define KVG 1
#define NPB 3
#define TMASK 0xf
#define TI(i) ((TMASK>>(i))&1)
#define PB(c) (NPB==2? ((c)&1) : ((c)!=1))
#define G2D 8
#define KV_PER 11
#define PFLAG ""
#define STAG 0
#define PREP0 4
#define CONV_START 30
#define CODE_LINES 256
constexpr unsigned SPIN_CAP=1u<<16, LSPIN_CAP=1u<<20;

DI unsigned pk(float a,float b){ f32x2 v={a,b}; bf2 r=__builtin_convertvector(v,bf2); return __builtin_bit_cast(unsigned,r); }
DI float asf(unsigned u){ return __builtin_bit_cast(float,u); }
DI unsigned asu(float f){ return __builtin_bit_cast(unsigned,f); }
DI bool allof(bool p){ return __builtin_amdgcn_ballot_w64(p)==~0ull; }
DI float lo16(unsigned u){ return asf(u<<16); }
DI float hi16(unsigned u){ return asf(u&0xffff0000u); }
DI bf16x8 ldsr128(lptr p){ return *(const LAS bf16x8*)p; }
DI s16x4 ldstr(lptr p){ return __builtin_amdgcn_ds_read_tr16_b64_v4i16((LAS s16x4*)p); }
DI bf16x8 cat(s16x4 a,s16x4 b){ return __builtin_shufflevector(a,b,0,1,2,3,4,5,6,7); }
DI u32x4 pack8(const f32x16& x,int s,float sc){ u32x4 r; r[0]=pk(x[8*s]*sc,x[8*s+1]*sc); r[1]=pk(x[8*s+2]*sc,x[8*s+3]*sc); r[2]=pk(x[8*s+4]*sc,x[8*s+5]*sc); r[3]=pk(x[8*s+6]*sc,x[8*s+7]*sc); return r; }
DI u32x4 pack8(const f32x16& x,int s){ u32x4 r; r[0]=pk(x[8*s],x[8*s+1]); r[1]=pk(x[8*s+2],x[8*s+3]); r[2]=pk(x[8*s+4],x[8*s+5]); r[3]=pk(x[8*s+6],x[8*s+7]); return r; }
#define DMA4_BODY(FL) "s_mov_b32 m0, %2\n\ts_nop 0\n\t" \
   "global_load_lds_dwordx4 %0, %1" FL "\n\tglobal_load_lds_dwordx4 %0, %1 offset:1024" FL "\n\tglobal_load_lds_dwordx4 %0, %1 offset:2048" FL "\n\tglobal_load_lds_dwordx4 %0, %1 offset:3072" FL
DI void dma4(unsigned voff,const void* sbase,unsigned ldsaddr){ asm volatile(DMA4_BODY(" nt") :: "v"(voff), "s"(sbase), "s"(ldsaddr) : "memory"); }
DI void dma4_sc1(unsigned voff,const void* sbase,unsigned ldsaddr){ asm volatile(DMA4_BODY(" sc1") :: "v"(voff), "s"(sbase), "s"(ldsaddr) : "memory"); }
DI void dma4v(unsigned v0,unsigned v1,unsigned v2,unsigned v3,const void* sbase,unsigned ldsaddr){
  asm volatile("s_mov_b32 m0, %5\n\ts_nop 0\n\t"
    "global_load_lds_dwordx4 %0, %4\n\tglobal_load_lds_dwordx4 %1, %4 offset:1024\n\tglobal_load_lds_dwordx4 %2, %4 offset:2048\n\tglobal_load_lds_dwordx4 %3, %4 offset:3072"
    :: "v"(v0),"v"(v1),"v"(v2),"v"(v3), "s"(sbase), "s"(ldsaddr) : "memory");
}
DI void dma4v_sc1(unsigned v0,unsigned v1,unsigned v2,unsigned v3,const void* sbase,unsigned ldsaddr){
  asm volatile("s_mov_b32 m0, %5\n\ts_nop 0\n\t"
    "global_load_lds_dwordx4 %0, %4 sc1\n\tglobal_load_lds_dwordx4 %1, %4 offset:1024 sc1\n\tglobal_load_lds_dwordx4 %2, %4 offset:2048 sc1\n\tglobal_load_lds_dwordx4 %3, %4 offset:3072 sc1"
    :: "v"(v0),"v"(v1),"v"(v2),"v"(v3), "s"(sbase), "s"(ldsaddr) : "memory");
}
DI void touch(unsigned voff,const void* sbase,unsigned junk){
  asm volatile("s_mov_b32 m0, %2\n\ts_nop 0\n\tglobal_load_lds_dword %0, %1" :: "v"(voff), "s"(sbase), "s"(junk) : "memory");
}
DI void touch64(const void* a,unsigned junk){
  asm volatile("s_mov_b32 m0, %1\n\ts_nop 0\n\tglobal_load_lds_dword %0, off" :: "v"(a), "s"(junk) : "memory");
}
DI bool flags_ready(const gu64* p,u64 nonce){ u64 v=__hip_atomic_load(p,RLX_AGENT); bool r=allof(v==nonce); MEMFENCE(); return r; }
template<int SL=2>
DI void wait_flags(const gu64* p,u64 nonce){
#pragma unroll 1
  for(unsigned sp=0;sp<SPIN_CAP;sp++){
    u64 v=__hip_atomic_load(p,RLX_AGENT);
    if(allof(v==nonce)) break;
    __builtin_amdgcn_s_sleep(SL);
  }
  MEMFENCE();
}
DI void lds_wait(LAS unsigned* p,unsigned n){
#pragma unroll 1
  for(unsigned sp=0;sp<LSPIN_CAP;sp++){
    unsigned v=__hip_atomic_load(p,RLX_WG);
    if(__builtin_amdgcn_readfirstlane(v)>=n) break;
    __builtin_amdgcn_s_sleep(1);
  }
  MEMFENCE();
}
DI void arrive(LAS unsigned* cnt){
  unsigned one=1u;
  asm volatile("s_mov_b64 exec, 1\n\tds_add_u32 %0, %1\n\ts_mov_b64 exec, -1" :: "v"(cnt), "v"(one) : "memory");
}
DI void arrive_publish(LAS unsigned* cnt,unsigned n,gu64* flag,u64 nonce,int lane){
  unsigned one=1u, old;
  asm volatile("s_mov_b64 exec, 1\n\tds_add_rtn_u32 %0, %1, %2\n\ts_mov_b64 exec, -1\n\ts_waitcnt lgkmcnt(0)" : "=&v"(old) : "v"(cnt), "v"(one) : "memory");
  if(__builtin_amdgcn_readfirstlane(old)==n-1){ if(lane==0) __hip_atomic_store(flag,nonce,RLX_AGENT); }
  MEMFENCE();
}
DI gu64* flagp(char* ws,int blk,int k){ return (gu64*)(ws+F_W+(unsigned)(blk*8+k)*128u); }
DI int blk_of(int R,int C){ return (((R&7)*4+(C&3))<<3)|((R>>3)*4+(C>>2)); }
DI float fmax3(float a,float b,float c){ return __builtin_fmaxf(__builtin_fmaxf(a,b),c); }
DI float xhalf_max(float m){ auto rr=__builtin_amdgcn_permlane32_swap(asu(m),asu(m),false,false); return __builtin_fmaxf(asf(rr[0]),asf(rr[1])); }
struct G1F { bf16x8 x0,x1; s16x4 w[6]; };

DI const gu64* kvflag(char* ws,int R,int C,int kind,int head,int lane){
  const int Cs=(C&12)|((C-1-kind)&3); const int rr=(lane<3)? lane : 2; const int Rl=(R-2+rr<0)? R : R-2+rr;
  return flagp(ws,blk_of(Rl,Cs),FK_KV0+head);
}
template<bool BOTH>
DI void kv_fetch(char* ws,unsigned lds0,int R,int C,int kind,int head,int half,int lane){
  const int Cs=(C&12)|((C-1-kind)&3);
#pragma unroll 1
  for(int i=(R<2)?2-R:0;i<3;i++){
    const char* src=ws+WS_KV+(size_t)blk_of(R-2+i,Cs)*32768+head*16384+kind*8192+half*4096;
    const unsigned dst=lds0+(1-head)*HR+(1-kind)*24576+i*8192+half*4096;
    dma4_sc1(lane*16,src,dst); if(BOTH) dma4_sc1(lane*16,src+4096,dst+4096);
  }
}
DI void o_touch(char* ws,unsigned junk,int R2,int j,int head,int lane,u64 nonce){
  wait_flags<4>(flagp(ws,blk_of(R2,2*(j&7)+(lane&1)),FK_O0+head),nonce);
#pragma unroll
  for(int k=0;k<2;k++){
    const unsigned off=WS_O+(unsigned)(R2*4+(lane>>4))*65536u+(unsigned)((2*(j&7)+k)*4096+head*2048+(lane&15)*128);
    asm volatile("s_mov_b32 m0, %2\n\ts_nop 0\n\tglobal_load_lds_dword %0, %1 sc1" :: "v"(off), "s"(ws), "s"(junk) : "memory");
  }
}
template<bool CHECK>
DI void attention(char* lds,LAS unsigned* cnt,char* ws,__amdgpu_buffer_rsrc_t wsr,int b,int R,int C,int wid,int lane,u64 nonce){
  const int late=wid>>2, jq=wid&3;
  const int T=4*R+jq;
  lptr VL=(lptr)lds+(1-late)*HR+jq*2048+lane*16;
  lptr KL=VL+24576;
  lptr QL=(lptr)lds+(1-late)*HR+49152+jq*2048+lane*16;
  const f32x16 zero=f32x16{};
  const float ninf=-__builtin_inff();
  const int dl=(lane&31)-4*(lane>>5);
  bf16x8 kf[9][2];
  const bf16x8 q0=ldsr128(QL), q1=ldsr128(QL+1024);
  kf[8][0]=ldsr128(KL+8*2048); kf[8][1]=ldsr128(KL+8*2048+1024);
#pragma unroll
  for(int u=0;u<4;u++){ kf[u][0]=ldsr128(KL+u*2048); kf[u][1]=ldsr128(KL+u*2048+1024); }
  f32x16 sd,sm,s[3];
  float m;
#pragma unroll
  for(int n=0;n<11;n++){
    SBAR();
    if(n==0){ sd=MFMA32(kf[8][0],q0,zero); sd=MFMA32(kf[8][1],q1,sd); PIN(sd); }
    else if(n==1){ sm=MFMA32(kf[0][0],q0,zero); sm=MFMA32(kf[0][1],q1,sm); PIN(sm); }
    else if(n<9){ const int u=n-1; s[u%3]=MFMA32(kf[u][0],q0,zero); s[u%3]=MFMA32(kf[u][1],q1,s[u%3]); PIN(s[u%3]); }
    if(n<4){ const int u=n+4; kf[u][0]=ldsr128(KL+u*2048); kf[u][1]=ldsr128(KL+u*2048+1024); }
    if(n==3){
#pragma unroll
      for(int i=0;i<16;i++){ const int a=(i&3)+8*(i>>2); sm[i]=(a<=dl)?sd[i]:((CHECK&&T<8)?ninf:sm[i]); }
    }
    if(n>=3){
      const int t=n-3; const f32x16& x=(t==0)?sm:s[t%3];
      float a=fmax3(x[0],x[1],x[2]), c=fmax3(x[3],x[4],x[5]);
      a=fmax3(a,x[6],x[7]); c=fmax3(c,x[8],x[9]); a=fmax3(a,x[10],x[11]); c=fmax3(c,x[12],x[13]); a=fmax3(a,x[14],x[15]);
      if(t==0) m=__builtin_fmaxf(a,c);
      else { const float mm=fmax3(m,a,c); m=(!CHECK||T-8+t>=0)?mm:m; }
      PIN(m);
    }
  }
  SBAR();
  arrive(cnt+(late?C_P1L:C_P1E));
  if(late){
    WAITV(0); arrive(cnt+C_W1);
    lds_wait(cnt+C_E,4); __builtin_amdgcn_s_setprio(2);
  }
  bf16x8 vfr[2][2];
  vfr[0][0]=ldsr128(VL+8*2048); vfr[0][1]=ldsr128(VL+8*2048+1024);
  vfr[1][0]=ldsr128(VL); vfr[1][1]=ldsr128(VL+1024);
  const u32x4 mk0=*(const LAS u32x4*)((lptr)lds+L_MASK+lane*16), mk1=*(const LAS u32x4*)((lptr)lds+L_MASK+1024+lane*16);
  m=xhalf_max(m);
  f32x16 negm;
#pragma unroll
  for(int i=0;i<16;i++) negm[i]=-m;
  PIN(negm);
  const unsigned one2=((((lane>>2)^(lane>>4))&1)==0)?0x3f803f80u:0u;
  const u32x4 ones={one2,one2,one2,one2};
  f32x16 o; f32x4 ls; u64 fy=0;
  u32x4 p0,p1;
#pragma unroll
  for(int u=0;u<=8;u++){
    SBAR();
    if(u==2){ if(wid==4) fy=__hip_atomic_load(flagp(ws,(lane&31)*8+(b&7),FK_Y),RLX_AGENT); SBAR(); }
    if(u==6){ if(wid==4){ PIN(fy); if(allof(fy==nonce)) arrive(cnt+C_YF); } SBAR(); }
    u32x4 n0,n1;
    if(u+1<8){ const int t=u+1; s[t&1]=MFMA32(kf[t][0],q0,negm); s[t&1]=MFMA32(kf[t][1],q1,s[t&1]); }
    if(u<8){
      f32x16& x=(u==0)?sm:s[u&1];
#pragma unroll
      for(int i=0;i<16;i++) x[i]=__builtin_amdgcn_exp2f((u==0)?x[i]-m:x[i]);
      n0=pack8(x,0); n1=pack8(x,1);
      if(CHECK && u>0){ if(T-8+u<0){ n0=u32x4{0,0,0,0}; n1=u32x4{0,0,0,0}; } }
    }
    if(u==1){
      const u32x4 d0=p0&mk0, d1=p1&mk1;
      o=MFMA32(vfr[0][0],B8(d0),zero); o=MFMA32(vfr[0][1],B8(d1),o);
      o=MFMA32(vfr[1][0],B8(p0^d0),o); o=MFMA32(vfr[1][1],B8(p1^d1),o);
      vfr[1][0]=ldsr128(VL+1*2048); vfr[1][1]=ldsr128(VL+1*2048+1024);
      vfr[0][0]=ldsr128(VL+2*2048); vfr[0][1]=ldsr128(VL+2*2048+1024);
      ls=MFMA16(B8(ones),B8(p0),(f32x4{0,0,0,0})); ls=MFMA16(B8(ones),B8(p1),ls);
    } else if(u>1){
      const int t=u-1;
      o=MFMA32(vfr[t&1][0],B8(p0),o); o=MFMA32(vfr[t&1][1],B8(p1),o);
      if(t+2<8){ vfr[t&1][0]=ldsr128(VL+(t+2)*2048); vfr[t&1][1]=ldsr128(VL+(t+2)*2048+1024); }
      ls=MFMA16(B8(ones),B8(p0),ls); ls=MFMA16(B8(ones),B8(p1),ls);
    }
    if(u>1 && u<7 && !CHECK){
#pragma unroll
      for(int g_=0;g_<6;g_++){ __builtin_amdgcn_sched_group_barrier(0x8,1,0); __builtin_amdgcn_sched_group_barrier(0x402,4,0); }
    }
    if(u+1<8) PIN(s[(u+1)&1]);
    if(u>0){ PIN(ls); PIN(o); }
    if(u<8){ PIN(n0); PIN(n1); p0=n0; p1=n1; }
  }
  SBAR();
  arrive(cnt+(late?C_L:C_E));
  const float il=__builtin_amdgcn_rcpf(ls[0]);
  const unsigned oo=WS_O+(unsigned)(T*64+2*(2*C+late))*1024u+lane*16u;
  __builtin_amdgcn_raw_buffer_store_b128(pack8(o,0,il),wsr,oo,0,SC1);
  __builtin_amdgcn_raw_buffer_store_b128(pack8(o,1,il),wsr,oo+1024,0,SC1);
}

__global__ __launch_bounds__(512,2) NOPK void fused(const u16* Aq,const u16* Ak,const u16* Av,const u16* Ask,const u16* Ah,const u16* AD,
                                               const u16* X,const u16* W,const u16* WO,u16* out,char* ws){
  extern __shared__ __attribute__((aligned(16))) char lds[];
  const char* kpc; asm volatile("s_getpc_b64 %0":"=s"(kpc));
  asm volatile(""::"s"(X),"s"(W),"s"(ws),"s"(WO),"s"(Ak));
  const int tid=threadIdx.x, lane=tid&63; const int wid=__builtin_amdgcn_readfirstlane(tid>>6);
  const int b=blockIdx.x, xcd=b&7, j=b>>3;
  const int R=(xcd>>2)*8+(j>>2), C=(xcd&3)*4+(j&3);
  const int wgi=xcd*32+j;
  const int R2=(xcd>>1)*4+(j>>3), C2=(xcd&1)*8+(j&7), KO=2*(j&7)+4*(j>>3);
  const unsigned lds0=(unsigned)(uintptr_t)lds;
  const unsigned a0=(unsigned)((lane>>3)*2048+(((lane&7)^(lane>>4))*16)), a1=(a0^64u)+(16384-1024);
  const unsigned b0=(unsigned)((lane>>3)*6144+(((lane&7)^(((lane>>4)&1)<<2))*16));
  const char* xb=(const char*)X+(size_t)R*128*2048; const char* wb=(const char*)W+(C&12)*128;
  const int cs[3]={(C&3)*128,2048+((C+1)&3)*128,4096+((C+2)&3)*128};
#pragma unroll
  for(int k=0;k<2;k++){
    asm volatile("s_mov_b32 m0, %3\n\ts_nop 0\n\tglobal_load_lds_dwordx4 %0, %2\n\tglobal_load_lds_dwordx4 %1, %2 offset:1024" :: "v"(a0),"v"(a1),"s"(xb+wid*32768+k*128),"s"(lds0+k*G1_SLOT+wid*2048) : "memory");
#pragma unroll
    for(int g=0;g<3;g++)
      asm volatile("s_mov_b32 m0, %2\n\ts_nop 0\n\tglobal_load_lds_dwordx4 %0, %1" :: "v"(b0),"s"(wb+cs[g]+wid*49152+k*(64*6144)),"s"(lds0+k*G1_SLOT+16384+g*8192+wid*1024) : "memory");
    if(k==0){ if(lane<2) touch((unsigned)((lane? 256+j : j*8+wid)*128),kpc,lds0+L_JUNK); }
    SBAR();
  }
  LAS unsigned* cnt=(LAS unsigned*)((lwptr)lds+L_CNT);
  const __amdgpu_buffer_rsrc_t wsr=__builtin_amdgcn_make_buffer_rsrc(ws,0,WS_END,0x00020000);
  const __amdgpu_buffer_rsrc_t outr=__builtin_amdgcn_make_buffer_rsrc(out,0,8*MB,0x00020000);
  u64 nonce;
#define LOAD_NONCE() do{ asm volatile("s_load_dwordx2 %0, %1, 0x0\n\ts_waitcnt lgkmcnt(0)" : "=s"(nonce) : "s"(ws+F_E) : "memory"); \
    nonce=nonce*6364136223846793005ull+1442695040888963407ull; }while(0)
  auto wo_block=[&](int i) NOPK __attribute__((always_inline)) {
    const int hd=2*((i+KO)&15)+(i>>4);
    const unsigned vo=(unsigned)((lane>>3)*2048+C2*128+(((lane&7)^(((lane>>4)&1)<<2))*16));
    dma4v(vo,vo+16384-1024,vo+2*(16384-1024),vo+3*(16384-1024),(const char*)WO+(size_t)hd*65536,lds0+L_WO+(31-i)*4096);
  };
  if(__builtin_expect(wid>=4,1)){
    const int v=wid-4;
    const unsigned junk=lds0+L_JUNK;
    const int kvit=KVA+(j%KVG), prep0=kvit+2;
    if(__builtin_expect(wid==4,1)){
      xb+=256; wb+=2*64*6144;
      unsigned la=lds0+2*G1_SLOT;
      WAITV(6);
#pragma unroll 1
      for(int k=0;k<14;k++){
        WAITV(40); BAR();
#pragma unroll
        for(int g=0;g<4;g++){
          dma4v(a0,a1,a0+2*(16384-1024),a1+2*(16384-1024),xb+g*65536,la+g*4096);
          if(g<3){
            dma4v(b0,b0+(49152-1024),b0+2*(49152-1024),b0+3*(49152-1024),wb+cs[g],la+16384+g*8192);
            dma4v(b0,b0+(49152-1024),b0+2*(49152-1024),b0+3*(49152-1024),wb+cs[g]+32*6144,la+16384+g*8192+4096);
          }
        }
        xb+=128; wb+=64*6144; la+=G1_SLOT; if(la==lds0+3*G1_SLOT) la=lds0;
      }
      LOAD_NONCE();
      WAITV(40); BAR();
    } else if(wid==5){
      __builtin_amdgcn_s_setprio(3);
      const char* ta; unsigned tstep;
      { const int sg=lane/20, i=lane-sg*20; const bool isx=i<8; const int rem=i-8, kr=(xcd>>2)*32+j;
        tstep=isx?128u:64u*6144u;
        ta=(isx? (const char*)X+(size_t)((xcd>>2)*1024+((xcd&3)*32+j)*8+i)*2048 : (const char*)W+(kr*6144+(rem>>2)*2048+((xcd&3)*4+(rem&3))*128))+(size_t)tstep*(2+sg);
        tstep*=3; }
#define STOUCH(n) do{ if(lane<(n)) touch64(ta,junk); ta+=tstep; }while(0)
      STOUCH(60);
      if(j==0) *(LAS u32x4*)((lwptr)lds+L_CV+lane*16)=u32x4{0,0,0,0};
      if(lane<28) cnt[lane]=0u;
      { const int bq=lane&31, hi=lane>>5; u32x4 mk[2];
#pragma unroll
        for(int d=0;d<8;d++){ const int i=2*d; const int a=(i&3)+8*(i>>2)+4*hi; mk[d>>2][d&3]=((a<=bq)?0xffffu:0u)|((a+1<=bq)?0xffff0000u:0u); }
        *(LAS u32x4*)((lwptr)lds+L_MASK+lane*16)=mk[0]; *(LAS u32x4*)((lwptr)lds+L_MASK+1024+lane*16)=mk[1]; }
      LOAD_NONCE();
      WAITV(7);
      BAR();
      WAITV(1);
      if(lane<6) touch((unsigned)(F_W+(b*8+lane)*128),ws,junk);
      const unsigned pv=(unsigned)((lane>>4)*2048+(lane&15)*16);
      const char* ksrc=(const char*)Ak+(size_t)j*64*2048+xcd*256; const char* vsrc=(const char*)Av+(size_t)j*64*2048+xcd*256;
      if(j>0) asm volatile("s_mov_b32 m0, %2\n\ts_nop 0\n\tglobal_load_lds_dwordx4 %0, %1" :: "v"(pv^(7u<<5)), "s"(ksrc-4*2048), "s"(lds0+L_CV) : "memory");
      int g2=0, nt=0;
#pragma unroll 1
      for(int i=0;i<12;i++){
        if(TI(i)){ STOUCH((nt==3)?40:60); nt++; }
        if(i==kvit) touch((unsigned)((wgi*64+lane)*128),WO,junk);
#pragma unroll 1
        for(;g2<4 && i==kvit;g2++){
          const unsigned p0=pv^(unsigned)(g2<<6), p1=p0^32u;
          dma4v(p0,p0+(8192-1024),p1+2*(8192-1024),p1+3*(8192-1024),ksrc+g2*32768,lds0+L_CV+1024+g2*4096);
          dma4v(p0,p0+(8192-1024),p1+2*(8192-1024),p1+3*(8192-1024),vsrc+g2*32768,lds0+L_CV+17408+g2*4096);
        }
        if(i==prep0-1){ if(TI(i)) WAITV(1); else WAITV(0); }
        BAR();
      }
      u64 fxv=__hip_atomic_load(flagp(ws,(lane&31)*8+xcd,FK_X),RLX_AGENT);
      BAR();
      SBAR(); PIN(fxv);
      if(allof(fxv==nonce)){ arrive(cnt+C_XF); WAITL(); }
      BAR();
      __builtin_amdgcn_s_setprio(0);
    } else {
      __builtin_amdgcn_s_setprio(1);
      const int hv=wid-6, rg=lane&7;
      u32x4 sk[4];
      LOAD_NONCE();
      WAITV(6);
      BAR();
      WAITV(0);
#pragma unroll
      for(int i=0;i<4;i++) sk[i]=*(const u32x4*)(Ask+(xcd*128+(hv*16+(i>>1)*8+(lane>>3))*4)*4+(i&1)*8);
#pragma unroll 1
      for(int i=0;i<prep0;i++) BAR();
      SBAR();
      asm volatile("":"+v"(sk[0]),"+v"(sk[1]),"+v"(sk[2]),"+v"(sk[3]));
#pragma unroll 1
      for(int g=0;g<2;g++){
        const int c4=hv*16+g*8+(lane>>3);
        u32x2 kk[11], vv[8];
        lptr kb=(lptr)lds+L_CV+(8*rg+4)*256+(c4&1)*8;
#pragma unroll
        for(int i=0;i<3;i++) kk[i]=*(const LAS u32x2*)(kb+(i-3)*256+(((c4>>1)^(((rg-1)&7)<<1))*16));
#pragma unroll
        for(int i=0;i<8;i++){ kk[3+i]=*(const LAS u32x2*)(kb+i*256+(((c4>>1)^(rg<<1))*16)); vv[i]=*(const LAS u32x2*)(kb+16384+i*256+(((c4>>1)^(rg<<1))*16)); }
#pragma unroll
        for(int c=0;c<4;c++){
          const unsigned w01=sk[c>>1][(c&1)*2], w23=sk[c>>1][(c&1)*2+1];
          const float w0=lo16(w01),w1=hi16(w01),w2=lo16(w23),w3=hi16(w23);
          float kf[11];
#pragma unroll
          for(int i=0;i<11;i++) kf[i]=(c&1)?hi16(kk[i][c>>1]):lo16(kk[i][c>>1]);
          float o[8];
#pragma unroll
          for(int i=0;i<8;i++){ float vf=(c&1)?hi16(vv[i][c>>1]):lo16(vv[i][c>>1]); o[i]=(kf[i]*w3+kf[i+1]*w2+kf[i+2]*w1+kf[i+3]*w0)*vf; }
          u32x4 r; r[0]=pk(o[0],o[1]); r[1]=pk(o[2],o[3]); r[2]=pk(o[4],o[5]); r[3]=pk(o[6],o[7]);
          __builtin_amdgcn_raw_buffer_store_b128(r,wsr,WS_XT+(unsigned)(xcd*128+c4*4+c)*4096u+(unsigned)(j*128+rg*16),0,SC1);
          SBAR();
          if(PB(c)){ BAR(); SBAR(); }
        }
        sk[0]=sk[2]; sk[1]=sk[3];
      }
      WAITV(0);
      BAR();
      if(hv==0 && lane==0) __hip_atomic_store(flagp(ws,b,FK_X),nonce,RLX_AGENT);
#pragma unroll 1
      for(int i=prep0+2*NPB+2;i<=13;i++) BAR();
      __builtin_amdgcn_s_setprio(0);
      __builtin_amdgcn_s_sleep(8);
      dma4(lane*16,(const char*)Ah+(size_t)(wgi*4+2*hv)*4096,lds0+L_CV+(2*hv)*CV_W+CV_H);
      dma4(lane*16,(const char*)Ah+(size_t)(wgi*4+2*hv+1)*4096,lds0+L_CV+(2*hv+1)*CV_W+CV_H);
      BAR();
    }
    const int ch=wgi*4+v;
    const float Dc=lo16(AD[ch]);
    if(lane<32) *(LAS unsigned*)((lwptr)lds+L_CV+v*CV_W+lane*4)=0u;
    if(lane<40) *(LAS unsigned*)((lwptr)lds+L_CV+v*CV_W+CV_H+4096+lane*4)=0u;
    const bool xr=__builtin_amdgcn_readfirstlane(cnt[C_XF])!=0;
    if(xr){ dma4_sc1(lane*16,ws+WS_XT+(size_t)ch*4096,lds0+L_CV+v*CV_W+CV_X); WAITV(4); }
    else WAITV(0);
    __builtin_amdgcn_s_setprio(2);
    BAR();
    if(!xr){ wait_flags<4>(flagp(ws,(lane&31)*8+xcd,FK_X),nonce); dma4_sc1(lane*16,ws+WS_XT+(size_t)ch*4096,lds0+L_CV+v*CV_W+CV_X); }
    bool h1;
    {
      lwptr LW=(lwptr)lds+L_CV+v*CV_W;
      const int g=lane>>4,q=(lane&15)>>2,p=lane&3;
      lptr ha=(lptr)LW+CV_H+8*(4*p+8*g+q-12);
      lptr xa=(lptr)LW+CV_X+8*(p+8-8*g-q);
      PIN(ha); PIN(xa);
      bf16x8 A[16],B0[16],B1[16];
#pragma unroll
      for(int d=0;d<16;d++) A[d]=cat(ldstr(ha+256*d),ldstr(ha+256*d+32));
      WAITV(0);
      float zf=0.f; PIN(zf);
#define MM0(z,a,b) asm volatile("v_mfma_f32_16x16x32_bf16 %0, %1, %2, 0" : "=&v"(z) : "v"(a), "v"(b))
#define MMA(z,a,b) asm volatile("v_mfma_f32_16x16x32_bf16 %0, %1, %2, %0" : "+v"(z) : "v"(a), "v"(b))
#define LDB(P,e) B##P[e]=cat(ldstr(xa+256*(e)+128*(P)+32),ldstr(xa+256*(e)+128*(P)))
#define ROR(d,s,z) asm volatile("v_add_f32_dpp %0, %1, %2 wave_ror:1 row_mask:0xf bank_mask:0xf" : "=&v"(d) : "v"(s), "v"(z))
#define SHR(d,s,z) asm volatile("v_add_f32_dpp %0, %1, %2 wave_shr:1 row_mask:0xf bank_mask:0xf" : "+v"(d) : "v"(s), "v"(z))
#define ROR2(d,s,z,e,t,y) asm volatile("v_add_f32_dpp %0, %2, %3 wave_ror:1 row_mask:0xf bank_mask:0xf\n\tv_add_f32_dpp %1, %4, %5 wave_ror:1 row_mask:0xf bank_mask:0xf" : "=&v"(d), "=&v"(e) : "v"(s), "v"(z), "v"(t), "v"(y))
#define SHR2(d,s,z,e,t,y) asm volatile("v_add_f32_dpp %0, %2, %3 wave_shr:1 row_mask:0xf bank_mask:0xf\n\tv_add_f32_dpp %1, %4, %5 wave_shr:1 row_mask:0xf bank_mask:0xf" : "+v"(d), "+v"(e) : "v"(s), "v"(z), "v"(t), "v"(y))
      const unsigned yo=WS_Y+(unsigned)ch*4096u+lane*16u;
#define STY(C) __builtin_amdgcn_raw_buffer_store_b128(yw,wsr,yo+(C)*1024,0,SC1)
#define NOP1() asm volatile("s_nop 0")
#define VNOP() __builtin_amdgcn_s_setprio(2)
      unsigned xa_,xb_;
      lptr xe=(lptr)LW+CV_X+2*lane; PIN(xe);
#define LDX(c) do{ xa_=*(const LAS u16*)(xe+256*(c)); xb_=*(const LAS u16*)(xe+256*(c)+128); }while(0)
#define SHX() do{ xa_<<=16; xb_<<=16; PIN(xa_); PIN(xb_); }while(0)
#define FMD(z,x) do{ z[0]=__builtin_fmaf(asf(x),Dc,z[0]); PIN(z); }while(0)
      const gu64* kf1=kvflag(ws,R,C,v>>1,1,lane);
      u64 f1v; unsigned c1v;
#define HOOK1() do{ f1v=__hip_atomic_load(kf1,RLX_AGENT); c1v=*(volatile LAS unsigned*)(cnt+C_KV1); }while(0)
#define HOOK2() do{ h1=allof(f1v==nonce) && __builtin_amdgcn_readfirstlane(c1v)>=2; MEMFENCE(); if(h1) kv_fetch<false>(ws,lds0,R,C,v>>1,1,v&1,lane); }while(0)
      SBAR();
f32x4 z0,z1,z2,z3,z4,z5,z6,z7,z8,z9,z10,z11,z12,z13,z14,z15,z16,z17,z18,z19,z20,z21,z22,z23,z24,z25,z26,z27,z28,z29,z30,z31;
float t2_0,t1_0,u_0,t2_1,t1_1,u_1,t2_2,t1_2,u_2,t2_3,t1_3,u_3,t2_4,t1_4,u_4,t2_5,t1_5,u_5,t2_6,t1_6,u_6,t2_7,t1_7,u_7,t2_8,t1_8,u_8,t2_9,t1_9,u_9,t2_10,t1_10,u_10,t2_11,t1_11,u_11,t2_12,t1_12,u_12,t2_13,t1_13,u_13,t2_14,t1_14,u_14,t2_15,t1_15,u_15,t2_16,t1_16,u_16,t2_17,t1_17,u_17,t2_18,t1_18,u_18,t2_19,t1_19,u_19,t2_20,t1_20,u_20,t2_21,t1_21,u_21,t2_22,t1_22,u_22,t2_23,t1_23,u_23,t2_24,t1_24,u_24,t2_25,t1_25,u_25,t2_26,t1_26,u_26,t2_27,t1_27,u_27,t2_28,t1_28,u_28,t2_29,t1_29,u_29,t2_30,t1_30,u_30,t2_31,t1_31,u_31;
u32x4 yw;

LDB(0,0); LDB(1,0);
LDB(0,1); LDB(1,1);
LDB(0,2); LDB(1,2);
LDX(0);
SBAR();
MM0(z0,A[0],B0[0]); SBAR(); LDB(0,3); SBAR();
MM0(z1,A[0],B1[0]); SBAR(); LDB(1,3); SBAR();
MM0(z2,A[1],B0[0]); SBAR(); LDB(0,4); SBAR();
MM0(z3,A[1],B1[0]); SBAR(); LDB(1,4); SBAR();
MMA(z2,A[0],B0[1]); SBAR();  SBAR();
MMA(z3,A[0],B1[1]); SBAR();  SBAR();
MM0(z4,A[2],B0[0]); SBAR();  SBAR();
MM0(z5,A[2],B1[0]); SBAR();  SBAR();
MMA(z4,A[1],B0[1]); SBAR();  SBAR();
MMA(z5,A[1],B1[1]); SBAR();  SBAR();
MMA(z4,A[0],B0[2]); SBAR(); ROR2(t2_0,zf,z0[2],t2_1,z0[3],z1[2]); SBAR();
MMA(z5,A[0],B1[2]); SBAR(); LDB(0,5); SBAR();
MM0(z6,A[3],B0[0]); SBAR(); SHR2(t2_0,z0[3],z0[2],t2_1,z1[3],z1[2]); SBAR();
MM0(z7,A[3],B1[0]); SBAR(); LDB(1,5); SBAR();
MMA(z6,A[2],B0[1]); SBAR(); ROR2(t1_0,zf,z0[1],t1_1,t2_0,z1[1]); SBAR();
MMA(z7,A[2],B1[1]); SBAR(); SHX(); SBAR();
MMA(z6,A[1],B0[2]); SBAR(); SHR2(t1_0,t2_0,z0[1],t1_1,t2_1,z1[1]); SBAR();
MMA(z7,A[1],B1[2]); SBAR(); FMD(z0,xa_); FMD(z1,xb_); SBAR();
MMA(z6,A[0],B0[3]); SBAR(); ROR2(u_0,zf,z0[0],u_1,t1_0,z1[0]); SBAR();
MMA(z7,A[0],B1[3]); SBAR(); LDX(1); SBAR();
MM0(z8,A[4],B0[0]); SBAR(); SHR2(u_0,t1_0,z0[0],u_1,t1_1,z1[0]); SBAR();
MM0(z9,A[4],B1[0]); SBAR(); yw[0]=pk(u_0,u_1); PIN(yw); SBAR();
MMA(z8,A[3],B0[1]); SBAR(); ROR2(t2_2,z1[3],z2[2],t2_3,z2[3],z3[2]); SBAR();
MMA(z9,A[3],B1[1]); SBAR(); LDB(0,6); SBAR();
MMA(z8,A[2],B0[2]); SBAR(); SHR2(t2_2,z2[3],z2[2],t2_3,z3[3],z3[2]); SBAR();
MMA(z9,A[2],B1[2]); SBAR(); LDB(1,6); SBAR();
MMA(z8,A[1],B0[3]); SBAR(); ROR2(t1_2,t2_1,z2[1],t1_3,t2_2,z3[1]); SBAR();
MMA(z9,A[1],B1[3]); SBAR(); SHX(); SBAR();
MMA(z8,A[0],B0[4]); SBAR(); SHR2(t1_2,t2_2,z2[1],t1_3,t2_3,z3[1]); SBAR();
MMA(z9,A[0],B1[4]); SBAR(); FMD(z2,xa_); FMD(z3,xb_); SBAR();
MM0(z10,A[5],B0[0]); SBAR(); ROR2(u_2,t1_1,z2[0],u_3,t1_2,z3[0]); SBAR();
MM0(z11,A[5],B1[0]); SBAR(); LDX(2); SBAR();
MMA(z10,A[4],B0[1]); SBAR(); SHR2(u_2,t1_2,z2[0],u_3,t1_3,z3[0]); SBAR();
MMA(z11,A[4],B1[1]); SBAR(); yw[1]=pk(u_2,u_3); PIN(yw); SBAR();
MMA(z10,A[3],B0[2]); SBAR(); ROR2(t2_4,z3[3],z4[2],t2_5,z4[3],z5[2]); SBAR();
MMA(z11,A[3],B1[2]); SBAR(); LDB(0,7); SBAR();
MMA(z10,A[2],B0[3]); SBAR(); SHR2(t2_4,z4[3],z4[2],t2_5,z5[3],z5[2]); SBAR();
MMA(z11,A[2],B1[3]); SBAR(); LDB(1,7); SBAR();
MMA(z10,A[1],B0[4]); SBAR(); ROR2(t1_4,t2_3,z4[1],t1_5,t2_4,z5[1]); SBAR();
MMA(z11,A[1],B1[4]); SBAR(); SHX(); SBAR();
MMA(z10,A[0],B0[5]); SBAR(); SHR2(t1_4,t2_4,z4[1],t1_5,t2_5,z5[1]); SBAR();
MMA(z11,A[0],B1[5]); SBAR(); FMD(z4,xa_); FMD(z5,xb_); SBAR();
MM0(z12,A[6],B0[0]); SBAR(); ROR2(u_4,t1_3,z4[0],u_5,t1_4,z5[0]); SBAR();
MM0(z13,A[6],B1[0]); SBAR(); LDX(3); SBAR();
MMA(z12,A[5],B0[1]); SBAR(); SHR2(u_4,t1_4,z4[0],u_5,t1_5,z5[0]); SBAR();
MMA(z13,A[5],B1[1]); SBAR(); yw[2]=pk(u_4,u_5); PIN(yw); SBAR();
MMA(z12,A[4],B0[2]); SBAR(); ROR2(t2_6,z5[3],z6[2],t2_7,z6[3],z7[2]); SBAR();
MMA(z13,A[4],B1[2]); SBAR(); LDB(0,8); SBAR();
MMA(z12,A[3],B0[3]); SBAR(); SHR2(t2_6,z6[3],z6[2],t2_7,z7[3],z7[2]); SBAR();
MMA(z13,A[3],B1[3]); SBAR(); LDB(1,8); SBAR();
MMA(z12,A[2],B0[4]); SBAR(); ROR2(t1_6,t2_5,z6[1],t1_7,t2_6,z7[1]); SBAR();
MMA(z13,A[2],B1[4]); SBAR(); SHX(); SBAR();
MMA(z12,A[1],B0[5]); SBAR(); SHR2(t1_6,t2_6,z6[1],t1_7,t2_7,z7[1]); SBAR();
MMA(z13,A[1],B1[5]); SBAR(); FMD(z6,xa_); FMD(z7,xb_); SBAR();
MMA(z12,A[0],B0[6]); SBAR(); ROR2(u_6,t1_5,z6[0],u_7,t1_6,z7[0]); SBAR();
MMA(z13,A[0],B1[6]); SBAR(); LDX(4); SBAR();
MM0(z14,A[7],B0[0]); SBAR(); SHR2(u_6,t1_6,z6[0],u_7,t1_7,z7[0]); SBAR();
MM0(z15,A[7],B1[0]); SBAR(); yw[3]=pk(u_6,u_7); PIN(yw); STY(0); SBAR();
MMA(z14,A[6],B0[1]); SBAR(); ROR2(t2_8,z7[3],z8[2],t2_9,z8[3],z9[2]); SBAR();
MMA(z15,A[6],B1[1]); SBAR(); LDB(0,9); SBAR();
MMA(z14,A[5],B0[2]); SBAR(); SHR2(t2_8,z8[3],z8[2],t2_9,z9[3],z9[2]); SBAR();
MMA(z15,A[5],B1[2]); SBAR(); LDB(1,9); SBAR();
MMA(z14,A[4],B0[3]); SBAR(); ROR2(t1_8,t2_7,z8[1],t1_9,t2_8,z9[1]); SBAR();
MMA(z15,A[4],B1[3]); SBAR(); SHX(); SBAR();
MMA(z14,A[3],B0[4]); SBAR(); SHR2(t1_8,t2_8,z8[1],t1_9,t2_9,z9[1]); SBAR();
MMA(z15,A[3],B1[4]); SBAR(); FMD(z8,xa_); FMD(z9,xb_); SBAR();
MMA(z14,A[2],B0[5]); SBAR(); ROR2(u_8,t1_7,z8[0],u_9,t1_8,z9[0]); SBAR();
MMA(z15,A[2],B1[5]); SBAR(); LDX(5); SBAR();
MMA(z14,A[1],B0[6]); SBAR(); SHR2(u_8,t1_8,z8[0],u_9,t1_9,z9[0]); SBAR();
MMA(z15,A[1],B1[6]); SBAR(); yw[0]=pk(u_8,u_9); PIN(yw); SBAR();
MMA(z14,A[0],B0[7]); SBAR(); ROR2(t2_10,z9[3],z10[2],t2_11,z10[3],z11[2]); SBAR();
MMA(z15,A[0],B1[7]); SBAR(); LDB(0,10); SBAR();
MM0(z16,A[8],B0[0]); SBAR(); SHR2(t2_10,z10[3],z10[2],t2_11,z11[3],z11[2]); SBAR();
MM0(z17,A[8],B1[0]); SBAR(); LDB(1,10); SBAR();
MMA(z16,A[7],B0[1]); SBAR(); ROR2(t1_10,t2_9,z10[1],t1_11,t2_10,z11[1]); SBAR();
MMA(z17,A[7],B1[1]); SBAR(); SHX(); SBAR();
MMA(z16,A[6],B0[2]); SBAR(); SHR2(t1_10,t2_10,z10[1],t1_11,t2_11,z11[1]); SBAR();
MMA(z17,A[6],B1[2]); SBAR(); FMD(z10,xa_); FMD(z11,xb_); SBAR();
MMA(z16,A[5],B0[3]); SBAR(); ROR2(u_10,t1_9,z10[0],u_11,t1_10,z11[0]); SBAR();
MMA(z17,A[5],B1[3]); SBAR(); LDX(6); SBAR();
MMA(z16,A[4],B0[4]); SBAR(); SHR2(u_10,t1_10,z10[0],u_11,t1_11,z11[0]); SBAR();
MMA(z17,A[4],B1[4]); SBAR(); yw[1]=pk(u_10,u_11); PIN(yw); SBAR();
MMA(z16,A[3],B0[5]); SBAR(); ROR2(t2_12,z11[3],z12[2],t2_13,z12[3],z13[2]); SBAR();
MMA(z17,A[3],B1[5]); SBAR(); LDB(0,11); SBAR();
MMA(z16,A[2],B0[6]); SBAR(); SHR2(t2_12,z12[3],z12[2],t2_13,z13[3],z13[2]); SBAR();
MMA(z17,A[2],B1[6]); SBAR(); LDB(1,11); SBAR();
MMA(z16,A[1],B0[7]); SBAR(); ROR2(t1_12,t2_11,z12[1],t1_13,t2_12,z13[1]); SBAR();
MMA(z17,A[1],B1[7]); SBAR(); SHX(); SBAR();
MMA(z16,A[0],B0[8]); SBAR(); SHR2(t1_12,t2_12,z12[1],t1_13,t2_13,z13[1]); SBAR();
MMA(z17,A[0],B1[8]); SBAR(); FMD(z12,xa_); FMD(z13,xb_); SBAR();
MM0(z18,A[9],B0[0]); SBAR(); ROR2(u_12,t1_11,z12[0],u_13,t1_12,z13[0]); SBAR();
MM0(z19,A[9],B1[0]); SBAR(); LDX(7); SBAR();
MMA(z18,A[8],B0[1]); SBAR(); SHR2(u_12,t1_12,z12[0],u_13,t1_13,z13[0]); SBAR();
MMA(z19,A[8],B1[1]); SBAR(); yw[2]=pk(u_12,u_13); PIN(yw); SBAR();
MMA(z18,A[7],B0[2]); SBAR(); ROR2(t2_14,z13[3],z14[2],t2_15,z14[3],z15[2]); SBAR();
MMA(z19,A[7],B1[2]); SBAR(); LDB(0,12); SBAR();
MMA(z18,A[6],B0[3]); SBAR(); SHR2(t2_14,z14[3],z14[2],t2_15,z15[3],z15[2]); SBAR();
MMA(z19,A[6],B1[3]); SBAR(); LDB(1,12); SBAR();
MMA(z18,A[5],B0[4]); SBAR(); ROR2(t1_14,t2_13,z14[1],t1_15,t2_14,z15[1]); SBAR();
MMA(z19,A[5],B1[4]); SBAR(); SHX(); SBAR();
MMA(z18,A[4],B0[5]); SBAR(); SHR2(t1_14,t2_14,z14[1],t1_15,t2_15,z15[1]); SBAR();
MMA(z19,A[4],B1[5]); SBAR(); FMD(z14,xa_); FMD(z15,xb_); SBAR();
MMA(z18,A[3],B0[6]); SBAR(); ROR2(u_14,t1_13,z14[0],u_15,t1_14,z15[0]); SBAR();
MMA(z19,A[3],B1[6]); SBAR(); LDX(8); SBAR();
MMA(z18,A[2],B0[7]); SBAR(); SHR2(u_14,t1_14,z14[0],u_15,t1_15,z15[0]); SBAR();
MMA(z19,A[2],B1[7]); SBAR(); yw[3]=pk(u_14,u_15); PIN(yw); STY(1); SBAR();
MMA(z18,A[1],B0[8]); SBAR(); ROR2(t2_16,z15[3],z16[2],t2_17,z16[3],z17[2]); SBAR();
MMA(z19,A[1],B1[8]); SBAR(); LDB(0,13); SBAR();
MMA(z18,A[0],B0[9]); SBAR(); SHR2(t2_16,z16[3],z16[2],t2_17,z17[3],z17[2]); SBAR();
MMA(z19,A[0],B1[9]); SBAR(); LDB(1,13); SBAR();
MM0(z20,A[10],B0[0]); SBAR(); ROR2(t1_16,t2_15,z16[1],t1_17,t2_16,z17[1]); SBAR();
MM0(z21,A[10],B1[0]); SBAR(); SHX(); SBAR();
MMA(z20,A[9],B0[1]); SBAR(); SHR2(t1_16,t2_16,z16[1],t1_17,t2_17,z17[1]); SBAR();
MMA(z21,A[9],B1[1]); SBAR(); FMD(z16,xa_); FMD(z17,xb_); SBAR();
MMA(z20,A[8],B0[2]); SBAR(); ROR2(u_16,t1_15,z16[0],u_17,t1_16,z17[0]); SBAR();
MMA(z21,A[8],B1[2]); SBAR(); LDX(9); SBAR();
MMA(z20,A[7],B0[3]); SBAR(); SHR2(u_16,t1_16,z16[0],u_17,t1_17,z17[0]); SBAR();
MMA(z21,A[7],B1[3]); SBAR(); yw[0]=pk(u_16,u_17); PIN(yw); SBAR();
MMA(z20,A[6],B0[4]); SBAR(); ROR2(t2_18,z17[3],z18[2],t2_19,z18[3],z19[2]); SBAR();
MMA(z21,A[6],B1[4]); SBAR(); LDB(0,14); SBAR();
MMA(z20,A[5],B0[5]); SBAR(); SHR2(t2_18,z18[3],z18[2],t2_19,z19[3],z19[2]); SBAR();
MMA(z21,A[5],B1[5]); SBAR(); LDB(1,14); SBAR();
MMA(z20,A[4],B0[6]); SBAR(); ROR2(t1_18,t2_17,z18[1],t1_19,t2_18,z19[1]); SBAR();
MMA(z21,A[4],B1[6]); SBAR(); SHX(); SBAR();
MMA(z20,A[3],B0[7]); SBAR(); SHR2(t1_18,t2_18,z18[1],t1_19,t2_19,z19[1]); SBAR();
MMA(z21,A[3],B1[7]); SBAR(); FMD(z18,xa_); FMD(z19,xb_); SBAR();
MMA(z20,A[2],B0[8]); SBAR(); ROR2(u_18,t1_17,z18[0],u_19,t1_18,z19[0]); SBAR();
MMA(z21,A[2],B1[8]); SBAR(); LDX(10); SBAR();
MMA(z20,A[1],B0[9]); SBAR(); SHR2(u_18,t1_18,z18[0],u_19,t1_19,z19[0]); SBAR();
MMA(z21,A[1],B1[9]); SBAR(); yw[1]=pk(u_18,u_19); PIN(yw); SBAR();
MMA(z20,A[0],B0[10]); SBAR();  SBAR();
MMA(z21,A[0],B1[10]); SBAR();  SBAR();
MM0(z22,A[11],B0[0]); SBAR();  SBAR();
MM0(z23,A[11],B1[0]); SBAR();  SBAR();
MMA(z22,A[10],B0[1]); SBAR();  SBAR();
MMA(z23,A[10],B1[1]); SBAR();  SBAR();
MMA(z22,A[9],B0[2]); SBAR();  SBAR();
MMA(z23,A[9],B1[2]); SBAR();  SBAR();
MMA(z22,A[8],B0[3]); SBAR();  SBAR();
MMA(z23,A[8],B1[3]); SBAR();  SBAR();
MMA(z22,A[7],B0[4]); SBAR(); ROR2(t2_20,z19[3],z20[2],t2_21,z20[3],z21[2]); SBAR();
MMA(z23,A[7],B1[4]); SBAR(); LDB(0,15); SBAR();
MMA(z22,A[6],B0[5]); SBAR(); SHR2(t2_20,z20[3],z20[2],t2_21,z21[3],z21[2]); SBAR();
MMA(z23,A[6],B1[5]); SBAR(); LDB(1,15); SBAR();
MMA(z22,A[5],B0[6]); SBAR(); ROR2(t1_20,t2_19,z20[1],t1_21,t2_20,z21[1]); SBAR();
MMA(z23,A[5],B1[6]); SBAR(); SHX(); SBAR();
MMA(z22,A[4],B0[7]); SBAR(); SHR2(t1_20,t2_20,z20[1],t1_21,t2_21,z21[1]); SBAR();
MMA(z23,A[4],B1[7]); SBAR(); FMD(z20,xa_); FMD(z21,xb_); SBAR();
MMA(z22,A[3],B0[8]); SBAR(); ROR2(u_20,t1_19,z20[0],u_21,t1_20,z21[0]); SBAR();
MMA(z23,A[3],B1[8]); SBAR(); LDX(11); SBAR();
MMA(z22,A[2],B0[9]); SBAR(); SHR2(u_20,t1_20,z20[0],u_21,t1_21,z21[0]); SBAR();
MMA(z23,A[2],B1[9]); SBAR(); yw[2]=pk(u_20,u_21); PIN(yw); SBAR();
MMA(z22,A[1],B0[10]); SBAR();  SBAR();
MMA(z23,A[1],B1[10]); SBAR();  SBAR();
MMA(z22,A[0],B0[11]); SBAR();  SBAR();
MMA(z23,A[0],B1[11]); SBAR();  SBAR();
MM0(z24,A[12],B0[0]); SBAR();  SBAR();
MM0(z25,A[12],B1[0]); SBAR();  SBAR();
MMA(z24,A[11],B0[1]); SBAR();  SBAR();
MMA(z25,A[11],B1[1]); SBAR();  SBAR();
MMA(z24,A[10],B0[2]); SBAR();  SBAR();
MMA(z25,A[10],B1[2]); SBAR();  SBAR();
MMA(z24,A[9],B0[3]); SBAR();  SBAR();
MMA(z25,A[9],B1[3]); SBAR();  SBAR();
MMA(z24,A[8],B0[4]); SBAR(); ROR2(t2_22,z21[3],z22[2],t2_23,z22[3],z23[2]); SBAR();
MMA(z25,A[8],B1[4]); SBAR(); VNOP(); SBAR();
MMA(z24,A[7],B0[5]); SBAR(); SHR2(t2_22,z22[3],z22[2],t2_23,z23[3],z23[2]); SBAR();
MMA(z25,A[7],B1[5]); SBAR(); VNOP(); SBAR();
MMA(z24,A[6],B0[6]); SBAR(); ROR2(t1_22,t2_21,z22[1],t1_23,t2_22,z23[1]); SBAR();
MMA(z25,A[6],B1[6]); SBAR(); SHX(); SBAR();
MMA(z24,A[5],B0[7]); SBAR(); SHR2(t1_22,t2_22,z22[1],t1_23,t2_23,z23[1]); SBAR();
MMA(z25,A[5],B1[7]); SBAR(); FMD(z22,xa_); FMD(z23,xb_); SBAR();
MMA(z24,A[4],B0[8]); SBAR(); ROR2(u_22,t1_21,z22[0],u_23,t1_22,z23[0]); SBAR();
MMA(z25,A[4],B1[8]); SBAR(); LDX(12); SBAR();
MMA(z24,A[3],B0[9]); SBAR(); SHR2(u_22,t1_22,z22[0],u_23,t1_23,z23[0]); SBAR();
MMA(z25,A[3],B1[9]); SBAR(); yw[3]=pk(u_22,u_23); PIN(yw); STY(2); SBAR();
MMA(z24,A[2],B0[10]); SBAR();  SBAR();
MMA(z25,A[2],B1[10]); SBAR();  SBAR();
MMA(z24,A[1],B0[11]); SBAR();  SBAR();
MMA(z25,A[1],B1[11]); SBAR();  SBAR();
MMA(z24,A[0],B0[12]); SBAR();  SBAR();
MMA(z25,A[0],B1[12]); SBAR();  SBAR();
MM0(z26,A[13],B0[0]); SBAR();  SBAR();
MM0(z27,A[13],B1[0]); SBAR();  SBAR();
MMA(z26,A[12],B0[1]); SBAR();  SBAR();
MMA(z27,A[12],B1[1]); SBAR();  SBAR();
MMA(z26,A[11],B0[2]); SBAR();  SBAR();
MMA(z27,A[11],B1[2]); SBAR();  SBAR();
MMA(z26,A[10],B0[3]); SBAR();  SBAR();
MMA(z27,A[10],B1[3]); SBAR();  SBAR();
MMA(z26,A[9],B0[4]); SBAR(); ROR2(t2_24,z23[3],z24[2],t2_25,z24[3],z25[2]); SBAR();
MMA(z27,A[9],B1[4]); SBAR(); VNOP(); SBAR();
MMA(z26,A[8],B0[5]); SBAR(); SHR2(t2_24,z24[3],z24[2],t2_25,z25[3],z25[2]); SBAR();
MMA(z27,A[8],B1[5]); SBAR(); VNOP(); SBAR();
MMA(z26,A[7],B0[6]); SBAR(); ROR2(t1_24,t2_23,z24[1],t1_25,t2_24,z25[1]); SBAR();
MMA(z27,A[7],B1[6]); SBAR(); SHX(); SBAR();
MMA(z26,A[6],B0[7]); SBAR(); SHR2(t1_24,t2_24,z24[1],t1_25,t2_25,z25[1]); SBAR();
MMA(z27,A[6],B1[7]); SBAR(); FMD(z24,xa_); FMD(z25,xb_); SBAR();
MMA(z26,A[5],B0[8]); SBAR(); ROR2(u_24,t1_23,z24[0],u_25,t1_24,z25[0]); SBAR();
MMA(z27,A[5],B1[8]); SBAR(); LDX(13); SBAR();
MMA(z26,A[4],B0[9]); SBAR(); SHR2(u_24,t1_24,z24[0],u_25,t1_25,z25[0]); SBAR();
MMA(z27,A[4],B1[9]); SBAR(); yw[0]=pk(u_24,u_25); PIN(yw); SBAR();
MMA(z26,A[3],B0[10]); SBAR();  SBAR();
MMA(z27,A[3],B1[10]); SBAR();  SBAR();
MMA(z26,A[2],B0[11]); SBAR();  SBAR();
MMA(z27,A[2],B1[11]); SBAR();  SBAR();
MMA(z26,A[1],B0[12]); SBAR();  SBAR();
MMA(z27,A[1],B1[12]); SBAR();  SBAR();
MMA(z26,A[0],B0[13]); SBAR();  SBAR();
MMA(z27,A[0],B1[13]); SBAR();  SBAR();
MM0(z28,A[14],B0[0]); SBAR();  SBAR();
MM0(z29,A[14],B1[0]); SBAR();  SBAR();
MMA(z28,A[13],B0[1]); SBAR();  SBAR();
MMA(z29,A[13],B1[1]); SBAR();  SBAR();
MMA(z28,A[12],B0[2]); SBAR();  SBAR();
MMA(z29,A[12],B1[2]); SBAR();  SBAR();
MMA(z28,A[11],B0[3]); SBAR();  SBAR();
MMA(z29,A[11],B1[3]); SBAR();  SBAR();
MMA(z28,A[10],B0[4]); SBAR(); ROR2(t2_26,z25[3],z26[2],t2_27,z26[3],z27[2]); SBAR();
MMA(z29,A[10],B1[4]); SBAR(); VNOP(); SBAR();
MMA(z28,A[9],B0[5]); SBAR(); SHR2(t2_26,z26[3],z26[2],t2_27,z27[3],z27[2]); SBAR();
MMA(z29,A[9],B1[5]); SBAR(); VNOP(); SBAR();
MMA(z28,A[8],B0[6]); SBAR(); ROR2(t1_26,t2_25,z26[1],t1_27,t2_26,z27[1]); SBAR();
MMA(z29,A[8],B1[6]); SBAR(); SHX(); SBAR();
MMA(z28,A[7],B0[7]); SBAR(); SHR2(t1_26,t2_26,z26[1],t1_27,t2_27,z27[1]); SBAR();
MMA(z29,A[7],B1[7]); SBAR(); FMD(z26,xa_); FMD(z27,xb_); SBAR();
MMA(z28,A[6],B0[8]); SBAR(); ROR2(u_26,t1_25,z26[0],u_27,t1_26,z27[0]); SBAR();
MMA(z29,A[6],B1[8]); SBAR(); LDX(14); SBAR();
MMA(z28,A[5],B0[9]); SBAR(); SHR2(u_26,t1_26,z26[0],u_27,t1_27,z27[0]); SBAR();
MMA(z29,A[5],B1[9]); SBAR(); yw[1]=pk(u_26,u_27); PIN(yw); SBAR();
MMA(z28,A[4],B0[10]); SBAR();  SBAR();
MMA(z29,A[4],B1[10]); SBAR();  SBAR();
MMA(z28,A[3],B0[11]); SBAR();  SBAR();
MMA(z29,A[3],B1[11]); SBAR();  SBAR();
MMA(z28,A[2],B0[12]); SBAR();  SBAR();
MMA(z29,A[2],B1[12]); SBAR();  SBAR();
MMA(z28,A[1],B0[13]); SBAR();  SBAR();
MMA(z29,A[1],B1[13]); SBAR();  SBAR();
MMA(z28,A[0],B0[14]); SBAR();  SBAR();
MMA(z29,A[0],B1[14]); SBAR();  SBAR();
MM0(z30,A[15],B0[0]); SBAR();  SBAR();
MM0(z31,A[15],B1[0]); SBAR();  SBAR();
MMA(z30,A[14],B0[1]); SBAR();  SBAR();
MMA(z31,A[14],B1[1]); SBAR();  SBAR();
MMA(z30,A[13],B0[2]); SBAR();  SBAR();
MMA(z31,A[13],B1[2]); SBAR();  SBAR();
MMA(z30,A[12],B0[3]); SBAR();  SBAR();
MMA(z31,A[12],B1[3]); SBAR();  SBAR();
MMA(z30,A[11],B0[4]); SBAR(); ROR2(t2_28,z27[3],z28[2],t2_29,z28[3],z29[2]); SBAR();
MMA(z31,A[11],B1[4]); SBAR(); VNOP(); SBAR();
MMA(z30,A[10],B0[5]); SBAR(); SHR2(t2_28,z28[3],z28[2],t2_29,z29[3],z29[2]); SBAR();
HOOK1(); SBAR();
MMA(z31,A[10],B1[5]); SBAR(); VNOP(); SBAR();
MMA(z30,A[9],B0[6]); SBAR(); ROR2(t1_28,t2_27,z28[1],t1_29,t2_28,z29[1]); SBAR();
MMA(z31,A[9],B1[6]); SBAR(); SHX(); SBAR();
MMA(z30,A[8],B0[7]); SBAR(); SHR2(t1_28,t2_28,z28[1],t1_29,t2_29,z29[1]); SBAR();
MMA(z31,A[8],B1[7]); SBAR(); FMD(z28,xa_); FMD(z29,xb_); SBAR();
MMA(z30,A[7],B0[8]); SBAR(); ROR2(u_28,t1_27,z28[0],u_29,t1_28,z29[0]); SBAR();
MMA(z31,A[7],B1[8]); SBAR(); LDX(15); SBAR();
MMA(z30,A[6],B0[9]); SBAR(); SHR2(u_28,t1_28,z28[0],u_29,t1_29,z29[0]); SBAR();
MMA(z31,A[6],B1[9]); SBAR(); yw[2]=pk(u_28,u_29); PIN(yw); SBAR();
MMA(z30,A[5],B0[10]); SBAR();  SBAR();
MMA(z31,A[5],B1[10]); SBAR();  SBAR();
MMA(z30,A[4],B0[11]); SBAR();  SBAR();
MMA(z31,A[4],B1[11]); SBAR();  SBAR();
MMA(z30,A[3],B0[12]); SBAR();  SBAR();
MMA(z31,A[3],B1[12]); SBAR();  SBAR();
MMA(z30,A[2],B0[13]); SBAR();  SBAR();
MMA(z31,A[2],B1[13]); SBAR();  SBAR();
MMA(z30,A[1],B0[14]); SBAR();  SBAR();
MMA(z31,A[1],B1[14]); SBAR();  SBAR();
MMA(z30,A[0],B0[15]); SBAR();  SBAR();
MMA(z31,A[0],B1[15]); SBAR();  SBAR();
NOP1(); SBAR();  SBAR();
NOP1(); SBAR();  SBAR();
NOP1(); SBAR();  SBAR();
NOP1(); SBAR();  SBAR();
NOP1(); SBAR();  SBAR();
NOP1(); SBAR();  SBAR();
NOP1(); SBAR();  SBAR();
NOP1(); SBAR();  SBAR();
NOP1(); SBAR(); ROR2(t2_30,z29[3],z30[2],t2_31,z30[3],z31[2]); SBAR();
NOP1(); SBAR(); VNOP(); SBAR();
NOP1(); SBAR(); SHR2(t2_30,z30[3],z30[2],t2_31,z31[3],z31[2]); SBAR();
NOP1(); SBAR(); VNOP(); SBAR();
NOP1(); SBAR(); ROR2(t1_30,t2_29,z30[1],t1_31,t2_30,z31[1]); SBAR();
NOP1(); SBAR(); SHX(); SBAR();
NOP1(); SBAR(); SHR2(t1_30,t2_30,z30[1],t1_31,t2_31,z31[1]); SBAR();
NOP1(); SBAR(); FMD(z30,xa_); FMD(z31,xb_); SBAR();
NOP1(); SBAR(); ROR2(u_30,t1_29,z30[0],u_31,t1_30,z31[0]); SBAR();
NOP1(); SBAR(); VNOP(); SBAR();
NOP1(); SBAR(); SHR2(u_30,t1_30,z30[0],u_31,t1_31,z31[0]); SBAR();
NOP1(); SBAR(); yw[3]=pk(u_30,u_31); PIN(yw); STY(3); SBAR();
      if(v==3 && lane<32) touch((unsigned)((j*32+lane)*2048+((xcd&1)*8+((3-(int)((uintptr_t)WO>>7))&7))*128),WO,junk);
      HOOK2();
    }
    if(!h1){
      wait_flags<4>(kvflag(ws,R,C,v>>1,1,lane),nonce);
      lds_wait(cnt+C_KV1,2);
      kv_fetch<false>(ws,lds0,R,C,v>>1,1,v&1,lane);
    }
    WAITV(0);
    arrive_publish(cnt+C_H1,4,flagp(ws,b,FK_Y),nonce,lane);
    lds_wait(cnt+C_H1,4); lds_wait(cnt+C_P1E,4);
#pragma unroll 1
    for(int i=v;i<WO_P1;i+=4) wo_block(i);
    if(v<2) touch((unsigned)((512*(j>>3)+8*(j&7)+64*(lane>>3)+(lane&7))*2048+(xcd*2+v)*128),Aq,junk);
    __builtin_amdgcn_s_setprio(LP1);
  } else {
    const int wr=wid>>1, wc=wid&1;
    const int r=lane&31, h=lane>>5;
    lptr L=(lptr)lds;
    lptr xa[4];
    { int row=wr*64+r; int sw=(row>>1)&7;
#pragma unroll
      for(int ks=0;ks<4;ks++) xa[ks]=L+row*128+(((2*ks+h)^sw)*16); }
    lptr wa;
    { int blk=(lane>>4)&1,q=(lane&15)>>2,p=lane&3; int c=(wc*4+2*blk+(p>>1))^(((q>>1)&1)<<2); wa=L+16384+(8*h+q)*128+c*16+8*(p&1); }
    f32x16 aq[2],ak[2],av[2];
#pragma unroll
    for(int i=0;i<2;i++){ aq[i]=f32x16{}; ak[i]=f32x16{}; av[i]=f32x16{}; }
    G1F F0,F1;
#define G1_RX(F,ks) do{ F.x0=ldsr128(xa[ks]+so); F.x1=ldsr128(xa[ks]+so+4096); }while(0)
#define G1_RW(F,ks,sg) do{ F.w[2*(sg)]=ldstr(wa+so+(sg)*8192+(16*(ks))*128); F.w[2*(sg)+1]=ldstr(wa+so+(sg)*8192+(16*(ks)+4)*128); }while(0)
#define G1_W(F,sg) cat(F.w[2*(sg)],F.w[2*(sg)+1])
#define G1_STEP(F,N,ks,LD) do{ \
    ak[0]=MFMA32(G1_W(F,1),F.x0,ak[0]); PIN(ak[0]); if(LD) G1_RX(N,ks); SBAR(); \
    ak[1]=MFMA32(G1_W(F,1),F.x1,ak[1]); PIN(ak[1]); if(LD) G1_RW(N,ks,1); SBAR(); \
    av[0]=MFMA32(F.x0,G1_W(F,2),av[0]); PIN(av[0]); if(LD) G1_RW(N,ks,2); SBAR(); \
    av[1]=MFMA32(F.x1,G1_W(F,2),av[1]); PIN(av[1]); if(LD) G1_RW(N,ks,0); SBAR(); \
    aq[0]=MFMA32(G1_W(F,0),F.x0,aq[0]); PIN(aq[0]); SBAR(); \
    aq[1]=MFMA32(G1_W(F,0),F.x1,aq[1]); PIN(aq[1]); SBAR(); }while(0)
    __builtin_amdgcn_s_setprio(3);
    asm volatile("":"+v"(aq[0]),"+v"(aq[1]),"+v"(ak[0]),"+v"(ak[1]),"+v"(av[0]),"+v"(av[1]));
    LOAD_NONCE();
    WAITV(6);
    BAR();
    int so=0;
    G1_RX(F0,0); G1_RW(F0,0,1); G1_RW(F0,0,2); G1_RW(F0,0,0); SBAR();
#pragma unroll 1
    for(int t=0;t<15;t++){
      G1_STEP(F0,F1,1,true);
      G1_STEP(F1,F0,2,true);
      G1_STEP(F0,F1,3,true);
      so=(so==2*G1_SLOT)?0:so+G1_SLOT;
      asm volatile("s_waitcnt vmcnt(0) lgkmcnt(0)":::"memory"); BAR(); SBAR();
      G1_STEP(F1,F0,0,true);
    }
    G1_STEP(F0,F1,1,true);
    G1_STEP(F1,F0,2,true);
    G1_STEP(F0,F1,3,true);
    arrive(cnt+C_RD);
    SBAR();
    G1_STEP(F1,F0,0,false);
    const float qs=0.17677669529663687f*1.4426950408889634f;
    u32x4 kf[2][2],vf[2][2];
    const unsigned go=WS_KV+(unsigned)b*32768u+wc*16384u+(2*wr)*2048u+lane*16u;
#define KV_STORE() _Pragma("unroll") for(int rt=0;rt<2;rt++) _Pragma("unroll") for(int s=0;s<2;s++) __builtin_amdgcn_raw_buffer_store_b128(kf[rt][s],wsr,go+rt*2048+s*1024,0,SC1); \
      _Pragma("unroll") for(int rt=0;rt<2;rt++) _Pragma("unroll") for(int s=0;s<2;s++) __builtin_amdgcn_raw_buffer_store_b128(vf[rt][s],wsr,go+8192+rt*2048+s*1024,0,SC1)
#pragma unroll
    for(int rt=0;rt<2;rt++)
#pragma unroll
      for(int s=0;s<2;s++){ kf[rt][s]=pack8(ak[rt],s); vf[rt][s]=pack8(av[rt],s); }
    if(wc==0){ KV_STORE(); }
    lds_wait(cnt+C_RD,4);
#pragma unroll
    for(int rt=0;rt<2;rt++){
      lwptr lq=(lwptr)lds+(1-wc)*HR+49152+(2*wr+rt)*2048+lane*16;
#pragma unroll
      for(int s=0;s<2;s++) *(LAS u32x4*)(lq+s*1024)=pack8(aq[rt],s,qs);
    }
    if(R<2){
#pragma unroll 1
      for(int i=0;i<8*(2-R);i++) *(LAS u32x4*)((lwptr)lds+(1-wc)*HR+wr*24576+i*1024+lane*16)=u32x4{0,0,0,0};
    }
    if(wc==1){ lds_wait(cnt+C_KV0,2); KV_STORE(); }
    WAITV(0);
    arrive_publish(cnt+C_KV0+wc,2,flagp(ws,b,FK_KV0+wc),nonce,lane);
    if(wid==0){
      lds_wait(cnt+C_KV0,2);
      const int kind=(lane>>2)&1, ri=lane&3;
      const gu64* fp=kvflag(ws,R,C,kind,0,ri);
      unsigned todo=(R>=2)?0x77u:((R==1)?0x66u:0x44u);
#pragma unroll 1
      for(unsigned sp=0;sp<SPIN_CAP && todo;sp++){
        u64 fv=0;
        if(lane<8 && ((todo>>lane)&1)) fv=__hip_atomic_load(fp,RLX_AGENT);
        unsigned rdy=(unsigned)__builtin_amdgcn_ballot_w64(fv==nonce)&todo;
        MEMFENCE();
        todo&=~rdy;
        if(!rdy) __builtin_amdgcn_s_sleep(2);
#pragma unroll 1
        while(rdy){
          const int bit=__builtin_ctz(rdy); rdy&=rdy-1;
          const int kd=bit>>2, i=bit&3;
          const char* src=ws+WS_KV+(size_t)blk_of(R-2+i,(C&12)|((C-1-kd)&3))*32768+kd*8192;
          const unsigned dst=lds0+HR+(1-kd)*24576+i*8192;
          dma4_sc1(lane*16,src,dst); dma4_sc1(lane*16,src+4096,dst+4096);
        }
      }
      WAITV(0);
      arrive(cnt+C_H0K);
    } else lds_wait(cnt+C_H0K,1);
    __builtin_amdgcn_s_setprio(1);
  }
  const int late=wid>>2;
  if(R>=2) attention<false>(lds,cnt,ws,wsr,b,R,C,wid,lane,nonce);
  else     attention<true >(lds,cnt,ws,wsr,b,R,C,wid,lane,nonce);
  if(!late){
    WAITV(0);
    arrive_publish(cnt+C_O0,4,flagp(ws,b,FK_O0),nonce,lane);
    if(wid<3){ lds_wait(cnt+C_E,4); lds_wait(cnt+C_P1L,4);
#pragma unroll 1
      for(int i=WO_P1+wid;i<32;i+=3) wo_block(i);
    }
    const int T2=R2*4+wid;
    const unsigned ob=WS_O+(unsigned)T2*65536u+lane*16u;
    u32x4 of[G2D][2];
#define G2_LDO(i,n) do{ const unsigned so_=(unsigned)((((n)+KO)&15)*4096); of[(i)%G2D][0]=__builtin_amdgcn_raw_buffer_load_b128(wsr,obv,so_,OAUX); of[(i)%G2D][1]=__builtin_amdgcn_raw_buffer_load_b128(wsr,obv+1024,so_,OAUX); }while(0)
    const int h=lane>>5,blk=(lane>>4)&1,q=(lane&15)>>2,p=lane&3;
    lptr wa[2];
#pragma unroll
    for(int ct=0;ct<2;ct++){ int c=(ct*4+2*blk+(p>>1))^(((q>>1)&1)<<2); wa[ct]=(lptr)lds+L_WO+(31-G2D)*4096+(4*h+q)*128+c*16+8*(p&1); }
    f32x16 acc[2]; acc[0]=f32x16{}; acc[1]=f32x16{};
    s16x4 wf[2][8];
#define G2_LDW(i) do{ _Pragma("unroll") for(int s_=0;s_<2;s_++) _Pragma("unroll") for(int ct_=0;ct_<2;ct_++){ lptr a_=wa[ct_]+(G2D-(i))*4096+s_*2048; \
      wf[(i)&1][4*s_+2*ct_]=ldstr(a_); wf[(i)&1][4*s_+2*ct_+1]=ldstr(a_+1024); } }while(0)
    lds_wait(cnt+C_W1,4);
    G2_LDW(0);
    PIN(acc[0]); PIN(acc[1]);
    if(wid==3){ wait_flags(flagp(ws,blk_of(R2,lane&15),FK_O0),nonce); arrive(cnt+C_EF); } else lds_wait(cnt+C_EF,1);
    unsigned obv=ob;
#pragma unroll
    for(int i=0;i<G2D;i++) G2_LDO(i,i);
    u64 f1=0;
    constexpr int NIT=32/G2D, ITL=16/G2D-1;
#pragma unroll 1
    for(int it=0;it<NIT;it++){
      if(it==1) arrive(cnt+C_W2);
      if(it==ITL){
        if(wid==3){ if(!allof(f1==nonce)) wait_flags(flagp(ws,blk_of(R2,lane&15),FK_O1),nonce); MEMFENCE(); arrive(cnt+C_LF); }
        else lds_wait(cnt+C_LF,1);
        __builtin_amdgcn_s_setprio(3);
      }
      if(it==ITL+1) lds_wait(cnt+C_W2,4);
      if(it==NIT-1) arrive(cnt+C_G2);
      PIN(wa[0]); PIN(wa[1]);
      const int nb=G2D*it+G2D;
      obv=ob+((it<ITL)?0u:((it==NIT-1)?0x40000000u:2048u)); PIN(obv);
#pragma unroll
      for(int i=0;i<G2D;i++){
        SBAR();
        if(i==G2D-4){ if(wid==3 && it==ITL-1) f1=__hip_atomic_load(flagp(ws,blk_of(R2,lane&15),FK_O1),RLX_AGENT); }
        G2_LDW(i+1);
#pragma unroll
        for(int s=0;s<2;s++)
#pragma unroll
          for(int ct=0;ct<2;ct++) acc[ct]=MFMA32(cat(wf[i&1][4*s+2*ct],wf[i&1][4*s+2*ct+1]),B8(of[i][s]),acc[ct]);
#pragma unroll
        for(int g_=0;g_<4;g_++){ __builtin_amdgcn_sched_group_barrier(0x100,2,0); __builtin_amdgcn_sched_group_barrier(0x8,1,0); }
        PIN(acc[0]); PIN(acc[1]);
        SBAR();
        G2_LDO(i,nb+i);
      }
      wa[0]-=G2D*4096; wa[1]-=G2D*4096;
    }
    lwptr stg=(lwptr)lds+L_WO+(31-wid)*4096;
#pragma unroll
    for(int ct=0;ct<2;ct++){
#pragma unroll
      for(int gp=0;gp<2;gp++){
        unsigned a0=pk(acc[ct][8*gp],acc[ct][8*gp+1]), a1=pk(acc[ct][8*gp+2],acc[ct][8*gp+3]);
        unsigned b0=pk(acc[ct][8*gp+4],acc[ct][8*gp+5]), b1=pk(acc[ct][8*gp+6],acc[ct][8*gp+7]);
        auto r0=__builtin_amdgcn_permlane32_swap(a0,b0,false,false); auto r1=__builtin_amdgcn_permlane32_swap(a1,b1,false,false);
        u32x4 vv={r0[0],r1[0],r0[1],r1[1]};
        *(LAS u32x4*)(stg+(lane&31)*128+(((ct*4+gp*2+h)^(lane&7))*16))=vv;
      }
    }
#pragma unroll
    for(int i=0;i<4;i++){
      const int row=8*i+(lane>>3);
      const u32x4 vv=*(const LAS u32x4*)(stg+row*128+(((lane&7)^(row&7))*16));
      __builtin_amdgcn_raw_buffer_store_b128(vv,outr,(unsigned)(T2*32+row)*4096u+2048u+C2*128u+(lane&7)*16u,0,SC1);
    }
  } else {
    const int v=wid-4;
    const int chb=xcd*128+(v&1)*64, rb=512*(j>>3)+8*(j&7)+4*(v>>1)+64*(lane>>3);
    u32x4 qq[4],yy[4];
    const bool yr=__builtin_amdgcn_readfirstlane(*(volatile LAS unsigned*)(cnt+C_YF))!=0;
    MEMFENCE();
    unsigned yo=WS_Y+(unsigned)(chb+(lane>>2))*4096u+(unsigned)(j*128+(v>>1)*64+(lane&3)*16);
    unsigned yo1=yr? yo : 0x40000000u;
#pragma unroll
    for(int i=0;i<4;i++){ qq[i]=*(const u32x4*)(Aq+(size_t)(rb+i)*1024+chb+(lane&7)*8); yy[i]=__builtin_amdgcn_raw_buffer_load_b128(wsr,yo1,i*65536,SC1); }
    WAITV(8);
    arrive_publish(cnt+C_O1,4,flagp(ws,b,FK_O1),nonce,lane);
    __builtin_amdgcn_s_setprio(0);
    if(!yr){ wait_flags<8>(flagp(ws,(lane&31)*8+xcd,FK_Y),nonce);
#pragma unroll
      for(int i=0;i<4;i++) yy[i]=__builtin_amdgcn_raw_buffer_load_b128(wsr,yo,i*65536,SC1); }
    lds_wait(cnt+C_L,4);
    lwptr stg=(lwptr)lds+v*4096;
#pragma unroll
    for(int i=0;i<4;i++) *(LAS u32x4*)(stg+i*1024+lane*16)=yy[i];
    { const int g=lane>>4,q=(lane&15)>>2,p=lane&3;
      bf16x8 tt[4];
#pragma unroll
      for(int n=0;n<4;n++){ lptr a=(lptr)stg+(8*(2*n+(g>>1))+q)*64+(g&1)*32+8*p; tt[n]=cat(ldstr(a),ldstr(a+256)); }
      MEMFENCE();
      const int srow=16*(g&1)+(lane&15);
#pragma unroll
      for(int n=0;n<4;n++) *(LAS bf16x8*)(stg+srow*128+(((2*n+(g>>1))^(srow&7))*16))=tt[n];
    }
    u32x4 ro[4];
#pragma unroll
    for(int i=0;i<4;i++){
      const int row=8*i+(lane>>3);
      const u32x4 yt=*(const LAS u32x4*)(stg+row*128+(((lane&7)^(row&7))*16));
#pragma unroll
      for(int d=0;d<4;d++) ro[i][d]=pk(lo16(qq[i][d])*lo16(yt[d]),hi16(qq[i][d])*hi16(yt[d]));
      PIN(ro[i]);
    }
    if(b==0 && wid==7){
#pragma unroll 1
      for(unsigned sp=0;sp<SPIN_CAP;sp++){
        u64 f[4];
#pragma unroll
        for(int i=0;i<4;i++) f[i]=__hip_atomic_load(flagp(ws,i*64+lane,FK_Y),RLX_AGENT);
        if(allof(f[0]==nonce && f[1]==nonce && f[2]==nonce && f[3]==nonce)) break;
        __builtin_amdgcn_s_sleep(16);
      }
      if(lane==0) __hip_atomic_store((gu64*)(ws+F_E),nonce,RLX_AGENT);
    }
    lds_wait(cnt+C_G2,4);
#pragma unroll
    for(int i=0;i<4;i++) __builtin_amdgcn_raw_buffer_store_b128(ro[i],outr,(unsigned)(rb+i)*4096u+chb*2u+(lane&7)*16u,0,SC1);
  }
}

extern "C" void kernel_launch(void* const* d_in, const int* in_sizes, int n_in, void* d_out, int out_size, void* d_ws, size_t ws_size, hipStream_t stream){
  (void)hipFuncSetAttribute((const void*)fused,hipFuncAttributeMaxDynamicSharedMemorySize,LDS_BYTES);
  fused<<<256,512,LDS_BYTES,stream>>>((const u16*)d_in[0],(const u16*)d_in[1],(const u16*)d_in[2],(const u16*)d_in[3],(const u16*)d_in[4],(const u16*)d_in[5],
                                      (const u16*)d_in[6],(const u16*)d_in[7],(const u16*)d_in[8],(u16*)d_out,(char*)d_ws+WSOFF);
}
```

```cpp
#include <hip/hip_runtime.h>
#include <hip/hip_bf16.h>
#include <stdint.h>
#if defined(__HIP_DEVICE_COMPILE__)
#define NOPK __attribute__((target("no-packed-fp32-ops")))
#else
#define NOPK
#endif
typedef unsigned short u16;
typedef unsigned long long u64;
typedef short bf16x8 __attribute__((ext_vector_type(8)));
typedef short s16x4 __attribute__((ext_vector_type(4)));
typedef float f32x16 __attribute__((ext_vector_type(16)));
typedef float f32x4 __attribute__((ext_vector_type(4)));
typedef float f32x2 __attribute__((ext_vector_type(2)));
typedef __bf16 bf2 __attribute__((ext_vector_type(2)));
typedef unsigned u32x4 __attribute__((ext_vector_type(4)));
typedef unsigned u32x2 __attribute__((ext_vector_type(2)));
#define LAS __attribute__((address_space(3)))
#define GAS __attribute__((address_space(1)))
typedef LAS const char* lptr;
typedef LAS char* lwptr;
typedef GAS u64 gu64;
#define DI __device__ __forceinline__ NOPK
#define MFMA32(a,b,c) __builtin_amdgcn_mfma_f32_32x32x16_bf16((a),(b),(c),0,0,0)
#define MFMA16(a,b,c) __builtin_amdgcn_mfma_f32_16x16x32_bf16((a),(b),(c),0,0,0)
#define SBAR() __builtin_amdgcn_sched_barrier(0)
#define PIN(x) asm volatile("":"+v"(x))
#define MEMFENCE() asm volatile("":::"memory")
#define BAR() asm volatile("s_barrier":::"memory")
#define WAITV(n) asm volatile("s_waitcnt vmcnt(" #n ")":::"memory")
#define WAITL() asm volatile("s_waitcnt lgkmcnt(0)":::"memory")
#define RLX_AGENT __ATOMIC_RELAXED, __HIP_MEMORY_SCOPE_AGENT
#define RLX_WG __ATOMIC_RELAXED, __HIP_MEMORY_SCOPE_WORKGROUP
#define SC1 16
#define B8(x) __builtin_bit_cast(bf16x8,(x))

constexpr unsigned MB=1u<<20;
constexpr unsigned WS_KV=0, WS_O=8*MB, WS_XT=12*MB, WS_Y=16*MB, WS_F=20*MB, WS_END=21*MB;
constexpr unsigned F_E=WS_F, F_W=WS_F+4096;
enum { FK_KV0=0, FK_KV1, FK_O0, FK_O1, FK_X, FK_Y };
constexpr int G1_SLOT=40960;
constexpr int HR=57344;
constexpr int L_CV=126976, CV_W=8480, CV_H=128, CV_X=128+4096+160;
constexpr int L_MISC=L_CV+4*CV_W;
constexpr int L_WO=L_MISC-131072;
constexpr int WO_P1=19;
static_assert(L_WO+(32-WO_P1)*4096>=HR+24576 && L_WO>=24576,"wo image");
constexpr int L_CNT=L_MISC;
constexpr int L_JUNK=L_CNT+128;
constexpr int L_MASK=L_JUNK+256;
constexpr int LDS_BYTES=L_MASK+2048;
static_assert(LDS_BYTES<=163840,"lds");
enum { C_RD=0, C_S15, C_KV0, C_KV1, C_H0K, C_H1, C_P1E, C_P1L, C_E, C_L, C_W1, C_W2, C_O0, C_O1, C_EF, C_LF, C_XF, C_YF, C_G2, C_NUM };
constexpr int G2D=8;
constexpr unsigned SPIN_CAP=1u<<17, LSPIN_CAP=1u<<22;

DI unsigned pk(float a,float b){ f32x2 v={a,b}; bf2 r=__builtin_convertvector(v,bf2); return __builtin_bit_cast(unsigned,r); }
DI float asf(unsigned u){ return __builtin_bit_cast(float,u); }
DI unsigned asu(float f){ return __builtin_bit_cast(unsigned,f); }
DI bool allof(bool p){ return __builtin_amdgcn_ballot_w64(p)==~0ull; }
DI float lo16(unsigned u){ return asf(u<<16); }
DI float hi16(unsigned u){ return asf(u&0xffff0000u); }
DI bf16x8 ldsr128(lptr p){ return *(const LAS bf16x8*)p; }
DI s16x4 ldstr(lptr p){ return __builtin_amdgcn_ds_read_tr16_b64_v4i16((LAS s16x4*)p); }
DI bf16x8 cat(s16x4 a,s16x4 b){ return __builtin_shufflevector(a,b,0,1,2,3,4,5,6,7); }
DI u32x4 pack8(const f32x16& x,int s,float sc){ u32x4 r; r[0]=pk(x[8*s]*sc,x[8*s+1]*sc); r[1]=pk(x[8*s+2]*sc,x[8*s+3]*sc); r[2]=pk(x[8*s+4]*sc,x[8*s+5]*sc); r[3]=pk(x[8*s+6]*sc,x[8*s+7]*sc); return r; }
DI u32x4 pack8(const f32x16& x,int s){ u32x4 r; r[0]=pk(x[8*s],x[8*s+1]); r[1]=pk(x[8*s+2],x[8*s+3]); r[2]=pk(x[8*s+4],x[8*s+5]); r[3]=pk(x[8*s+6],x[8*s+7]); return r; }
#define DMA4_BODY(FL) "s_mov_b32 m0, %2\n\ts_nop 0\n\t" \
   "global_load_lds_dwordx4 %0, %1" FL "\n\tglobal_load_lds_dwordx4 %0, %1 offset:1024" FL "\n\tglobal_load_lds_dwordx4 %0, %1 offset:2048" FL "\n\tglobal_load_lds_dwordx4 %0, %1 offset:3072" FL
DI void dma4(unsigned voff,const void* sbase,unsigned ldsaddr){ asm volatile(DMA4_BODY(" nt") :: "v"(voff), "s"(sbase), "s"(ldsaddr) : "memory"); }
DI void dma4_sc1(unsigned voff,const void* sbase,unsigned ldsaddr){ asm volatile(DMA4_BODY(" sc1") :: "v"(voff), "s"(sbase), "s"(ldsaddr) : "memory"); }
DI void dma4v(unsigned v0,unsigned v1,unsigned v2,unsigned v3,const void* sbase,unsigned ldsaddr){
  asm volatile("s_mov_b32 m0, %5\n\ts_nop 0\n\t"
    "global_load_lds_dwordx4 %0, %4\n\tglobal_load_lds_dwordx4 %1, %4 offset:1024\n\tglobal_load_lds_dwordx4 %2, %4 offset:2048\n\tglobal_load_lds_dwordx4 %3, %4 offset:3072"
    :: "v"(v0),"v"(v1),"v"(v2),"v"(v3), "s"(sbase), "s"(ldsaddr) : "memory");
}
DI void touch(unsigned voff,const void* sbase,unsigned junk){
  asm volatile("s_mov_b32 m0, %2\n\ts_nop 0\n\tglobal_load_lds_dword %0, %1" :: "v"(voff), "s"(sbase), "s"(junk) : "memory");
}
DI void touch64(const void* a,unsigned junk){
  asm volatile("s_mov_b32 m0, %1\n\ts_nop 0\n\tglobal_load_lds_dword %0, off" :: "v"(a), "s"(junk) : "memory");
}
template<int SL=2>
DI void wait_flags(const gu64* p,u64 nonce){
#pragma unroll 1
  for(unsigned sp=0;sp<SPIN_CAP;sp++){
    u64 v=__hip_atomic_load(p,RLX_AGENT);
    if(allof(v==nonce)) break;
    __builtin_amdgcn_s_sleep(SL);
  }
  MEMFENCE();
}
DI void lds_wait(LAS unsigned* p,unsigned n){
#pragma unroll 1
  for(unsigned sp=0;sp<LSPIN_CAP;sp++){
    unsigned v=__hip_atomic_load(p,RLX_WG);
    if(__builtin_amdgcn_readfirstlane(v)>=n) break;
    __builtin_amdgcn_s_sleep(1);
  }
  MEMFENCE();
}
DI void arrive(LAS unsigned* cnt){
  unsigned one=1u;
  asm volatile("s_mov_b64 exec, 1\n\tds_add_u32 %0, %1\n\ts_mov_b64 exec, -1" :: "v"(cnt), "v"(one) : "memory");
}
DI void arrive_publish(LAS unsigned* cnt,unsigned n,gu64* flag,u64 nonce,int lane){
  unsigned one=1u, old;
  asm volatile("s_mov_b64 exec, 1\n\tds_add_rtn_u32 %0, %1, %2\n\ts_mov_b64 exec, -1\n\ts_waitcnt lgkmcnt(0)" : "=&v"(old) : "v"(cnt), "v"(one) : "memory");
  if(__builtin_amdgcn_readfirstlane(old)==n-1){ if(lane==0) __hip_atomic_store(flag,nonce,RLX_AGENT); }
  MEMFENCE();
}
DI gu64* flagp(char* ws,int blk,int k){ return (gu64*)(ws+F_W+(unsigned)(blk*8+k)*128u); }
DI int blk_of(int R,int C){ return (((R&7)*4+(C&3))<<3)|((R>>3)*4+(C>>2)); }
DI float fmax3(float a,float b,float c){ return __builtin_fmaxf(__builtin_fmaxf(a,b),c); }
DI float xhalf_max(float m){ auto rr=__builtin_amdgcn_permlane32_swap(asu(m),asu(m),false,false); return __builtin_fmaxf(asf(rr[0]),asf(rr[1])); }
struct G1F { bf16x8 x0,x1; s16x4 w[6]; };

DI const gu64* kvflag(char* ws,int R,int C,int kind,int head,int lane){
  const int Cs=(C&12)|((C-1-kind)&3); const int rr=(lane<3)? lane : 2; const int Rl=(R-2+rr<0)? R : R-2+rr;
  return flagp(ws,blk_of(Rl,Cs),FK_KV0+head);
}
DI void kv_fetch(char* ws,unsigned lds0,int R,int C,int kind,int head,int half,int lane){
  const int Cs=(C&12)|((C-1-kind)&3);
#pragma unroll 1
  for(int i=(R<2)?2-R:0;i<3;i++){
    const char* src=ws+WS_KV+(size_t)blk_of(R-2+i,Cs)*32768+head*16384+kind*8192+half*4096;
    const unsigned dst=lds0+(1-head)*HR+(1-kind)*24576+i*8192+half*4096;
    dma4_sc1(lane*16,src,dst);
  }
}
template<bool CHECK>
DI void attention(char* lds,LAS unsigned* cnt,char* ws,__amdgpu_buffer_rsrc_t wsr,int b,int R,int C,int wid,int lane,u64 nonce){
  const int late=wid>>2, jq=wid&3;
  const int T=4*R+jq;
  lptr VL=(lptr)lds+(1-late)*HR+jq*2048+lane*16;
  lptr KL=VL+24576;
  lptr QL=(lptr)lds+(1-late)*HR+49152+jq*2048+lane*16;
  const f32x16 zero=f32x16{};
  const float ninf=-__builtin_inff();
  const int dl=(lane&31)-4*(lane>>5);
  bf16x8 kf[9][2];
  const bf16x8 q0=ldsr128(QL), q1=ldsr128(QL+1024);
  kf[8][0]=ldsr128(KL+8*2048); kf[8][1]=ldsr128(KL+8*2048+1024);
#pragma unroll
  for(int u=0;u<4;u++){ kf[u][0]=ldsr128(KL+u*2048); kf[u][1]=ldsr128(KL+u*2048+1024); }
  f32x16 sd,sm,s[3];
  float m;
#pragma unroll
  for(int n=0;n<11;n++){
    SBAR();
    if(n==0){ sd=MFMA32(kf[8][0],q0,zero); sd=MFMA32(kf[8][1],q1,sd); PIN(sd); }
    else if(n==1){ sm=MFMA32(kf[0][0],q0,zero); sm=MFMA32(kf[0][1],q1,sm); PIN(sm); }
    else if(n<9){ const int u=n-1; s[u%3]=MFMA32(kf[u][0],q0,zero); s[u%3]=MFMA32(kf[u][1],q1,s[u%3]); PIN(s[u%3]); }
    if(n<4){ const int u=n+4; kf[u][0]=ldsr128(KL+u*2048); kf[u][1]=ldsr128(KL+u*2048+1024); }
    if(n==3){
#pragma unroll
      for(int i=0;i<16;i++){ const int a=(i&3)+8*(i>>2); sm[i]=(a<=dl)?sd[i]:((CHECK&&T<8)?ninf:sm[i]); }
    }
    if(n>=3){
      const int t=n-3; const f32x16& x=(t==0)?sm:s[t%3];
      float a=fmax3(x[0],x[1],x[2]), c=fmax3(x[3],x[4],x[5]);
      a=fmax3(a,x[6],x[7]); c=fmax3(c,x[8],x[9]); a=fmax3(a,x[10],x[11]); c=fmax3(c,x[12],x[13]); a=fmax3(a,x[14],x[15]);
      if(t==0) m=__builtin_fmaxf(a,c);
      else { const float mm=fmax3(m,a,c); m=(!CHECK||T-8+t>=0)?mm:m; }
      PIN(m);
    }
  }
  SBAR();
  arrive(cnt+(late?C_P1L:C_P1E));
  if(late){
    WAITV(0); arrive(cnt+C_W1);
    lds_wait(cnt+C_E,4); __builtin_amdgcn_s_setprio(2);
  }
  bf16x8 vfr[2][2];
  vfr[0][0]=ldsr128(VL+8*2048); vfr[0][1]=ldsr128(VL+8*2048+1024);
  vfr[1][0]=ldsr128(VL); vfr[1][1]=ldsr128(VL+1024);
  const u32x4 mk0=*(const LAS u32x4*)((lptr)lds+L_MASK+lane*16), mk1=*(const LAS u32x4*)((lptr)lds+L_MASK+1024+lane*16);
  m=xhalf_max(m);
  f32x16 negm;
#pragma unroll
  for(int i=0;i<16;i++) negm[i]=-m;
  PIN(negm);
  const unsigned one2=((((lane>>2)^(lane>>4))&1)==0)?0x3f803f80u:0u;
  const u32x4 ones={one2,one2,one2,one2};
  f32x16 o; f32x4 ls; u64 fy=0;
  u32x4 p0,p1;
#pragma unroll
  for(int u=0;u<=8;u++){
    SBAR();
    if(u==2){ if(wid==4) fy=__hip_atomic_load(flagp(ws,(lane&31)*8+(b&7),FK_Y),RLX_AGENT); SBAR(); }
    if(u==6){ if(wid==4){ PIN(fy); if(allof(fy==nonce)) arrive(cnt+C_YF); } SBAR(); }
    u32x4 n0,n1;
    if(u+1<8){ const int t=u+1; s[t&1]=MFMA32(kf[t][0],q0,negm); s[t&1]=MFMA32(kf[t][1],q1,s[t&1]); }
    if(u<8){
      f32x16& x=(u==0)?sm:s[u&1];
#pragma unroll
      for(int i=0;i<16;i++) x[i]=__builtin_amdgcn_exp2f((u==0)?x[i]-m:x[i]);
      n0=pack8(x,0); n1=pack8(x,1);
      if(CHECK && u>0){ if(T-8+u<0){ n0=u32x4{0,0,0,0}; n1=u32x4{0,0,0,0}; } }
    }
    if(u==1){
      const u32x4 d0=p0&mk0, d1=p1&mk1;
      o=MFMA32(vfr[0][0],B8(d0),zero); o=MFMA32(vfr[0][1],B8(d1),o);
      o=MFMA32(vfr[1][0],B8(p0^d0),o); o=MFMA32(vfr[1][1],B8(p1^d1),o);
      vfr[1][0]=ldsr128(VL+1*2048); vfr[1][1]=ldsr128(VL+1*2048+1024);
      vfr[0][0]=ldsr128(VL+2*2048); vfr[0][1]=ldsr128(VL+2*2048+1024);
      ls=MFMA16(B8(ones),B8(p0),(f32x4{0,0,0,0})); ls=MFMA16(B8(ones),B8(p1),ls);
    } else if(u>1){
      const int t=u-1;
      o=MFMA32(vfr[t&1][0],B8(p0),o); o=MFMA32(vfr[t&1][1],B8(p1),o);
      if(t+2<8){ vfr[t&1][0]=ldsr128(VL+(t+2)*2048); vfr[t&1][1]=ldsr128(VL+(t+2)*2048+1024); }
      ls=MFMA16(B8(ones),B8(p0),ls); ls=MFMA16(B8(ones),B8(p1),ls);
    }
    if(u>1 && u<7 && !CHECK){
#pragma unroll
      for(int g_=0;g_<6;g_++){ __builtin_amdgcn_sched_group_barrier(0x8,1,0); __builtin_amdgcn_sched_group_barrier(0x402,4,0); }
    }
    if(u+1<8) PIN(s[(u+1)&1]);
    if(u>0){ PIN(ls); PIN(o); }
    if(u<8){ PIN(n0); PIN(n1); p0=n0; p1=n1; }
  }
  SBAR();
  arrive(cnt+(late?C_L:C_E));
  const float il=__builtin_amdgcn_rcpf(ls[0]);
  const unsigned oo=WS_O+(unsigned)(T*64+2*(2*C+late))*1024u+lane*16u;
  __builtin_amdgcn_raw_buffer_store_b128(pack8(o,0,il),wsr,oo,0,SC1);
  __builtin_amdgcn_raw_buffer_store_b128(pack8(o,1,il),wsr,oo+1024,0,SC1);
}

__global__ __launch_bounds__(512,2) NOPK void fused(const u16* Aq,const u16* Ak,const u16* Av,const u16* Ask,const u16* Ah,const u16* AD,
                                               const u16* X,const u16* W,const u16* WO,u16* out,char* ws){
  extern __shared__ __attribute__((aligned(16))) char lds[];
  const char* kpc; asm volatile("s_getpc_b64 %0":"=s"(kpc));
  asm volatile(""::"s"(X),"s"(W),"s"(ws),"s"(WO),"s"(Ak));
  const int tid=threadIdx.x, lane=tid&63; const int wid=__builtin_amdgcn_readfirstlane(tid>>6);
  const int b=blockIdx.x, xcd=b&7, j=b>>3;
  const int R=(xcd>>2)*8+(j>>2), C=(xcd&3)*4+(j&3);
  const int wgi=xcd*32+j;
  const int R2=((xcd>>1)^2)*4+(j>>3), C2=(xcd&1)*8+(j&7), KO=2*(j&7)+4*(j>>3);
  const unsigned lds0=(unsigned)(uintptr_t)lds;
  u64 ew=__hip_atomic_load((gu64*)(ws+F_E),RLX_AGENT);
  const unsigned a0=(unsigned)((lane>>3)*2048+(((lane&7)^(lane>>4))*16)), a1=(a0^64u)+(16384-1024);
  const unsigned b0=(unsigned)((lane>>3)*6144+(((lane&7)^(((lane>>4)&1)<<2))*16));
  const char* xb=(const char*)X+(size_t)R*128*2048; const char* wb=(const char*)W+(C&12)*128;
  const int cs[3]={(C&3)*128,2048+((C+1)&3)*128,4096+((C+2)&3)*128};
#pragma unroll
  for(int k=0;k<2;k++){
    asm volatile("s_mov_b32 m0, %3\n\ts_nop 0\n\tglobal_load_lds_dwordx4 %0, %2\n\tglobal_load_lds_dwordx4 %1, %2 offset:1024" :: "v"(a0),"v"(a1),"s"(xb+wid*32768+k*128),"s"(lds0+k*G1_SLOT+wid*2048) : "memory");
#pragma unroll
    for(int g=0;g<3;g++)
      asm volatile("s_mov_b32 m0, %2\n\ts_nop 0\n\tglobal_load_lds_dwordx4 %0, %1" :: "v"(b0),"s"(wb+cs[g]+wid*49152+k*(64*6144)),"s"(lds0+k*G1_SLOT+16384+g*8192+wid*1024) : "memory");
    if(k==0){ if(lane<2) touch((unsigned)((lane? 256+j : j*8+wid)*128),kpc,lds0+L_JUNK); }
    SBAR();
  }
  LAS unsigned* cnt=(LAS unsigned*)((lwptr)lds+L_CNT);
  const __amdgpu_buffer_rsrc_t wsr=__builtin_amdgcn_make_buffer_rsrc(ws,0,WS_END,0x00020000);
  const __amdgpu_buffer_rsrc_t outr=__builtin_amdgcn_make_buffer_rsrc(out,0,8*MB,0x00020000);
  u64 nonce;
#define LOAD_NONCE() do{ PIN(ew); nonce=((u64)__builtin_amdgcn_readfirstlane((unsigned)(ew>>32))<<32)|(unsigned)__builtin_amdgcn_readfirstlane((unsigned)ew); \
    nonce=nonce*6364136223846793005ull+1442695040888963407ull; }while(0)
  auto wo_block=[&](int i) NOPK __attribute__((always_inline)) {
    const int hd=2*((i+KO)&15)+(i>>4);
    const unsigned vo=(unsigned)((lane>>3)*2048+C2*128+(((lane&7)^(((lane>>4)&1)<<2))*16));
    dma4v(vo,vo+16384-1024,vo+2*(16384-1024),vo+3*(16384-1024),(const char*)WO+(size_t)hd*65536,lds0+L_WO+(31-i)*4096);
  };
  if(__builtin_expect(wid>=4,1)){
    const int v=wid-4;
    const unsigned junk=lds0+L_JUNK;
    if(__builtin_expect(wid==4,1)){
      xb+=256; wb+=2*64*6144;
      unsigned la=lds0+2*G1_SLOT;
      WAITV(6);
#pragma unroll 1
      for(int k=0;k<14;k++){
        WAITV(40); BAR();
#pragma unroll
        for(int g=0;g<4;g++){
          dma4v(a0,a1,a0+2*(16384-1024),a1+2*(16384-1024),xb+g*65536,la+g*4096);
          if(g<3){
            dma4v(b0,b0+(49152-1024),b0+2*(49152-1024),b0+3*(49152-1024),wb+cs[g],la+16384+g*8192);
            dma4v(b0,b0+(49152-1024),b0+2*(49152-1024),b0+3*(49152-1024),wb+cs[g]+32*6144,la+16384+g*8192+4096);
          }
        }
        xb+=128; wb+=64*6144; la+=G1_SLOT; if(la==lds0+3*G1_SLOT) la=lds0;
      }
      WAITV(40); BAR();
      WAITV(0); arrive(cnt+C_S15);
      LOAD_NONCE();
    } else if(wid==5){
      __builtin_amdgcn_s_setprio(3);
      const char* ta; unsigned tstep;
      { const int sg=lane/20, i=lane-sg*20; const bool isx=i<8; const int rem=i-8, kr=(xcd>>2)*32+j;
        tstep=isx?128u:64u*6144u;
        ta=(isx? (const char*)X+(size_t)((xcd>>2)*1024+((xcd&3)*32+j)*8+i)*2048 : (const char*)W+(kr*6144+(rem>>2)*2048+((xcd&3)*4+(rem&3))*128))+(size_t)tstep*(2+sg);
        tstep*=3; }
#define STOUCH(n) do{ if(lane<(n)) touch64(ta,junk); ta+=tstep; }while(0)
      STOUCH(60);
      if(j==0) *(LAS u32x4*)((lwptr)lds+L_CV+lane*16)=u32x4{0,0,0,0};
      if(lane<C_NUM) cnt[lane]=0u;
      { const int bq=lane&31, hi=lane>>5; u32x4 mk[2];
#pragma unroll
        for(int d=0;d<8;d++){ const int i=2*d; const int a=(i&3)+8*(i>>2)+4*hi; mk[d>>2][d&3]=((a<=bq)?0xffffu:0u)|((a+1<=bq)?0xffff0000u:0u); }
        *(LAS u32x4*)((lwptr)lds+L_MASK+lane*16)=mk[0]; *(LAS u32x4*)((lwptr)lds+L_MASK+1024+lane*16)=mk[1]; }
      WAITL();
      WAITV(7);
      BAR();
      WAITV(1);
      if(lane<6) asm volatile("s_mov_b32 m0, %2\n\ts_nop 0\n\tglobal_load_lds_dword %0, %1 sc1" :: "v"((unsigned)(F_W+(b*8+lane)*128)), "s"(ws), "s"(junk) : "memory");
      const unsigned pv=(unsigned)((lane>>4)*2048+(lane&15)*16);
      const char* ksrc=(const char*)Ak+(size_t)j*64*2048+xcd*256; const char* vsrc=(const char*)Av+(size_t)j*64*2048+xcd*256;
      if(j>0) asm volatile("s_mov_b32 m0, %2\n\ts_nop 0\n\tglobal_load_lds_dwordx4 %0, %1" :: "v"(pv^(7u<<5)), "s"(ksrc-4*2048), "s"(lds0+L_CV) : "memory");
      STOUCH(60); STOUCH(60);
      BAR();
      STOUCH(60); STOUCH(40);
      touch((unsigned)((wgi*64+lane)*128),WO,junk);
#pragma unroll 1
      for(int g2=0;g2<4;g2++){
        const unsigned p0=pv^(unsigned)(g2<<6), p1=p0^32u;
        dma4v(p0,p0+(8192-1024),p1+2*(8192-1024),p1+3*(8192-1024),ksrc+g2*32768,lds0+L_CV+1024+g2*4096);
        dma4v(p0,p0+(8192-1024),p1+2*(8192-1024),p1+3*(8192-1024),vsrc+g2*32768,lds0+L_CV+17408+g2*4096);
      }
      BAR();
      WAITV(0);
#pragma unroll 1
      for(int i=3;i<=12;i++) BAR();
      u64 fxv=__hip_atomic_load(flagp(ws,(lane&31)*8+xcd,FK_X),RLX_AGENT);
      BAR();
      SBAR(); PIN(fxv);
      LOAD_NONCE();
      if(allof(fxv==nonce)){ arrive(cnt+C_XF); WAITL(); }
      BAR();
      __builtin_amdgcn_s_setprio(0);
    } else {
      __builtin_amdgcn_s_setprio(1);
      const int hv=wid-6, rg=lane&7;
      u32x4 sk[4];
      WAITV(6);
      BAR();
      WAITV(0);
      LOAD_NONCE();
#pragma unroll
      for(int i=0;i<4;i++) sk[i]=*(const u32x4*)(Ask+(xcd*128+(hv*16+(i>>1)*8+(lane>>3))*4)*4+(i&1)*8);
      BAR(); BAR(); BAR();
      SBAR();
      asm volatile("":"+v"(sk[0]),"+v"(sk[1]),"+v"(sk[2]),"+v"(sk[3]));
#pragma unroll 1
      for(int g=0;g<2;g++){
        const int c4=hv*16+g*8+(lane>>3);
        u32x2 kk[11], vv[8];
        lptr kb=(lptr)lds+L_CV+(8*rg+4)*256+(c4&1)*8;
#pragma unroll
        for(int i=0;i<3;i++) kk[i]=*(const LAS u32x2*)(kb+(i-3)*256+(((c4>>1)^(((rg-1)&7)<<1))*16));
#pragma unroll
        for(int i=0;i<8;i++){ kk[3+i]=*(const LAS u32x2*)(kb+i*256+(((c4>>1)^(rg<<1))*16)); vv[i]=*(const LAS u32x2*)(kb+16384+i*256+(((c4>>1)^(rg<<1))*16)); }
#pragma unroll
        for(int c=0;c<4;c++){
          const unsigned w01=sk[c>>1][(c&1)*2], w23=sk[c>>1][(c&1)*2+1];
          const float w0=lo16(w01),w1=hi16(w01),w2=lo16(w23),w3=hi16(w23);
          float kf[11];
#pragma unroll
          for(int i=0;i<11;i++) kf[i]=(c&1)?hi16(kk[i][c>>1]):lo16(kk[i][c>>1]);
          float o[8];
#pragma unroll
          for(int i=0;i<8;i++){ float vf=(c&1)?hi16(vv[i][c>>1]):lo16(vv[i][c>>1]); o[i]=(kf[i]*w3+kf[i+1]*w2+kf[i+2]*w1+kf[i+3]*w0)*vf; }
          u32x4 r; r[0]=pk(o[0],o[1]); r[1]=pk(o[2],o[3]); r[2]=pk(o[4],o[5]); r[3]=pk(o[6],o[7]);
          __builtin_amdgcn_raw_buffer_store_b128(r,wsr,WS_XT+(unsigned)(xcd*128+c4*4+c)*4096u+(unsigned)(j*128+rg*16),0,SC1);
          SBAR();
          if(c!=1){ BAR(); SBAR(); }
        }
        sk[0]=sk[2]; sk[1]=sk[3];
      }
      WAITV(0);
      BAR();
      if(hv==0 && lane==0) __hip_atomic_store(flagp(ws,b,FK_X),nonce,RLX_AGENT);
      BAR(); BAR(); BAR(); BAR();
    }
    const int ch=wgi*4+v;
    const float Dc=lo16(AD[ch]);
    const bool xr=__builtin_amdgcn_readfirstlane(cnt[C_XF])!=0;
    dma4(lane*16,(const char*)Ah+(size_t)ch*4096,lds0+L_CV+v*CV_W+CV_H);
    if(xr) dma4_sc1(lane*16,ws+WS_XT+(size_t)ch*4096,lds0+L_CV+v*CV_W+CV_X);
    __builtin_amdgcn_s_setprio(2);
    if(!xr){ wait_flags<4>(flagp(ws,(lane&31)*8+xcd,FK_X),nonce); dma4_sc1(lane*16,ws+WS_XT+(size_t)ch*4096,lds0+L_CV+v*CV_W+CV_X); }
    WAITV(4);
    if(lane<32) *(LAS unsigned*)((lwptr)lds+L_CV+v*CV_W+lane*4)=0u;
    if(lane<40) *(LAS unsigned*)((lwptr)lds+L_CV+v*CV_W+CV_H+4096+lane*4)=0u;
    bool h1;
    {
      lwptr LW=(lwptr)lds+L_CV+v*CV_W;
      const int g=lane>>4,q=(lane&15)>>2,p=lane&3;
      lptr ha=(lptr)LW+CV_H+8*(4*p+8*g+q-12);
      lptr xa=(lptr)LW+CV_X+8*(p+8-8*g-q);
      PIN(ha); PIN(xa);
      bf16x8 A[16],B0[16],B1[16];
#pragma unroll
      for(int d=0;d<16;d++) A[d]=cat(ldstr(ha+256*d),ldstr(ha+256*d+32));
      WAITV(0);
      float zf=0.f; PIN(zf);
#define MM0(z,a,b) asm volatile("v_mfma_f32_16x16x32_bf16 %0, %1, %2, 0" : "=&v"(z) : "v"(a), "v"(b))
#define MMA(z,a,b) asm volatile("v_mfma_f32_16x16x32_bf16 %0, %1, %2, %0" : "+v"(z) : "v"(a), "v"(b))
#define LDB(P,e) B##P[e]=cat(ldstr(xa+256*(e)+128*(P)+32),ldstr(xa+256*(e)+128*(P)))
#define ROR2(d,s,z,e,t,y) asm volatile("v_add_f32_dpp %0, %2, %3 wave_ror:1 row_mask:0xf bank_mask:0xf\n\tv_add_f32_dpp %1, %4, %5 wave_ror:1 row_mask:0xf bank_mask:0xf" : "=&v"(d), "=&v"(e) : "v"(s), "v"(z), "v"(t), "v"(y))
#define SHR2(d,s,z,e,t,y) asm volatile("v_add_f32_dpp %0, %2, %3 wave_shr:1 row_mask:0xf bank_mask:0xf\n\tv_add_f32_dpp %1, %4, %5 wave_shr:1 row_mask:0xf bank_mask:0xf" : "+v"(d), "+v"(e) : "v"(s), "v"(z), "v"(t), "v"(y))
      const unsigned yo=WS_Y+(unsigned)ch*4096u+lane*16u;
#define STY(C) __builtin_amdgcn_raw_buffer_store_b128(yw[C],wsr,yo+(C)*1024,0,SC1)
#define NOP1() asm volatile("s_nop 0")
#define VNOP() __builtin_amdgcn_s_setprio(2)
      unsigned xa_,xb_;
      lptr xe=(lptr)LW+CV_X+2*lane; PIN(xe);
#define LDX(c) do{ xa_=*(const LAS u16*)(xe+256*(c)); xb_=*(const LAS u16*)(xe+256*(c)+128); }while(0)
#define SHX() do{ xa_<<=16; xb_<<=16; PIN(xa_); PIN(xb_); }while(0)
#define FMD(z,x) do{ z[0]=__builtin_fmaf(asf(x),Dc,z[0]); PIN(z); }while(0)
      const gu64* kf1=kvflag(ws,R,C,v>>1,1,lane);
      u64 f1v; unsigned c1v;
#define HOOK1() do{ f1v=__hip_atomic_load(kf1,RLX_AGENT); c1v=*(volatile LAS unsigned*)(cnt+C_KV1); }while(0)
#define HOOK2() do{ h1=allof(f1v==nonce) && __builtin_amdgcn_readfirstlane(c1v)>=2; MEMFENCE(); if(h1) kv_fetch(ws,lds0,R,C,v>>1,1,v&1,lane); }while(0)
      SBAR();
f32x4 z0,z1,z2,z3,z4,z5,z6,z7,z8,z9,z10,z11,z12,z13,z14,z15,z16,z17,z18,z19,z20,z21,z22,z23,z24,z25,z26,z27,z28,z29,z30,z31;
float t2_0,t1_0,u_0,t2_1,t1_1,u_1,t2_2,t1_2,u_2,t2_3,t1_3,u_3,t2_4,t1_4,u_4,t2_5,t1_5,u_5,t2_6,t1_6,u_6,t2_7,t1_7,u_7,t2_8,t1_8,u_8,t2_9,t1_9,u_9,t2_10,t1_10,u_10,t2_11,t1_11,u_11,t2_12,t1_12,u_12,t2_13,t1_13,u_13,t2_14,t1_14,u_14,t2_15,t1_15,u_15,t2_16,t1_16,u_16,t2_17,t1_17,u_17,t2_18,t1_18,u_18,t2_19,t1_19,u_19,t2_20,t1_20,u_20,t2_21,t1_21,u_21,t2_22,t1_22,u_22,t2_23,t1_23,u_23,t2_24,t1_24,u_24,t2_25,t1_25,u_25,t2_26,t1_26,u_26,t2_27,t1_27,u_27,t2_28,t1_28,u_28,t2_29,t1_29,u_29,t2_30,t1_30,u_30,t2_31,t1_31,u_31;
u32x4 yw[4];

LDB(0,0); LDB(1,0);
LDB(0,1); LDB(1,1);
LDB(0,2); LDB(1,2);
LDX(0);
SBAR();
MM0(z0,A[0],B0[0]); SBAR(); LDB(0,3); SBAR();
MM0(z1,A[0],B1[0]); SBAR(); LDB(1,3); SBAR();
MM0(z2,A[1],B0[0]); SBAR(); LDB(0,4); SBAR();
MM0(z3,A[1],B1[0]); SBAR(); LDB(1,4); SBAR();
MMA(z2,A[0],B0[1]); SBAR();  SBAR();
MMA(z3,A[0],B1[1]); SBAR();  SBAR();
MM0(z4,A[2],B0[0]); SBAR();  SBAR();
MM0(z5,A[2],B1[0]); SBAR();  SBAR();
MMA(z4,A[1],B0[1]); SBAR();  SBAR();
MMA(z5,A[1],B1[1]); SBAR();  SBAR();
MMA(z4,A[0],B0[2]); SBAR(); ROR2(t2_0,zf,z0[2],t2_1,z0[3],z1[2]); SBAR();
MMA(z5,A[0],B1[2]); SBAR(); LDB(0,5); SBAR();
MM0(z6,A[3],B0[0]); SBAR(); SHR2(t2_0,z0[3],z0[2],t2_1,z1[3],z1[2]); SBAR();
MM0(z7,A[3],B1[0]); SBAR(); LDB(1,5); SBAR();
MMA(z6,A[2],B0[1]); SBAR(); ROR2(t1_0,zf,z0[1],t1_1,t2_0,z1[1]); SBAR();
MMA(z7,A[2],B1[1]); SBAR(); SHX(); SBAR();
MMA(z6,A[1],B0[2]); SBAR(); SHR2(t1_0,t2_0,z0[1],t1_1,t2_1,z1[1]); SBAR();
MMA(z7,A[1],B1[2]); SBAR(); FMD(z0,xa_); FMD(z1,xb_); SBAR();
MMA(z6,A[0],B0[3]); SBAR(); ROR2(u_0,zf,z0[0],u_1,t1_0,z1[0]); SBAR();
MMA(z7,A[0],B1[3]); SBAR(); LDX(1); SBAR();
MM0(z8,A[4],B0[0]); SBAR(); SHR2(u_0,t1_0,z0[0],u_1,t1_1,z1[0]); SBAR();
MM0(z9,A[4],B1[0]); SBAR(); yw[0][0]=pk(u_0,u_1); PIN(yw[0]); SBAR();
MMA(z8,A[3],B0[1]); SBAR(); ROR2(t2_2,z1[3],z2[2],t2_3,z2[3],z3[2]); SBAR();
MMA(z9,A[3],B1[1]); SBAR(); LDB(0,6); SBAR();
MMA(z8,A[2],B0[2]); SBAR(); SHR2(t2_2,z2[3],z2[2],t2_3,z3[3],z3[2]); SBAR();
MMA(z9,A[2],B1[2]); SBAR(); LDB(1,6); SBAR();
MMA(z8,A[1],B0[3]); SBAR(); ROR2(t1_2,t2_1,z2[1],t1_3,t2_2,z3[1]); SBAR();
MMA(z9,A[1],B1[3]); SBAR(); SHX(); SBAR();
MMA(z8,A[0],B0[4]); SBAR(); SHR2(t1_2,t2_2,z2[1],t1_3,t2_3,z3[1]); SBAR();
MMA(z9,A[0],B1[4]); SBAR(); FMD(z2,xa_); FMD(z3,xb_); SBAR();
MM0(z10,A[5],B0[0]); SBAR(); ROR2(u_2,t1_1,z2[0],u_3,t1_2,z3[0]); SBAR();
MM0(z11,A[5],B1[0]); SBAR(); LDX(2); SBAR();
MMA(z10,A[4],B0[1]); SBAR(); SHR2(u_2,t1_2,z2[0],u_3,t1_3,z3[0]); SBAR();
MMA(z11,A[4],B1[1]); SBAR(); yw[0][1]=pk(u_2,u_3); PIN(yw[0]); SBAR();
MMA(z10,A[3],B0[2]); SBAR(); ROR2(t2_4,z3[3],z4[2],t2_5,z4[3],z5[2]); SBAR();
MMA(z11,A[3],B1[2]); SBAR(); LDB(0,7); SBAR();
MMA(z10,A[2],B0[3]); SBAR(); SHR2(t2_4,z4[3],z4[2],t2_5,z5[3],z5[2]); SBAR();
MMA(z11,A[2],B1[3]); SBAR(); LDB(1,7); SBAR();
MMA(z10,A[1],B0[4]); SBAR(); ROR2(t1_4,t2_3,z4[1],t1_5,t2_4,z5[1]); SBAR();
MMA(z11,A[1],B1[4]); SBAR(); SHX(); SBAR();
MMA(z10,A[0],B0[5]); SBAR(); SHR2(t1_4,t2_4,z4[1],t1_5,t2_5,z5[1]); SBAR();
MMA(z11,A[0],B1[5]); SBAR(); FMD(z4,xa_); FMD(z5,xb_); SBAR();
MM0(z12,A[6],B0[0]); SBAR(); ROR2(u_4,t1_3,z4[0],u_5,t1_4,z5[0]); SBAR();
MM0(z13,A[6],B1[0]); SBAR(); LDX(3); SBAR();
MMA(z12,A[5],B0[1]); SBAR(); SHR2(u_4,t1_4,z4[0],u_5,t1_5,z5[0]); SBAR();
MMA(z13,A[5],B1[1]); SBAR(); yw[0][2]=pk(u_4,u_5); PIN(yw[0]); SBAR();
MMA(z12,A[4],B0[2]); SBAR(); ROR2(t2_6,z5[3],z6[2],t2_7,z6[3],z7[2]); SBAR();
MMA(z13,A[4],B1[2]); SBAR(); LDB(0,8); SBAR();
MMA(z12,A[3],B0[3]); SBAR(); SHR2(t2_6,z6[3],z6[2],t2_7,z7[3],z7[2]); SBAR();
MMA(z13,A[3],B1[3]); SBAR(); LDB(1,8); SBAR();
MMA(z12,A[2],B0[4]); SBAR(); ROR2(t1_6,t2_5,z6[1],t1_7,t2_6,z7[1]); SBAR();
MMA(z13,A[2],B1[4]); SBAR(); SHX(); SBAR();
MMA(z12,A[1],B0[5]); SBAR(); SHR2(t1_6,t2_6,z6[1],t1_7,t2_7,z7[1]); SBAR();
MMA(z13,A[1],B1[5]); SBAR(); FMD(z6,xa_); FMD(z7,xb_); SBAR();
MMA(z12,A[0],B0[6]); SBAR(); ROR2(u_6,t1_5,z6[0],u_7,t1_6,z7[0]); SBAR();
MMA(z13,A[0],B1[6]); SBAR(); LDX(4); SBAR();
MM0(z14,A[7],B0[0]); SBAR(); SHR2(u_6,t1_6,z6[0],u_7,t1_7,z7[0]); SBAR();
MM0(z15,A[7],B1[0]); SBAR(); yw[0][3]=pk(u_6,u_7); PIN(yw[0]); STY(0); SBAR();
MMA(z14,A[6],B0[1]); SBAR(); ROR2(t2_8,z7[3],z8[2],t2_9,z8[3],z9[2]); SBAR();
MMA(z15,A[6],B1[1]); SBAR(); LDB(0,9); SBAR();
MMA(z14,A[5],B0[2]); SBAR(); SHR2(t2_8,z8[3],z8[2],t2_9,z9[3],z9[2]); SBAR();
MMA(z15,A[5],B1[2]); SBAR(); LDB(1,9); SBAR();
MMA(z14,A[4],B0[3]); SBAR(); ROR2(t1_8,t2_7,z8[1],t1_9,t2_8,z9[1]); SBAR();
MMA(z15,A[4],B1[3]); SBAR(); SHX(); SBAR();
MMA(z14,A[3],B0[4]); SBAR(); SHR2(t1_8,t2_8,z8[1],t1_9,t2_9,z9[1]); SBAR();
MMA(z15,A[3],B1[4]); SBAR(); FMD(z8,xa_); FMD(z9,xb_); SBAR();
MMA(z14,A[2],B0[5]); SBAR(); ROR2(u_8,t1_7,z8[0],u_9,t1_8,z9[0]); SBAR();
MMA(z15,A[2],B1[5]); SBAR(); LDX(5); SBAR();
MMA(z14,A[1],B0[6]); SBAR(); SHR2(u_8,t1_8,z8[0],u_9,t1_9,z9[0]); SBAR();
MMA(z15,A[1],B1[6]); SBAR(); yw[1][0]=pk(u_8,u_9); PIN(yw[1]); SBAR();
MMA(z14,A[0],B0[7]); SBAR(); ROR2(t2_10,z9[3],z10[2],t2_11,z10[3],z11[2]); SBAR();
MMA(z15,A[0],B1[7]); SBAR(); LDB(0,10); SBAR();
MM0(z16,A[8],B0[0]); SBAR(); SHR2(t2_10,z10[3],z10[2],t2_11,z11[3],z11[2]); SBAR();
MM0(z17,A[8],B1[0]); SBAR(); LDB(1,10); SBAR();
MMA(z16,A[7],B0[1]); SBAR(); ROR2(t1_10,t2_9,z10[1],t1_11,t2_10,z11[1]); SBAR();
MMA(z17,A[7],B1[1]); SBAR(); SHX(); SBAR();
MMA(z16,A[6],B0[2]); SBAR(); SHR2(t1_10,t2_10,z10[1],t1_11,t2_11,z11[1]); SBAR();
MMA(z17,A[6],B1[2]); SBAR(); FMD(z10,xa_); FMD(z11,xb_); SBAR();
MMA(z16,A[5],B0[3]); SBAR(); ROR2(u_10,t1_9,z10[0],u_11,t1_10,z11[0]); SBAR();
MMA(z17,A[5],B1[3]); SBAR(); LDX(6); SBAR();
MMA(z16,A[4],B0[4]); SBAR(); SHR2(u_10,t1_10,z10[0],u_11,t1_11,z11[0]); SBAR();
MMA(z17,A[4],B1[4]); SBAR(); yw[1][1]=pk(u_10,u_11); PIN(yw[1]); SBAR();
MMA(z16,A[3],B0[5]); SBAR(); ROR2(t2_12,z11[3],z12[2],t2_13,z12[3],z13[2]); SBAR();
MMA(z17,A[3],B1[5]); SBAR(); LDB(0,11); SBAR();
MMA(z16,A[2],B0[6]); SBAR(); SHR2(t2_12,z12[3],z12[2],t2_13,z13[3],z13[2]); SBAR();
MMA(z17,A[2],B1[6]); SBAR(); LDB(1,11); SBAR();
MMA(z16,A[1],B0[7]); SBAR(); ROR2(t1_12,t2_11,z12[1],t1_13,t2_12,z13[1]); SBAR();
MMA(z17,A[1],B1[7]); SBAR(); SHX(); SBAR();
MMA(z16,A[0],B0[8]); SBAR(); SHR2(t1_12,t2_12,z12[1],t1_13,t2_13,z13[1]); SBAR();
MMA(z17,A[0],B1[8]); SBAR(); FMD(z12,xa_); FMD(z13,xb_); SBAR();
MM0(z18,A[9],B0[0]); SBAR(); ROR2(u_12,t1_11,z12[0],u_13,t1_12,z13[0]); SBAR();
MM0(z19,A[9],B1[0]); SBAR(); LDX(7); SBAR();
MMA(z18,A[8],B0[1]); SBAR(); SHR2(u_12,t1_12,z12[0],u_13,t1_13,z13[0]); SBAR();
MMA(z19,A[8],B1[1]); SBAR(); yw[1][2]=pk(u_12,u_13); PIN(yw[1]); SBAR();
MMA(z18,A[7],B0[2]); SBAR(); ROR2(t2_14,z13[3],z14[2],t2_15,z14[3],z15[2]); SBAR();
MMA(z19,A[7],B1[2]); SBAR(); LDB(0,12); SBAR();
MMA(z18,A[6],B0[3]); SBAR(); SHR2(t2_14,z14[3],z14[2],t2_15,z15[3],z15[2]); SBAR();
MMA(z19,A[6],B1[3]); SBAR(); LDB(1,12); SBAR();
MMA(z18,A[5],B0[4]); SBAR(); ROR2(t1_14,t2_13,z14[1],t1_15,t2_14,z15[1]); SBAR();
MMA(z19,A[5],B1[4]); SBAR(); SHX(); SBAR();
MMA(z18,A[4],B0[5]); SBAR(); SHR2(t1_14,t2_14,z14[1],t1_15,t2_15,z15[1]); SBAR();
MMA(z19,A[4],B1[5]); SBAR(); FMD(z14,xa_); FMD(z15,xb_); SBAR();
MMA(z18,A[3],B0[6]); SBAR(); ROR2(u_14,t1_13,z14[0],u_15,t1_14,z15[0]); SBAR();
MMA(z19,A[3],B1[6]); SBAR(); LDX(8); SBAR();
MMA(z18,A[2],B0[7]); SBAR(); SHR2(u_14,t1_14,z14[0],u_15,t1_15,z15[0]); SBAR();
MMA(z19,A[2],B1[7]); SBAR(); yw[1][3]=pk(u_14,u_15); PIN(yw[1]); STY(1); SBAR();
MMA(z18,A[1],B0[8]); SBAR(); ROR2(t2_16,z15[3],z16[2],t2_17,z16[3],z17[2]); SBAR();
MMA(z19,A[1],B1[8]); SBAR(); LDB(0,13); SBAR();
MMA(z18,A[0],B0[9]); SBAR(); SHR2(t2_16,z16[3],z16[2],t2_17,z17[3],z17[2]); SBAR();
MMA(z19,A[0],B1[9]); SBAR(); LDB(1,13); SBAR();
MM0(z20,A[10],B0[0]); SBAR(); ROR2(t1_16,t2_15,z16[1],t1_17,t2_16,z17[1]); SBAR();
MM0(z21,A[10],B1[0]); SBAR(); SHX(); SBAR();
MMA(z20,A[9],B0[1]); SBAR(); SHR2(t1_16,t2_16,z16[1],t1_17,t2_17,z17[1]); SBAR();
MMA(z21,A[9],B1[1]); SBAR(); FMD(z16,xa_); FMD(z17,xb_); SBAR();
MMA(z20,A[8],B0[2]); SBAR(); ROR2(u_16,t1_15,z16[0],u_17,t1_16,z17[0]); SBAR();
MMA(z21,A[8],B1[2]); SBAR(); LDX(9); SBAR();
MMA(z20,A[7],B0[3]); SBAR(); SHR2(u_16,t1_16,z16[0],u_17,t1_17,z17[0]); SBAR();
MMA(z21,A[7],B1[3]); SBAR(); yw[2][0]=pk(u_16,u_17); PIN(yw[2]); SBAR();
MMA(z20,A[6],B0[4]); SBAR(); ROR2(t2_18,z17[3],z18[2],t2_19,z18[3],z19[2]); SBAR();
MMA(z21,A[6],B1[4]); SBAR(); LDB(0,14); SBAR();
MMA(z20,A[5],B0[5]); SBAR(); SHR2(t2_18,z18[3],z18[2],t2_19,z19[3],z19[2]); SBAR();
MMA(z21,A[5],B1[5]); SBAR(); LDB(1,14); SBAR();
MMA(z20,A[4],B0[6]); SBAR(); ROR2(t1_18,t2_17,z18[1],t1_19,t2_18,z19[1]); SBAR();
MMA(z21,A[4],B1[6]); SBAR(); SHX(); SBAR();
MMA(z20,A[3],B0[7]); SBAR(); SHR2(t1_18,t2_18,z18[1],t1_19,t2_19,z19[1]); SBAR();
MMA(z21,A[3],B1[7]); SBAR(); FMD(z18,xa_); FMD(z19,xb_); SBAR();
MMA(z20,A[2],B0[8]); SBAR(); ROR2(u_18,t1_17,z18[0],u_19,t1_18,z19[0]); SBAR();
MMA(z21,A[2],B1[8]); SBAR(); LDX(10); SBAR();
MMA(z20,A[1],B0[9]); SBAR(); SHR2(u_18,t1_18,z18[0],u_19,t1_19,z19[0]); SBAR();
MMA(z21,A[1],B1[9]); SBAR(); yw[2][1]=pk(u_18,u_19); PIN(yw[2]); SBAR();
MMA(z20,A[0],B0[10]); SBAR();  SBAR();
MMA(z21,A[0],B1[10]); SBAR();  SBAR();
MM0(z22,A[11],B0[0]); SBAR();  SBAR();
MM0(z23,A[11],B1[0]); SBAR();  SBAR();
MMA(z22,A[10],B0[1]); SBAR();  SBAR();
MMA(z23,A[10],B1[1]); SBAR();  SBAR();
MMA(z22,A[9],B0[2]); SBAR();  SBAR();
MMA(z23,A[9],B1[2]); SBAR();  SBAR();
MMA(z22,A[8],B0[3]); SBAR();  SBAR();
MMA(z23,A[8],B1[3]); SBAR();  SBAR();
MMA(z22,A[7],B0[4]); SBAR(); ROR2(t2_20,z19[3],z20[2],t2_21,z20[3],z21[2]); SBAR();
MMA(z23,A[7],B1[4]); SBAR(); LDB(0,15); SBAR();
MMA(z22,A[6],B0[5]); SBAR(); SHR2(t2_20,z20[3],z20[2],t2_21,z21[3],z21[2]); SBAR();
MMA(z23,A[6],B1[5]); SBAR(); LDB(1,15); SBAR();
MMA(z22,A[5],B0[6]); SBAR(); ROR2(t1_20,t2_19,z20[1],t1_21,t2_20,z21[1]); SBAR();
MMA(z23,A[5],B1[6]); SBAR(); SHX(); SBAR();
MMA(z22,A[4],B0[7]); SBAR(); SHR2(t1_20,t2_20,z20[1],t1_21,t2_21,z21[1]); SBAR();
MMA(z23,A[4],B1[7]); SBAR(); FMD(z20,xa_); FMD(z21,xb_); SBAR();
MMA(z22,A[3],B0[8]); SBAR(); ROR2(u_20,t1_19,z20[0],u_21,t1_20,z21[0]); SBAR();
MMA(z23,A[3],B1[8]); SBAR(); LDX(11); SBAR();
MMA(z22,A[2],B0[9]); SBAR(); SHR2(u_20,t1_20,z20[0],u_21,t1_21,z21[0]); SBAR();
MMA(z23,A[2],B1[9]); SBAR(); yw[2][2]=pk(u_20,u_21); PIN(yw[2]); SBAR();
MMA(z22,A[1],B0[10]); SBAR();  SBAR();
MMA(z23,A[1],B1[10]); SBAR();  SBAR();
MMA(z22,A[0],B0[11]); SBAR();  SBAR();
MMA(z23,A[0],B1[11]); SBAR();  SBAR();
MM0(z24,A[12],B0[0]); SBAR();  SBAR();
MM0(z25,A[12],B1[0]); SBAR();  SBAR();
MMA(z24,A[11],B0[1]); SBAR();  SBAR();
MMA(z25,A[11],B1[1]); SBAR();  SBAR();
MMA(z24,A[10],B0[2]); SBAR();  SBAR();
MMA(z25,A[10],B1[2]); SBAR();  SBAR();
MMA(z24,A[9],B0[3]); SBAR();  SBAR();
MMA(z25,A[9],B1[3]); SBAR();  SBAR();
MMA(z24,A[8],B0[4]); SBAR(); ROR2(t2_22,z21[3],z22[2],t2_23,z22[3],z23[2]); SBAR();
MMA(z25,A[8],B1[4]); SBAR(); VNOP(); SBAR();
MMA(z24,A[7],B0[5]); SBAR(); SHR2(t2_22,z22[3],z22[2],t2_23,z23[3],z23[2]); SBAR();
MMA(z25,A[7],B1[5]); SBAR(); VNOP(); SBAR();
MMA(z24,A[6],B0[6]); SBAR(); ROR2(t1_22,t2_21,z22[1],t1_23,t2_22,z23[1]); SBAR();
MMA(z25,A[6],B1[6]); SBAR(); SHX(); SBAR();
MMA(z24,A[5],B0[7]); SBAR(); SHR2(t1_22,t2_22,z22[1],t1_23,t2_23,z23[1]); SBAR();
MMA(z25,A[5],B1[7]); SBAR(); FMD(z22,xa_); FMD(z23,xb_); SBAR();
MMA(z24,A[4],B0[8]); SBAR(); ROR2(u_22,t1_21,z22[0],u_23,t1_22,z23[0]); SBAR();
MMA(z25,A[4],B1[8]); SBAR(); LDX(12); SBAR();
MMA(z24,A[3],B0[9]); SBAR(); SHR2(u_22,t1_22,z22[0],u_23,t1_23,z23[0]); SBAR();
MMA(z25,A[3],B1[9]); SBAR(); yw[2][3]=pk(u_22,u_23); PIN(yw[2]); STY(2); SBAR();
MMA(z24,A[2],B0[10]); SBAR();  SBAR();
MMA(z25,A[2],B1[10]); SBAR();  SBAR();
MMA(z24,A[1],B0[11]); SBAR();  SBAR();
MMA(z25,A[1],B1[11]); SBAR();  SBAR();
MMA(z24,A[0],B0[12]); SBAR();  SBAR();
MMA(z25,A[0],B1[12]); SBAR();  SBAR();
MM0(z26,A[13],B0[0]); SBAR();  SBAR();
MM0(z27,A[13],B1[0]); SBAR();  SBAR();
MMA(z26,A[12],B0[1]); SBAR();  SBAR();
MMA(z27,A[12],B1[1]); SBAR();  SBAR();
MMA(z26,A[11],B0[2]); SBAR();  SBAR();
MMA(z27,A[11],B1[2]); SBAR();  SBAR();
MMA(z26,A[10],B0[3]); SBAR();  SBAR();
MMA(z27,A[10],B1[3]); SBAR();  SBAR();
MMA(z26,A[9],B0[4]); SBAR(); ROR2(t2_24,z23[3],z24[2],t2_25,z24[3],z25[2]); SBAR();
MMA(z27,A[9],B1[4]); SBAR(); VNOP(); SBAR();
MMA(z26,A[8],B0[5]); SBAR(); SHR2(t2_24,z24[3],z24[2],t2_25,z25[3],z25[2]); SBAR();
MMA(z27,A[8],B1[5]); SBAR(); VNOP(); SBAR();
MMA(z26,A[7],B0[6]); SBAR(); ROR2(t1_24,t2_23,z24[1],t1_25,t2_24,z25[1]); SBAR();
MMA(z27,A[7],B1[6]); SBAR(); SHX(); SBAR();
MMA(z26,A[6],B0[7]); SBAR(); SHR2(t1_24,t2_24,z24[1],t1_25,t2_25,z25[1]); SBAR();
MMA(z27,A[6],B1[7]); SBAR(); FMD(z24,xa_); FMD(z25,xb_); SBAR();
MMA(z26,A[5],B0[8]); SBAR(); ROR2(u_24,t1_23,z24[0],u_25,t1_24,z25[0]); SBAR();
MMA(z27,A[5],B1[8]); SBAR(); LDX(13); SBAR();
MMA(z26,A[4],B0[9]); SBAR(); SHR2(u_24,t1_24,z24[0],u_25,t1_25,z25[0]); SBAR();
MMA(z27,A[4],B1[9]); SBAR(); yw[3][0]=pk(u_24,u_25); PIN(yw[3]); SBAR();
MMA(z26,A[3],B0[10]); SBAR();  SBAR();
MMA(z27,A[3],B1[10]); SBAR();  SBAR();
MMA(z26,A[2],B0[11]); SBAR();  SBAR();
MMA(z27,A[2],B1[11]); SBAR();  SBAR();
MMA(z26,A[1],B0[12]); SBAR();  SBAR();
MMA(z27,A[1],B1[12]); SBAR();  SBAR();
MMA(z26,A[0],B0[13]); SBAR();  SBAR();
MMA(z27,A[0],B1[13]); SBAR();  SBAR();
MM0(z28,A[14],B0[0]); SBAR();  SBAR();
MM0(z29,A[14],B1[0]); SBAR();  SBAR();
MMA(z28,A[13],B0[1]); SBAR();  SBAR();
MMA(z29,A[13],B1[1]); SBAR();  SBAR();
MMA(z28,A[12],B0[2]); SBAR();  SBAR();
MMA(z29,A[12],B1[2]); SBAR();  SBAR();
MMA(z28,A[11],B0[3]); SBAR();  SBAR();
MMA(z29,A[11],B1[3]); SBAR();  SBAR();
MMA(z28,A[10],B0[4]); SBAR(); ROR2(t2_26,z25[3],z26[2],t2_27,z26[3],z27[2]); SBAR();
MMA(z29,A[10],B1[4]); SBAR(); VNOP(); SBAR();
MMA(z28,A[9],B0[5]); SBAR(); SHR2(t2_26,z26[3],z26[2],t2_27,z27[3],z27[2]); SBAR();
MMA(z29,A[9],B1[5]); SBAR(); VNOP(); SBAR();
MMA(z28,A[8],B0[6]); SBAR(); ROR2(t1_26,t2_25,z26[1],t1_27,t2_26,z27[1]); SBAR();
MMA(z29,A[8],B1[6]); SBAR(); SHX(); SBAR();
MMA(z28,A[7],B0[7]); SBAR(); SHR2(t1_26,t2_26,z26[1],t1_27,t2_27,z27[1]); SBAR();
MMA(z29,A[7],B1[7]); SBAR(); FMD(z26,xa_); FMD(z27,xb_); SBAR();
MMA(z28,A[6],B0[8]); SBAR(); ROR2(u_26,t1_25,z26[0],u_27,t1_26,z27[0]); SBAR();
MMA(z29,A[6],B1[8]); SBAR(); LDX(14); SBAR();
MMA(z28,A[5],B0[9]); SBAR(); SHR2(u_26,t1_26,z26[0],u_27,t1_27,z27[0]); SBAR();
MMA(z29,A[5],B1[9]); SBAR(); yw[3][1]=pk(u_26,u_27); PIN(yw[3]); SBAR();
MMA(z28,A[4],B0[10]); SBAR();  SBAR();
MMA(z29,A[4],B1[10]); SBAR();  SBAR();
MMA(z28,A[3],B0[11]); SBAR();  SBAR();
MMA(z29,A[3],B1[11]); SBAR();  SBAR();
MMA(z28,A[2],B0[12]); SBAR();  SBAR();
MMA(z29,A[2],B1[12]); SBAR();  SBAR();
MMA(z28,A[1],B0[13]); SBAR();  SBAR();
MMA(z29,A[1],B1[13]); SBAR();  SBAR();
MMA(z28,A[0],B0[14]); SBAR();  SBAR();
MMA(z29,A[0],B1[14]); SBAR();  SBAR();
MM0(z30,A[15],B0[0]); SBAR();  SBAR();
MM0(z31,A[15],B1[0]); SBAR();  SBAR();
MMA(z30,A[14],B0[1]); SBAR();  SBAR();
MMA(z31,A[14],B1[1]); SBAR();  SBAR();
MMA(z30,A[13],B0[2]); SBAR();  SBAR();
MMA(z31,A[13],B1[2]); SBAR();  SBAR();
MMA(z30,A[12],B0[3]); SBAR();  SBAR();
MMA(z31,A[12],B1[3]); SBAR();  SBAR();
MMA(z30,A[11],B0[4]); SBAR(); ROR2(t2_28,z27[3],z28[2],t2_29,z28[3],z29[2]); SBAR();
MMA(z31,A[11],B1[4]); SBAR(); VNOP(); SBAR();
MMA(z30,A[10],B0[5]); SBAR(); SHR2(t2_28,z28[3],z28[2],t2_29,z29[3],z29[2]); SBAR();
HOOK1(); SBAR();
MMA(z31,A[10],B1[5]); SBAR(); VNOP(); SBAR();
MMA(z30,A[9],B0[6]); SBAR(); ROR2(t1_28,t2_27,z28[1],t1_29,t2_28,z29[1]); SBAR();
MMA(z31,A[9],B1[6]); SBAR(); SHX(); SBAR();
MMA(z30,A[8],B0[7]); SBAR(); SHR2(t1_28,t2_28,z28[1],t1_29,t2_29,z29[1]); SBAR();
MMA(z31,A[8],B1[7]); SBAR(); FMD(z28,xa_); FMD(z29,xb_); SBAR();
MMA(z30,A[7],B0[8]); SBAR(); ROR2(u_28,t1_27,z28[0],u_29,t1_28,z29[0]); SBAR();
MMA(z31,A[7],B1[8]); SBAR(); LDX(15); SBAR();
MMA(z30,A[6],B0[9]); SBAR(); SHR2(u_28,t1_28,z28[0],u_29,t1_29,z29[0]); SBAR();
MMA(z31,A[6],B1[9]); SBAR(); yw[3][2]=pk(u_28,u_29); PIN(yw[3]); SBAR();
MMA(z30,A[5],B0[10]); SBAR();  SBAR();
MMA(z31,A[5],B1[10]); SBAR();  SBAR();
MMA(z30,A[4],B0[11]); SBAR();  SBAR();
MMA(z31,A[4],B1[11]); SBAR();  SBAR();
MMA(z30,A[3],B0[12]); SBAR();  SBAR();
MMA(z31,A[3],B1[12]); SBAR();  SBAR();
MMA(z30,A[2],B0[13]); SBAR();  SBAR();
MMA(z31,A[2],B1[13]); SBAR();  SBAR();
MMA(z30,A[1],B0[14]); SBAR();  SBAR();
MMA(z31,A[1],B1[14]); SBAR();  SBAR();
MMA(z30,A[0],B0[15]); SBAR();  SBAR();
MMA(z31,A[0],B1[15]); SBAR();  SBAR();
NOP1(); SBAR();  SBAR();
NOP1(); SBAR();  SBAR();
NOP1(); SBAR();  SBAR();
NOP1(); SBAR();  SBAR();
NOP1(); SBAR();  SBAR();
NOP1(); SBAR();  SBAR();
NOP1(); SBAR();  SBAR();
NOP1(); SBAR();  SBAR();
NOP1(); SBAR(); ROR2(t2_30,z29[3],z30[2],t2_31,z30[3],z31[2]); SBAR();
NOP1(); SBAR(); VNOP(); SBAR();
NOP1(); SBAR(); SHR2(t2_30,z30[3],z30[2],t2_31,z31[3],z31[2]); SBAR();
NOP1(); SBAR(); VNOP(); SBAR();
NOP1(); SBAR(); ROR2(t1_30,t2_29,z30[1],t1_31,t2_30,z31[1]); SBAR();
NOP1(); SBAR(); SHX(); SBAR();
NOP1(); SBAR(); SHR2(t1_30,t2_30,z30[1],t1_31,t2_31,z31[1]); SBAR();
NOP1(); SBAR(); FMD(z30,xa_); FMD(z31,xb_); SBAR();
NOP1(); SBAR(); ROR2(u_30,t1_29,z30[0],u_31,t1_30,z31[0]); SBAR();
NOP1(); SBAR(); VNOP(); SBAR();
NOP1(); SBAR(); SHR2(u_30,t1_30,z30[0],u_31,t1_31,z31[0]); SBAR();
NOP1(); SBAR(); yw[3][3]=pk(u_30,u_31); PIN(yw[3]); STY(3); SBAR();
      if(v==3 && lane<32) touch((unsigned)((j*32+lane)*2048+((xcd&1)*8+((3-(int)((uintptr_t)WO>>7))&7))*128),WO,junk);
      HOOK2();
    }
    if(!h1){
      wait_flags<4>(kvflag(ws,R,C,v>>1,1,lane),nonce);
      lds_wait(cnt+C_KV1,2);
      kv_fetch(ws,lds0,R,C,v>>1,1,v&1,lane);
    }
    WAITV(0);
    arrive_publish(cnt+C_H1,4,flagp(ws,b,FK_Y),nonce,lane);
    lds_wait(cnt+C_H1,4); lds_wait(cnt+C_P1E,4);
#pragma unroll 1
    for(int i=v;i<WO_P1;i+=4) wo_block(i);
    if(v<2) touch((unsigned)((512*(j>>3)+8*(j&7)+64*(lane>>3)+(lane&7))*2048+(xcd*2+v)*128),Aq,junk);
    __builtin_amdgcn_s_setprio(0);
  } else {
    const int wr=wid>>1, wc=wid&1;
    const int r=lane&31, h=lane>>5;
    lptr L=(lptr)lds;
    lptr xa[4];
    { int row=wr*64+r; int sw=(row>>1)&7;
#pragma unroll
      for(int ks=0;ks<4;ks++) xa[ks]=L+row*128+(((2*ks+h)^sw)*16); }
    lptr wa;
    { int blk=(lane>>4)&1,q=(lane&15)>>2,p=lane&3; int c=(wc*4+2*blk+(p>>1))^(((q>>1)&1)<<2); wa=L+16384+(8*h+q)*128+c*16+8*(p&1); }
    f32x16 aq[2],ak[2],av[2];
#pragma unroll
    for(int i=0;i<2;i++){ aq[i]=f32x16{}; ak[i]=f32x16{}; av[i]=f32x16{}; }
    G1F F0,F1;
#define G1_RX(F,ks) do{ F.x0=ldsr128(xa[ks]+so); F.x1=ldsr128(xa[ks]+so+4096); }while(0)
#define G1_RW(F,ks,sg) do{ F.w[2*(sg)]=ldstr(wa+so+(sg)*8192+(16*(ks))*128); F.w[2*(sg)+1]=ldstr(wa+so+(sg)*8192+(16*(ks)+4)*128); }while(0)
#define G1_W(F,sg) cat(F.w[2*(sg)],F.w[2*(sg)+1])
#define G1_STEP(F,N,ks,LD) do{ \
    ak[0]=MFMA32(G1_W(F,1),F.x0,ak[0]); PIN(ak[0]); if(LD) G1_RX(N,ks); SBAR(); \
    ak[1]=MFMA32(G1_W(F,1),F.x1,ak[1]); PIN(ak[1]); if(LD) G1_RW(N,ks,1); SBAR(); \
    av[0]=MFMA32(F.x0,G1_W(F,2),av[0]); PIN(av[0]); if(LD) G1_RW(N,ks,2); SBAR(); \
    av[1]=MFMA32(F.x1,G1_W(F,2),av[1]); PIN(av[1]); if(LD) G1_RW(N,ks,0); SBAR(); \
    aq[0]=MFMA32(G1_W(F,0),F.x0,aq[0]); PIN(aq[0]); SBAR(); \
    aq[1]=MFMA32(G1_W(F,0),F.x1,aq[1]); PIN(aq[1]); SBAR(); }while(0)
    __builtin_amdgcn_s_setprio(3);
    asm volatile("":"+v"(aq[0]),"+v"(aq[1]),"+v"(ak[0]),"+v"(ak[1]),"+v"(av[0]),"+v"(av[1]));
    WAITV(6);
    BAR();
    int so=0;
    G1_RX(F0,0); G1_RW(F0,0,1); G1_RW(F0,0,2); G1_RW(F0,0,0); SBAR();
#pragma unroll 1
    for(int t=0;t<14;t++){
      G1_STEP(F0,F1,1,true);
      G1_STEP(F1,F0,2,true);
      G1_STEP(F0,F1,3,true);
      so=(so==2*G1_SLOT)?0:so+G1_SLOT;
      if(t==0) WAITV(0);
      WAITL(); BAR(); SBAR();
      G1_STEP(F1,F0,0,true);
    }
    G1F E[8]; E[0]=F0;
#define G1_KV(n,LD) do{ \
    ak[0]=MFMA32(G1_W(E[n],1),E[n].x0,ak[0]); PIN(ak[0]); if(LD) G1_RX(E[(n)+1],((n)+1)&3); SBAR(); \
    ak[1]=MFMA32(G1_W(E[n],1),E[n].x1,ak[1]); PIN(ak[1]); if(LD) G1_RW(E[(n)+1],((n)+1)&3,1); SBAR(); \
    av[0]=MFMA32(E[n].x0,G1_W(E[n],2),av[0]); PIN(av[0]); if(LD) G1_RW(E[(n)+1],((n)+1)&3,2); SBAR(); \
    av[1]=MFMA32(E[n].x1,G1_W(E[n],2),av[1]); PIN(av[1]); if(LD) G1_RW(E[(n)+1],((n)+1)&3,0); SBAR(); }while(0)
#define G1_Q(n) do{ aq[0]=MFMA32(G1_W(E[n],0),E[n].x0,aq[0]); PIN(aq[0]); aq[1]=MFMA32(G1_W(E[n],0),E[n].x1,aq[1]); PIN(aq[1]); SBAR(); }while(0)
    G1_KV(0,true); G1_KV(1,true); G1_KV(2,true);
    LOAD_NONCE();
    so=0;
    lds_wait(cnt+C_S15,1); SBAR();
    G1_KV(3,true); G1_KV(4,true); G1_KV(5,true); G1_KV(6,true);
    arrive(cnt+C_RD);
    SBAR();
    { const int n=7; ak[0]=MFMA32(G1_W(E[n],1),E[n].x0,ak[0]); ak[1]=MFMA32(G1_W(E[n],1),E[n].x1,ak[1]); av[0]=MFMA32(E[n].x0,G1_W(E[n],2),av[0]); av[1]=MFMA32(E[n].x1,G1_W(E[n],2),av[1]);
      PIN(ak[0]); PIN(ak[1]); PIN(av[0]); PIN(av[1]); SBAR(); }
    const float qs=0.17677669529663687f*1.4426950408889634f;
    u32x4 kf[2][2],vf[2][2];
    const unsigned go=WS_KV+(unsigned)b*32768u+wc*16384u+(2*wr)*2048u+lane*16u;
#define KV_STORE() _Pragma("unroll") for(int rt=0;rt<2;rt++) _Pragma("unroll") for(int s=0;s<2;s++) __builtin_amdgcn_raw_buffer_store_b128(kf[rt][s],wsr,go+rt*2048+s*1024,0,SC1); \
      _Pragma("unroll") for(int rt=0;rt<2;rt++) _Pragma("unroll") for(int s=0;s<2;s++) __builtin_amdgcn_raw_buffer_store_b128(vf[rt][s],wsr,go+8192+rt*2048+s*1024,0,SC1)
    G1_Q(0);
    kf[0][0]=pack8(ak[0],0); kf[0][1]=pack8(ak[0],1); PIN(kf[0][0]); PIN(kf[0][1]);
    kf[1][0]=pack8(ak[1],0); kf[1][1]=pack8(ak[1],1); PIN(kf[1][0]); PIN(kf[1][1]); SBAR(); G1_Q(1);
    vf[0][0]=pack8(av[0],0); vf[0][1]=pack8(av[0],1); PIN(vf[0][0]); PIN(vf[0][1]);
    vf[1][0]=pack8(av[1],0); vf[1][1]=pack8(av[1],1); PIN(vf[1][0]); PIN(vf[1][1]); SBAR();
    if(wc==0){ KV_STORE(); }
    SBAR();
    G1_Q(2); G1_Q(3); G1_Q(4); G1_Q(5); G1_Q(6); G1_Q(7);
    lds_wait(cnt+C_RD,4);
#pragma unroll
    for(int rt=0;rt<2;rt++){
      lwptr lq=(lwptr)lds+(1-wc)*HR+49152+(2*wr+rt)*2048+lane*16;
#pragma unroll
      for(int s=0;s<2;s++) *(LAS u32x4*)(lq+s*1024)=pack8(aq[rt],s,qs);
    }
    if(R<2){
#pragma unroll 1
      for(int i=0;i<8*(2-R);i++) *(LAS u32x4*)((lwptr)lds+(1-wc)*HR+wr*24576+i*1024+lane*16)=u32x4{0,0,0,0};
    }
    if(wc==1){ lds_wait(cnt+C_KV0,2); KV_STORE(); }
    WAITV(0);
    arrive_publish(cnt+C_KV0+wc,2,flagp(ws,b,FK_KV0+wc),nonce,lane);
    if(wid==0){
      lds_wait(cnt+C_KV0,2);
      const int kind=(lane>>2)&1, ri=lane&3;
      const gu64* fp=kvflag(ws,R,C,kind,0,ri);
      unsigned todo=(R>=2)?0x77u:((R==1)?0x66u:0x44u);
#pragma unroll 1
      for(unsigned sp=0;sp<SPIN_CAP && todo;sp++){
        u64 fv=0;
        if(lane<8 && ((todo>>lane)&1)) fv=__hip_atomic_load(fp,RLX_AGENT);
        unsigned rdy=(unsigned)__builtin_amdgcn_ballot_w64(fv==nonce)&todo;
        MEMFENCE();
        todo&=~rdy;
        if(!rdy) __builtin_amdgcn_s_sleep(2);
#pragma unroll 1
        while(rdy){
          const int bit=__builtin_ctz(rdy); rdy&=rdy-1;
          const int kd=bit>>2, i=bit&3;
          const char* src=ws+WS_KV+(size_t)blk_of(R-2+i,(C&12)|((C-1-kd)&3))*32768+kd*8192;
          const unsigned dst=lds0+HR+(1-kd)*24576+i*8192;
          dma4_sc1(lane*16,src,dst); dma4_sc1(lane*16,src+4096,dst+4096);
        }
      }
      WAITV(0);
      arrive(cnt+C_H0K);
    } else lds_wait(cnt+C_H0K,1);
    __builtin_amdgcn_s_setprio(1);
  }
  const int late=wid>>2;
  if(R>=2) attention<false>(lds,cnt,ws,wsr,b,R,C,wid,lane,nonce);
  else     attention<true >(lds,cnt,ws,wsr,b,R,C,wid,lane,nonce);
  if(!late){
    WAITV(0);
    arrive_publish(cnt+C_O0,4,flagp(ws,b,FK_O0),nonce,lane);
    if(wid<3){ lds_wait(cnt+C_E,4); lds_wait(cnt+C_P1L,4);
#pragma unroll 1
      for(int i=WO_P1+wid;i<32;i+=3) wo_block(i);
    }
    const int T2=R2*4+wid;
    const unsigned ob=WS_O+(unsigned)T2*65536u+lane*16u;
    u32x4 of[G2D][2];
#define G2_LDO(i,n) do{ const unsigned so_=(unsigned)((((n)+KO)&15)*4096); of[(i)%G2D][0]=__builtin_amdgcn_raw_buffer_load_b128(wsr,obv,so_,SC1); of[(i)%G2D][1]=__builtin_amdgcn_raw_buffer_load_b128(wsr,obv+1024,so_,SC1); }while(0)
    const int h=lane>>5,blk=(lane>>4)&1,q=(lane&15)>>2,p=lane&3;
    lptr wa[2];
#pragma unroll
    for(int ct=0;ct<2;ct++){ int c=(ct*4+2*blk+(p>>1))^(((q>>1)&1)<<2); wa[ct]=(lptr)lds+L_WO+(31-G2D)*4096+(4*h+q)*128+c*16+8*(p&1); }
    f32x16 acc[2]; acc[0]=f32x16{}; acc[1]=f32x16{};
    s16x4 wf[2][8];
#define G2_LDW(i) do{ _Pragma("unroll") for(int s_=0;s_<2;s_++) _Pragma("unroll") for(int ct_=0;ct_<2;ct_++){ lptr a_=wa[ct_]+(G2D-(i))*4096+s_*2048; \
      wf[(i)&1][4*s_+2*ct_]=ldstr(a_); wf[(i)&1][4*s_+2*ct_+1]=ldstr(a_+1024); } }while(0)
    lds_wait(cnt+C_W1,4);
    G2_LDW(0);
    PIN(acc[0]); PIN(acc[1]);
    if(wid==3){ wait_flags(flagp(ws,blk_of(R2,lane&15),FK_O0),nonce); arrive(cnt+C_EF); } else lds_wait(cnt+C_EF,1);
    unsigned obv=ob;
#pragma unroll
    for(int i=0;i<G2D;i++) G2_LDO(i,i);
    u64 f1=0;
    constexpr int NIT=32/G2D, ITL=16/G2D-1;
#pragma unroll 1
    for(int it=0;it<NIT;it++){
      if(it==1) arrive(cnt+C_W2);
      if(it==ITL){
        if(wid==3){ if(!allof(f1==nonce)) wait_flags(flagp(ws,blk_of(R2,lane&15),FK_O1),nonce); MEMFENCE(); arrive(cnt+C_LF); }
        else lds_wait(cnt+C_LF,1);
        __builtin_amdgcn_s_setprio(3);
      }
      if(it==ITL+1) lds_wait(cnt+C_W2,4);
      if(it==NIT-1) arrive(cnt+C_G2);
      PIN(wa[0]); PIN(wa[1]);
      const int nb=G2D*it+G2D;
      obv=ob+((it<ITL)?0u:((it==NIT-1)?0x40000000u:2048u)); PIN(obv);
#pragma unroll
      for(int i=0;i<G2D;i++){
        SBAR();
        if(i==G2D-4){ if(wid==3 && it==ITL-1) f1=__hip_atomic_load(flagp(ws,blk_of(R2,lane&15),FK_O1),RLX_AGENT); }
        G2_LDW(i+1);
#pragma unroll
        for(int s=0;s<2;s++)
#pragma unroll
          for(int ct=0;ct<2;ct++) acc[ct]=MFMA32(cat(wf[i&1][4*s+2*ct],wf[i&1][4*s+2*ct+1]),B8(of[i][s]),acc[ct]);
#pragma unroll
        for(int g_=0;g_<4;g_++){ __builtin_amdgcn_sched_group_barrier(0x100,2,0); __builtin_amdgcn_sched_group_barrier(0x8,1,0); }
        PIN(acc[0]); PIN(acc[1]);
        SBAR();
        G2_LDO(i,nb+i);
      }
      wa[0]-=G2D*4096; wa[1]-=G2D*4096;
    }
    lwptr stg=(lwptr)lds+L_WO+(31-wid)*4096;
#pragma unroll
    for(int ct=0;ct<2;ct++){
#pragma unroll
      for(int gp=0;gp<2;gp++){
        unsigned a0=pk(acc[ct][8*gp],acc[ct][8*gp+1]), a1=pk(acc[ct][8*gp+2],acc[ct][8*gp+3]);
        unsigned b0=pk(acc[ct][8*gp+4],acc[ct][8*gp+5]), b1=pk(acc[ct][8*gp+6],acc[ct][8*gp+7]);
        auto r0=__builtin_amdgcn_permlane32_swap(a0,b0,false,false); auto r1=__builtin_amdgcn_permlane32_swap(a1,b1,false,false);
        u32x4 vv={r0[0],r1[0],r0[1],r1[1]};
        *(LAS u32x4*)(stg+(lane&31)*128+(((ct*4+gp*2+h)^(lane&7))*16))=vv;
      }
    }
#pragma unroll
    for(int i=0;i<4;i++){
      const int row=8*i+(lane>>3);
      const u32x4 vv=*(const LAS u32x4*)(stg+row*128+(((lane&7)^(row&7))*16));
      __builtin_amdgcn_raw_buffer_store_b128(vv,outr,(unsigned)(T2*32+row)*4096u+2048u+C2*128u+(lane&7)*16u,0,SC1);
    }
  } else {
    const int v=wid-4;
    const int chb=xcd*128+(v&1)*64, rb=512*(j>>3)+8*(j&7)+4*(v>>1)+64*(lane>>3);
    u32x4 qq[4],yy[4];
    const bool yr=__builtin_amdgcn_readfirstlane(*(volatile LAS unsigned*)(cnt+C_YF))!=0;
    MEMFENCE();
    unsigned yo=WS_Y+(unsigned)(chb+(lane>>2))*4096u+(unsigned)(j*128+(v>>1)*64+(lane&3)*16);
    unsigned yo1=yr? yo : 0x40000000u;
#pragma unroll
    for(int i=0;i<4;i++){ qq[i]=*(const u32x4*)(Aq+(size_t)(rb+i)*1024+chb+(lane&7)*8); yy[i]=__builtin_amdgcn_raw_buffer_load_b128(wsr,yo1,i*65536,SC1); }
    WAITV(8);
    arrive_publish(cnt+C_O1,4,flagp(ws,b,FK_O1),nonce,lane);
    __builtin_amdgcn_s_setprio(0);
    if(!yr){ wait_flags<8>(flagp(ws,(lane&31)*8+xcd,FK_Y),nonce);
#pragma unroll
      for(int i=0;i<4;i++) yy[i]=__builtin_amdgcn_raw_buffer_load_b128(wsr,yo,i*65536,SC1); }
    lds_wait(cnt+C_L,4);
    lwptr stg=(lwptr)lds+v*4096;
#pragma unroll
    for(int i=0;i<4;i++) *(LAS u32x4*)(stg+i*1024+lane*16)=yy[i];
    { const int g=lane>>4,q=(lane&15)>>2,p=lane&3;
      bf16x8 tt[4];
#pragma unroll
      for(int n=0;n<4;n++){ lptr a=(lptr)stg+(8*(2*n+(g>>1))+q)*64+(g&1)*32+8*p; tt[n]=cat(ldstr(a),ldstr(a+256)); }
      MEMFENCE();
      const int srow=16*(g&1)+(lane&15);
#pragma unroll
      for(int n=0;n<4;n++) *(LAS bf16x8*)(stg+srow*128+(((2*n+(g>>1))^(srow&7))*16))=tt[n];
    }
    u32x4 ro[4];
#pragma unroll
    for(int i=0;i<4;i++){
      const int row=8*i+(lane>>3);
      const u32x4 yt=*(const LAS u32x4*)(stg+row*128+(((lane&7)^(row&7))*16));
#pragma unroll
      for(int d=0;d<4;d++) ro[i][d]=pk(lo16(qq[i][d])*lo16(yt[d]),hi16(qq[i][d])*hi16(yt[d]));
      PIN(ro[i]);
    }
    if(b==0 && wid==7){
#pragma unroll 1
      for(unsigned sp=0;sp<SPIN_CAP;sp++){
        u64 f[4];
#pragma unroll
        for(int i=0;i<4;i++) f[i]=__hip_atomic_load(flagp(ws,i*64+lane,FK_Y),RLX_AGENT);
        if(allof(f[0]==nonce && f[1]==nonce && f[2]==nonce && f[3]==nonce)) break;
        __builtin_amdgcn_s_sleep(16);
      }
      if(lane==0) __hip_atomic_store((gu64*)(ws+F_E),nonce,RLX_AGENT);
    }
    lds_wait(cnt+C_G2,4);
#pragma unroll
    for(int i=0;i<4;i++) __builtin_amdgcn_raw_buffer_store_b128(ro[i],outr,(unsigned)(rb+i)*4096u+chb*2u+(lane&7)*16u,0,SC1);
  }
}

extern "C" void kernel_launch(void* const* d_in, const int* in_sizes, int n_in, void* d_out, int out_size, void* d_ws, size_t ws_size, hipStream_t stream){
  (void)hipFuncSetAttribute((const void*)fused,hipFuncAttributeMaxDynamicSharedMemorySize,LDS_BYTES);
  fused<<<256,512,LDS_BYTES,stream>>>((const u16*)d_in[0],(const u16*)d_in[1],(const u16*)d_in[2],(const u16*)d_in[3],(const u16*)d_in[4],(const u16*)d_in[5],
                                      (const u16*)d_in[6],(const u16*)d_in[7],(const u16*)d_in[8],(u16*)d_out,(char*)d_ws);
}
```

```cpp
#include <hip/hip_runtime.h>
#include <hip/hip_bf16.h>
#include <stdint.h>
#if defined(__HIP_DEVICE_COMPILE__)
#define NOPK __attribute__((target("no-packed-fp32-ops")))
#else
#define NOPK
#endif
typedef unsigned short u16;
typedef unsigned long long u64;
typedef short bf16x8 __attribute__((ext_vector_type(8)));
typedef short s16x4 __attribute__((ext_vector_type(4)));
typedef float f32x16 __attribute__((ext_vector_type(16)));
typedef float f32x4 __attribute__((ext_vector_type(4)));
typedef float f32x2 __attribute__((ext_vector_type(2)));
typedef __bf16 bf2 __attribute__((ext_vector_type(2)));
typedef unsigned u32x4 __attribute__((ext_vector_type(4)));
typedef unsigned u32x2 __attribute__((ext_vector_type(2)));
#define LAS __attribute__((address_space(3)))
#define GAS __attribute__((address_space(1)))
typedef LAS const char* lptr;
typedef LAS char* lwptr;
typedef GAS u64 gu64;
#define DI __device__ __forceinline__ NOPK
#define MFMA32(a,b,c) __builtin_amdgcn_mfma_f32_32x32x16_bf16((a),(b),(c),0,0,0)
#define MFMA16(a,b,c) __builtin_amdgcn_mfma_f32_16x16x32_bf16((a),(b),(c),0,0,0)
#define SBAR() __builtin_amdgcn_sched_barrier(0)
#define PIN(x) asm volatile("":"+v"(x))
#define MEMFENCE() asm volatile("":::"memory")
#define BAR() asm volatile("s_barrier":::"memory")
#define WAITV(n) asm volatile("s_waitcnt vmcnt(" #n ")":::"memory")
#define WAITL() asm volatile("s_waitcnt lgkmcnt(0)":::"memory")
#define RLX_AGENT __ATOMIC_RELAXED, __HIP_MEMORY_SCOPE_AGENT
#define RLX_WG __ATOMIC_RELAXED, __HIP_MEMORY_SCOPE_WORKGROUP
#define SC1 16
#define B8(x) __builtin_bit_cast(bf16x8,(x))

constexpr unsigned MB=1u<<20;
constexpr unsigned WS_KV=0, WS_O=8*MB, WS_XT=12*MB, WS_Y=16*MB, WS_F=20*MB, WS_END=21*MB;
constexpr unsigned F_E=WS_F, F_W=WS_F+4096;
enum { FK_KV0=0, FK_KV1, FK_O0, FK_O1, FK_X, FK_Y };
constexpr int G1_SLOT=40960;
constexpr int HR=57344;
constexpr int L_CV=126976, CV_W=8480, CV_H=128, CV_X=128+4096+160;
constexpr int L_MISC=L_CV+4*CV_W;
constexpr int L_WO=L_MISC-131072;
constexpr int WO_P1=19;
static_assert(L_WO+(32-WO_P1)*4096>=HR+24576 && L_WO>=24576,"wo image");
constexpr int L_CNT=L_MISC;
constexpr int L_JUNK=L_CNT+128;
constexpr int L_MASK=L_JUNK+256;
constexpr int LDS_BYTES=L_MASK+2048;
static_assert(LDS_BYTES<=163840,"lds");
enum { C_RD=0, C_S15, C_KV0, C_KV1, C_H0K, C_H1, C_P1E, C_P1L, C_E, C_L, C_W1, C_W2, C_O0, C_O1, C_EF, C_LF, C_XF, C_YF, C_G2, C_HI, C_NUM };
constexpr int G2D=8;
constexpr unsigned SPIN_CAP=1u<<17, LSPIN_CAP=1u<<22;

DI unsigned pk(float a,float b){ f32x2 v={a,b}; bf2 r=__builtin_convertvector(v,bf2); return __builtin_bit_cast(unsigned,r); }
DI float asf(unsigned u){ return __builtin_bit_cast(float,u); }
DI unsigned asu(float f){ return __builtin_bit_cast(unsigned,f); }
DI bool allof(bool p){ return __builtin_amdgcn_ballot_w64(p)==~0ull; }
DI float lo16(unsigned u){ return asf(u<<16); }
DI float hi16(unsigned u){ return asf(u&0xffff0000u); }
DI bf16x8 ldsr128(lptr p){ return *(const LAS bf16x8*)p; }
DI s16x4 ldstr(lptr p){ return __builtin_amdgcn_ds_read_tr16_b64_v4i16((LAS s16x4*)p); }
DI bf16x8 cat(s16x4 a,s16x4 b){ return __builtin_shufflevector(a,b,0,1,2,3,4,5,6,7); }
DI u32x4 pack8(const f32x16& x,int s,float sc){ u32x4 r; r[0]=pk(x[8*s]*sc,x[8*s+1]*sc); r[1]=pk(x[8*s+2]*sc,x[8*s+3]*sc); r[2]=pk(x[8*s+4]*sc,x[8*s+5]*sc); r[3]=pk(x[8*s+6]*sc,x[8*s+7]*sc); return r; }
DI u32x4 pack8(const f32x16& x,int s){ u32x4 r; r[0]=pk(x[8*s],x[8*s+1]); r[1]=pk(x[8*s+2],x[8*s+3]); r[2]=pk(x[8*s+4],x[8*s+5]); r[3]=pk(x[8*s+6],x[8*s+7]); return r; }
#define DMA4_BODY(FL) "s_mov_b32 m0, %2\n\ts_nop 0\n\t" \
   "global_load_lds_dwordx4 %0, %1" FL "\n\tglobal_load_lds_dwordx4 %0, %1 offset:1024" FL "\n\tglobal_load_lds_dwordx4 %0, %1 offset:2048" FL "\n\tglobal_load_lds_dwordx4 %0, %1 offset:3072" FL
DI void dma4(unsigned voff,const void* sbase,unsigned ldsaddr){ asm volatile(DMA4_BODY(" nt") :: "v"(voff), "s"(sbase), "s"(ldsaddr) : "memory"); }
DI void dma4_sc1(unsigned voff,const void* sbase,unsigned ldsaddr){ asm volatile(DMA4_BODY(" sc1") :: "v"(voff), "s"(sbase), "s"(ldsaddr) : "memory"); }
DI void dma4v(unsigned v0,unsigned v1,unsigned v2,unsigned v3,const void* sbase,unsigned ldsaddr){
  asm volatile("s_mov_b32 m0, %5\n\ts_nop 0\n\t"
    "global_load_lds_dwordx4 %0, %4\n\tglobal_load_lds_dwordx4 %1, %4 offset:1024\n\tglobal_load_lds_dwordx4 %2, %4 offset:2048\n\tglobal_load_lds_dwordx4 %3, %4 offset:3072"
    :: "v"(v0),"v"(v1),"v"(v2),"v"(v3), "s"(sbase), "s"(ldsaddr) : "memory");
}
DI void touch(unsigned voff,const void* sbase,unsigned junk){
  asm volatile("s_mov_b32 m0, %2\n\ts_nop 0\n\tglobal_load_lds_dword %0, %1" :: "v"(voff), "s"(sbase), "s"(junk) : "memory");
}
DI void touch64(const void* a,unsigned junk){
  asm volatile("s_mov_b32 m0, %1\n\ts_nop 0\n\tglobal_load_lds_dword %0, off" :: "v"(a), "s"(junk) : "memory");
}
template<int SL=2>
DI void wait_flags(const gu64* p,u64 nonce){
#pragma unroll 1
  for(unsigned sp=0;sp<SPIN_CAP;sp++){
    u64 v=__hip_atomic_load(p,RLX_AGENT);
    if(allof(v==nonce)) break;
    __builtin_amdgcn_s_sleep(SL);
  }
  MEMFENCE();
}
DI void lds_wait(LAS unsigned* p,unsigned n){
#pragma unroll 1
  for(unsigned sp=0;sp<LSPIN_CAP;sp++){
    unsigned v=__hip_atomic_load(p,RLX_WG);
    if(__builtin_amdgcn_readfirstlane(v)>=n) break;
    __builtin_amdgcn_s_sleep(1);
  }
  MEMFENCE();
}
DI void arrive(LAS unsigned* cnt){
  unsigned one=1u;
  asm volatile("s_mov_b64 exec, 1\n\tds_add_u32 %0, %1\n\ts_mov_b64 exec, -1" :: "v"(cnt), "v"(one) : "memory");
}
DI void arrive_publish(LAS unsigned* cnt,unsigned n,gu64* flag,u64 nonce,int lane){
  unsigned one=1u, old;
  asm volatile("s_mov_b64 exec, 1\n\tds_add_rtn_u32 %0, %1, %2\n\ts_mov_b64 exec, -1\n\ts_waitcnt lgkmcnt(0)" : "=&v"(old) : "v"(cnt), "v"(one) : "memory");
  if(__builtin_amdgcn_readfirstlane(old)==n-1){ if(lane==0) __hip_atomic_store(flag,nonce,RLX_AGENT); }
  MEMFENCE();
}
DI gu64* flagp(char* ws,int blk,int k){ return (gu64*)(ws+F_W+(unsigned)(blk*8+k)*128u); }
DI int blk_of(int R,int C){ return (((R&7)*4+(C&3))<<3)|((R>>3)*4+(C>>2)); }
DI float fmax3(float a,float b,float c){ return __builtin_fmaxf(__builtin_fmaxf(a,b),c); }
DI float xhalf_max(float m){ auto rr=__builtin_amdgcn_permlane32_swap(asu(m),asu(m),false,false); return __builtin_fmaxf(asf(rr[0]),asf(rr[1])); }
struct G1F { bf16x8 x0,x1; s16x4 w[6]; };

DI const gu64* kvflag(char* ws,int R,int C,int kind,int head,int lane){
  const int Cs=(C&12)|((C-1-kind)&3); const int rr=(lane<3)? lane : 2; const int Rl=(R-2+rr<0)? R : R-2+rr;
  return flagp(ws,blk_of(Rl,Cs),FK_KV0+head);
}
DI void kv_fetch(char* ws,unsigned lds0,int R,int C,int kind,int head,int half,int lane){
  const int Cs=(C&12)|((C-1-kind)&3);
#pragma unroll 1
  for(int i=(R<2)?2-R:0;i<3;i++){
    const char* src=ws+WS_KV+(size_t)blk_of(R-2+i,Cs)*32768+head*16384+kind*8192+half*4096;
    const unsigned dst=lds0+(1-head)*HR+(1-kind)*24576+i*8192+half*4096;
    dma4_sc1(lane*16,src,dst);
  }
}
template<bool CHECK>
DI void attention(char* lds,LAS unsigned* cnt,char* ws,__amdgpu_buffer_rsrc_t wsr,int b,int R,int C,int wid,bool dry,int lane,u64 nonce){
  const int late=wid>>2, jq=wid&3;
  const int T=4*R+jq;
  lptr VL=(lptr)lds+(1-late)*HR+jq*2048+lane*16;
  lptr KL=VL+24576;
  lptr QL=(lptr)lds+(1-late)*HR+49152+jq*2048+lane*16;
  const f32x16 zero=f32x16{};
  const float ninf=-__builtin_inff();
  const int dl=(lane&31)-4*(lane>>5);
  bf16x8 kf[9][2];
  const bf16x8 q0=ldsr128(QL), q1=ldsr128(QL+1024);
  kf[8][0]=ldsr128(KL+8*2048); kf[8][1]=ldsr128(KL+8*2048+1024);
#pragma unroll
  for(int u=0;u<4;u++){ kf[u][0]=ldsr128(KL+u*2048); kf[u][1]=ldsr128(KL+u*2048+1024); }
  f32x16 sd,sm,s[3];
  float m;
#pragma unroll
  for(int n=0;n<11;n++){
    SBAR();
    if(n==0){ sd=MFMA32(kf[8][0],q0,zero); sd=MFMA32(kf[8][1],q1,sd); PIN(sd); }
    else if(n==1){ sm=MFMA32(kf[0][0],q0,zero); sm=MFMA32(kf[0][1],q1,sm); PIN(sm); }
    else if(n<9){ const int u=n-1; s[u%3]=MFMA32(kf[u][0],q0,zero); s[u%3]=MFMA32(kf[u][1],q1,s[u%3]); PIN(s[u%3]); }
    if(n<4){ const int u=n+4; kf[u][0]=ldsr128(KL+u*2048); kf[u][1]=ldsr128(KL+u*2048+1024); }
    if(n==3){
#pragma unroll
      for(int i=0;i<16;i++){ const int a=(i&3)+8*(i>>2); sm[i]=(a<=dl)?sd[i]:((CHECK&&T<8)?ninf:sm[i]); }
    }
    if(n>=3){
      const int t=n-3; const f32x16& x=(t==0)?sm:s[t%3];
      float a=fmax3(x[0],x[1],x[2]), c=fmax3(x[3],x[4],x[5]);
      a=fmax3(a,x[6],x[7]); c=fmax3(c,x[8],x[9]); a=fmax3(a,x[10],x[11]); c=fmax3(c,x[12],x[13]); a=fmax3(a,x[14],x[15]);
      if(t==0) m=__builtin_fmaxf(a,c);
      else { const float mm=fmax3(m,a,c); m=(!CHECK||T-8+t>=0)?mm:m; }
      PIN(m);
    }
  }
  SBAR();
  if(!dry) arrive(cnt+(late?C_P1L:C_P1E));
  if(late){
    WAITV(0); arrive(cnt+C_W1);
    lds_wait(cnt+C_E,4); __builtin_amdgcn_s_setprio(2);
  }
  bf16x8 vfr[2][2];
  vfr[0][0]=ldsr128(VL+8*2048); vfr[0][1]=ldsr128(VL+8*2048+1024);
  vfr[1][0]=ldsr128(VL); vfr[1][1]=ldsr128(VL+1024);
  const u32x4 mk0=*(const LAS u32x4*)((lptr)lds+L_MASK+lane*16), mk1=*(const LAS u32x4*)((lptr)lds+L_MASK+1024+lane*16);
  m=xhalf_max(m);
  f32x16 negm;
#pragma unroll
  for(int i=0;i<16;i++) negm[i]=-m;
  PIN(negm);
  const unsigned one2=((((lane>>2)^(lane>>4))&1)==0)?0x3f803f80u:0u;
  const u32x4 ones={one2,one2,one2,one2};
  f32x16 o; f32x4 ls; u64 fy=0;
  u32x4 p0,p1;
#pragma unroll
  for(int u=0;u<=8;u++){
    SBAR();
    if(u==0||u==2||u==4||u==6){ if(dry){ if(__builtin_amdgcn_readfirstlane(*(volatile LAS unsigned*)(cnt+C_HI))!=0) return; } SBAR(); }
    if(u==2){ if(wid==4) fy=__hip_atomic_load(flagp(ws,(lane&31)*8+(b&7),FK_Y),RLX_AGENT); SBAR(); }
    if(u==6){ if(wid==4){ PIN(fy); if(allof(fy==nonce)) arrive(cnt+C_YF); } SBAR(); }
    u32x4 n0,n1;
    if(u+1<8){ const int t=u+1; s[t&1]=MFMA32(kf[t][0],q0,negm); s[t&1]=MFMA32(kf[t][1],q1,s[t&1]); }
    if(u<8){
      f32x16& x=(u==0)?sm:s[u&1];
#pragma unroll
      for(int i=0;i<16;i++) x[i]=__builtin_amdgcn_exp2f((u==0)?x[i]-m:x[i]);
      n0=pack8(x,0); n1=pack8(x,1);
      if(CHECK && u>0){ if(T-8+u<0){ n0=u32x4{0,0,0,0}; n1=u32x4{0,0,0,0}; } }
    }
    if(u==1){
      const u32x4 d0=p0&mk0, d1=p1&mk1;
      o=MFMA32(vfr[0][0],B8(d0),zero); o=MFMA32(vfr[0][1],B8(d1),o);
      o=MFMA32(vfr[1][0],B8(p0^d0),o); o=MFMA32(vfr[1][1],B8(p1^d1),o);
      vfr[1][0]=ldsr128(VL+1*2048); vfr[1][1]=ldsr128(VL+1*2048+1024);
      vfr[0][0]=ldsr128(VL+2*2048); vfr[0][1]=ldsr128(VL+2*2048+1024);
      ls=MFMA16(B8(ones),B8(p0),(f32x4{0,0,0,0})); ls=MFMA16(B8(ones),B8(p1),ls);
    } else if(u>1){
      const int t=u-1;
      o=MFMA32(vfr[t&1][0],B8(p0),o); o=MFMA32(vfr[t&1][1],B8(p1),o);
      if(t+2<8){ vfr[t&1][0]=ldsr128(VL+(t+2)*2048); vfr[t&1][1]=ldsr128(VL+(t+2)*2048+1024); }
      ls=MFMA16(B8(ones),B8(p0),ls); ls=MFMA16(B8(ones),B8(p1),ls);
    }
    if(u>1 && u<7 && !CHECK){
#pragma unroll
      for(int g_=0;g_<6;g_++){ __builtin_amdgcn_sched_group_barrier(0x8,1,0); __builtin_amdgcn_sched_group_barrier(0x402,4,0); }
    }
    if(u+1<8) PIN(s[(u+1)&1]);
    if(u>0){ PIN(ls); PIN(o); }
    if(u<8){ PIN(n0); PIN(n1); p0=n0; p1=n1; }
  }
  SBAR();
  if(dry) return;
  arrive(cnt+(late?C_L:C_E));
  const float il=__builtin_amdgcn_rcpf(ls[0]);
  const unsigned oo=WS_O+(unsigned)(T*64+2*(2*C+late))*1024u+lane*16u;
  __builtin_amdgcn_raw_buffer_store_b128(pack8(o,0,il),wsr,oo,0,SC1);
  __builtin_amdgcn_raw_buffer_store_b128(pack8(o,1,il),wsr,oo+1024,0,SC1);
}

__global__ __launch_bounds__(512,2) NOPK void fused(const u16* Aq,const u16* Ak,const u16* Av,const u16* Ask,const u16* Ah,const u16* AD,
                                               const u16* X,const u16* W,const u16* WO,u16* out,char* ws){
  extern __shared__ __attribute__((aligned(16))) char lds[];
  const char* kpc; asm volatile("s_getpc_b64 %0":"=s"(kpc));
  asm volatile(""::"s"(X),"s"(W),"s"(ws),"s"(WO),"s"(Ak));
  const int tid=threadIdx.x, lane=tid&63; const int wid=__builtin_amdgcn_readfirstlane(tid>>6);
  const int b=blockIdx.x, xcd=b&7, j=b>>3;
  const int R=(xcd>>2)*8+(j>>2), C=(xcd&3)*4+(j&3);
  const int wgi=xcd*32+j;
  const int R2=((xcd>>1)^2)*4+(j>>3), C2=(xcd&1)*8+(j&7), KO=2*(j&7)+4*(j>>3);
  const unsigned lds0=(unsigned)(uintptr_t)lds;
  u64 ew=__hip_atomic_load((gu64*)(ws+F_E),RLX_AGENT);
  const unsigned a0=(unsigned)((lane>>3)*2048+(((lane&7)^(lane>>4))*16)), a1=(a0^64u)+(16384-1024);
  const unsigned b0=(unsigned)((lane>>3)*6144+(((lane&7)^(((lane>>4)&1)<<2))*16));
  const char* xb=(const char*)X+(size_t)R*128*2048; const char* wb=(const char*)W+(C&12)*128;
  const int cs[3]={(C&3)*128,2048+((C+1)&3)*128,4096+((C+2)&3)*128};
#pragma unroll
  for(int k=0;k<2;k++){
    asm volatile("s_mov_b32 m0, %3\n\ts_nop 0\n\tglobal_load_lds_dwordx4 %0, %2\n\tglobal_load_lds_dwordx4 %1, %2 offset:1024" :: "v"(a0),"v"(a1),"s"(xb+wid*32768+k*128),"s"(lds0+k*G1_SLOT+wid*2048) : "memory");
#pragma unroll
    for(int g=0;g<3;g++)
      asm volatile("s_mov_b32 m0, %2\n\ts_nop 0\n\tglobal_load_lds_dwordx4 %0, %1" :: "v"(b0),"s"(wb+cs[g]+wid*49152+k*(64*6144)),"s"(lds0+k*G1_SLOT+16384+g*8192+wid*1024) : "memory");
    if(k==0){ if(lane<2) touch((unsigned)((lane? 256+j : j*8+wid)*128),kpc,lds0+L_JUNK); }
    SBAR();
  }
  LAS unsigned* cnt=(LAS unsigned*)((lwptr)lds+L_CNT);
  const __amdgpu_buffer_rsrc_t wsr=__builtin_amdgcn_make_buffer_rsrc(ws,0,WS_END,0x00020000);
  const __amdgpu_buffer_rsrc_t outr=__builtin_amdgcn_make_buffer_rsrc(out,0,8*MB,0x00020000);
  u64 nonce;
#define LOAD_NONCE() do{ PIN(ew); nonce=((u64)__builtin_amdgcn_readfirstlane((unsigned)(ew>>32))<<32)|(unsigned)__builtin_amdgcn_readfirstlane((unsigned)ew); \
    nonce=nonce*6364136223846793005ull+1442695040888963407ull; }while(0)
  auto wo_block=[&](int i) NOPK __attribute__((always_inline)) {
    const int hd=2*((i+KO)&15)+(i>>4);
    const unsigned vo=(unsigned)((lane>>3)*2048+C2*128+(((lane&7)^(((lane>>4)&1)<<2))*16));
    dma4v(vo,vo+16384-1024,vo+2*(16384-1024),vo+3*(16384-1024),(const char*)WO+(size_t)hd*65536,lds0+L_WO+(31-i)*4096);
  };
  if(__builtin_expect(wid>=4,1)){
    const int v=wid-4;
    const unsigned junk=lds0+L_JUNK;
    if(__builtin_expect(wid==4,1)){
      xb+=256; wb+=2*64*6144;
      unsigned la=lds0+2*G1_SLOT;
      WAITV(6);
#pragma unroll 1
      for(int k=0;k<14;k++){
        WAITV(40); BAR();
#pragma unroll
        for(int g=0;g<4;g++){
          dma4v(a0,a1,a0+2*(16384-1024),a1+2*(16384-1024),xb+g*65536,la+g*4096);
          if(g<3){
            dma4v(b0,b0+(49152-1024),b0+2*(49152-1024),b0+3*(49152-1024),wb+cs[g],la+16384+g*8192);
            dma4v(b0,b0+(49152-1024),b0+2*(49152-1024),b0+3*(49152-1024),wb+cs[g]+32*6144,la+16384+g*8192+4096);
          }
        }
        xb+=128; wb+=64*6144; la+=G1_SLOT; if(la==lds0+3*G1_SLOT) la=lds0;
      }
      WAITV(40); BAR();
      WAITV(0); arrive(cnt+C_S15);
      LOAD_NONCE();
    } else if(wid==5){
      __builtin_amdgcn_s_setprio(3);
      const char* ta; unsigned tstep;
      { const int sg=lane/20, i=lane-sg*20; const bool isx=i<8; const int rem=i-8, kr=(xcd>>2)*32+j;
        tstep=isx?128u:64u*6144u;
        ta=(isx? (const char*)X+(size_t)((xcd>>2)*1024+((xcd&3)*32+j)*8+i)*2048 : (const char*)W+(kr*6144+(rem>>2)*2048+((xcd&3)*4+(rem&3))*128))+(size_t)tstep*(2+sg);
        tstep*=3; }
#define STOUCH(n) do{ if(lane<(n)) touch64(ta,junk); ta+=tstep; }while(0)
      STOUCH(60);
      if(j==0) *(LAS u32x4*)((lwptr)lds+L_CV+lane*16)=u32x4{0,0,0,0};
      if(lane<C_NUM) cnt[lane]=0u;
      { const int bq=lane&31, hi=lane>>5; u32x4 mk[2];
#pragma unroll
        for(int d=0;d<8;d++){ const int i=2*d; const int a=(i&3)+8*(i>>2)+4*hi; mk[d>>2][d&3]=((a<=bq)?0xffffu:0u)|((a+1<=bq)?0xffff0000u:0u); }
        *(LAS u32x4*)((lwptr)lds+L_MASK+lane*16)=mk[0]; *(LAS u32x4*)((lwptr)lds+L_MASK+1024+lane*16)=mk[1]; }
      WAITL();
      WAITV(7);
      BAR();
      WAITV(1);
      if(lane<6) asm volatile("s_mov_b32 m0, %2\n\ts_nop 0\n\tglobal_load_lds_dword %0, %1 sc1" :: "v"((unsigned)(F_W+(b*8+lane)*128)), "s"(ws), "s"(junk) : "memory");
      const unsigned pv=(unsigned)((lane>>4)*2048+(lane&15)*16);
      const char* ksrc=(const char*)Ak+(size_t)j*64*2048+xcd*256; const char* vsrc=(const char*)Av+(size_t)j*64*2048+xcd*256;
      if(j>0) asm volatile("s_mov_b32 m0, %2\n\ts_nop 0\n\tglobal_load_lds_dwordx4 %0, %1" :: "v"(pv^(7u<<5)), "s"(ksrc-4*2048), "s"(lds0+L_CV) : "memory");
      STOUCH(60); STOUCH(60);
      BAR();
      STOUCH(60); STOUCH(40);
      touch((unsigned)((wgi*64+lane)*128),WO,junk);
#pragma unroll 1
      for(int g2=0;g2<4;g2++){
        const unsigned p0=pv^(unsigned)(g2<<6), p1=p0^32u;
        dma4v(p0,p0+(8192-1024),p1+2*(8192-1024),p1+3*(8192-1024),ksrc+g2*32768,lds0+L_CV+1024+g2*4096);
        dma4v(p0,p0+(8192-1024),p1+2*(8192-1024),p1+3*(8192-1024),vsrc+g2*32768,lds0+L_CV+17408+g2*4096);
      }
      BAR();
      WAITV(0);
#pragma unroll 1
      for(int i=3;i<=12;i++) BAR();
      u64 fxv=__hip_atomic_load(flagp(ws,(lane&31)*8+xcd,FK_X),RLX_AGENT);
      BAR();
      SBAR(); PIN(fxv);
      LOAD_NONCE();
      if(allof(fxv==nonce)){ arrive(cnt+C_XF); WAITL(); }
      BAR();
      __builtin_amdgcn_s_setprio(0);
    } else {
      __builtin_amdgcn_s_setprio(1);
      const int hv=wid-6, rg=lane&7;
      u32x4 sk[4];
      WAITV(6);
      BAR();
      WAITV(0);
      LOAD_NONCE();
#pragma unroll
      for(int i=0;i<4;i++) sk[i]=*(const u32x4*)(Ask+(xcd*128+(hv*16+(i>>1)*8+(lane>>3))*4)*4+(i&1)*8);
      BAR(); BAR(); BAR();
      SBAR();
      asm volatile("":"+v"(sk[0]),"+v"(sk[1]),"+v"(sk[2]),"+v"(sk[3]));
#pragma unroll 1
      for(int g=0;g<2;g++){
        const int c4=hv*16+g*8+(lane>>3);
        u32x2 kk[11], vv[8];
        lptr kb=(lptr)lds+L_CV+(8*rg+4)*256+(c4&1)*8;
#pragma unroll
        for(int i=0;i<3;i++) kk[i]=*(const LAS u32x2*)(kb+(i-3)*256+(((c4>>1)^(((rg-1)&7)<<1))*16));
#pragma unroll
        for(int i=0;i<8;i++){ kk[3+i]=*(const LAS u32x2*)(kb+i*256+(((c4>>1)^(rg<<1))*16)); vv[i]=*(const LAS u32x2*)(kb+16384+i*256+(((c4>>1)^(rg<<1))*16)); }
#pragma unroll
        for(int c=0;c<4;c++){
          const unsigned w01=sk[c>>1][(c&1)*2], w23=sk[c>>1][(c&1)*2+1];
          const float w0=lo16(w01),w1=hi16(w01),w2=lo16(w23),w3=hi16(w23);
          float kf[11];
#pragma unroll
          for(int i=0;i<11;i++) kf[i]=(c&1)?hi16(kk[i][c>>1]):lo16(kk[i][c>>1]);
          float o[8];
#pragma unroll
          for(int i=0;i<8;i++){ float vf=(c&1)?hi16(vv[i][c>>1]):lo16(vv[i][c>>1]); o[i]=(kf[i]*w3+kf[i+1]*w2+kf[i+2]*w1+kf[i+3]*w0)*vf; }
          u32x4 r; r[0]=pk(o[0],o[1]); r[1]=pk(o[2],o[3]); r[2]=pk(o[4],o[5]); r[3]=pk(o[6],o[7]);
          __builtin_amdgcn_raw_buffer_store_b128(r,wsr,WS_XT+(unsigned)(xcd*128+c4*4+c)*4096u+(unsigned)(j*128+rg*16),0,SC1);
          SBAR();
          if(c!=1){ BAR(); SBAR(); }
        }
        sk[0]=sk[2]; sk[1]=sk[3];
      }
      WAITV(0);
      BAR();
      if(hv==0 && lane==0) __hip_atomic_store(flagp(ws,b,FK_X),nonce,RLX_AGENT);
      BAR(); BAR(); BAR(); BAR();
    }
    const int ch=wgi*4+v;
    const float Dc=lo16(AD[ch]);
    const bool xr=__builtin_amdgcn_readfirstlane(cnt[C_XF])!=0;
    dma4(lane*16,(const char*)Ah+(size_t)ch*4096,lds0+L_CV+v*CV_W+CV_H);
    if(xr) dma4_sc1(lane*16,ws+WS_XT+(size_t)ch*4096,lds0+L_CV+v*CV_W+CV_X);
    __builtin_amdgcn_s_setprio(2);
    if(!xr){ wait_flags<4>(flagp(ws,(lane&31)*8+xcd,FK_X),nonce); dma4_sc1(lane*16,ws+WS_XT+(size_t)ch*4096,lds0+L_CV+v*CV_W+CV_X); }
    WAITV(4);
    if(lane<32) *(LAS unsigned*)((lwptr)lds+L_CV+v*CV_W+lane*4)=0u;
    if(lane<40) *(LAS unsigned*)((lwptr)lds+L_CV+v*CV_W+CV_H+4096+lane*4)=0u;
    bool h1;
    {
      lwptr LW=(lwptr)lds+L_CV+v*CV_W;
      const int g=lane>>4,q=(lane&15)>>2,p=lane&3;
      lptr ha=(lptr)LW+CV_H+8*(4*p+8*g+q-12);
      lptr xa=(lptr)LW+CV_X+8*(p+8-8*g-q);
      PIN(ha); PIN(xa);
      bf16x8 A[16],B0[16],B1[16];
#pragma unroll
      for(int d=0;d<16;d++) A[d]=cat(ldstr(ha+256*d),ldstr(ha+256*d+32));
      WAITV(0);
      float zf=0.f; PIN(zf);
#define MM0(z,a,b) asm volatile("v_mfma_f32_16x16x32_bf16 %0, %1, %2, 0" : "=&v"(z) : "v"(a), "v"(b))
#define MMA(z,a,b) asm volatile("v_mfma_f32_16x16x32_bf16 %0, %1, %2, %0" : "+v"(z) : "v"(a), "v"(b))
#define LDB(P,e) B##P[e]=cat(ldstr(xa+256*(e)+128*(P)+32),ldstr(xa+256*(e)+128*(P)))
#define ROR2(d,s,z,e,t,y) asm volatile("v_add_f32_dpp %0, %2, %3 wave_ror:1 row_mask:0xf bank_mask:0xf\n\tv_add_f32_dpp %1, %4, %5 wave_ror:1 row_mask:0xf bank_mask:0xf" : "=&v"(d), "=&v"(e) : "v"(s), "v"(z), "v"(t), "v"(y))
#define SHR2(d,s,z,e,t,y) asm volatile("v_add_f32_dpp %0, %2, %3 wave_shr:1 row_mask:0xf bank_mask:0xf\n\tv_add_f32_dpp %1, %4, %5 wave_shr:1 row_mask:0xf bank_mask:0xf" : "+v"(d), "+v"(e) : "v"(s), "v"(z), "v"(t), "v"(y))
      const unsigned yo=WS_Y+(unsigned)ch*4096u+lane*16u;
#define STY(C) __builtin_amdgcn_raw_buffer_store_b128(yw[C],wsr,yo+(C)*1024,0,SC1)
#define NOP1() asm volatile("s_nop 0")
#define VNOP() __builtin_amdgcn_s_setprio(2)
      unsigned xa_,xb_;
      lptr xe=(lptr)LW+CV_X+2*lane; PIN(xe);
#define LDX(c) do{ xa_=*(const LAS u16*)(xe+256*(c)); xb_=*(const LAS u16*)(xe+256*(c)+128); }while(0)
#define SHX() do{ xa_<<=16; xb_<<=16; PIN(xa_); PIN(xb_); }while(0)
#define FMD(z,x) do{ z[0]=__builtin_fmaf(asf(x),Dc,z[0]); PIN(z); }while(0)
      const gu64* kf1=kvflag(ws,R,C,v>>1,1,lane);
      u64 f1v; unsigned c1v;
#define HOOK1() do{ f1v=__hip_atomic_load(kf1,RLX_AGENT); c1v=*(volatile LAS unsigned*)(cnt+C_KV1); }while(0)
#define HOOK2() do{ h1=allof(f1v==nonce) && __builtin_amdgcn_readfirstlane(c1v)>=2; MEMFENCE(); if(h1) kv_fetch(ws,lds0,R,C,v>>1,1,v&1,lane); }while(0)
      SBAR();
f32x4 z0,z1,z2,z3,z4,z5,z6,z7,z8,z9,z10,z11,z12,z13,z14,z15,z16,z17,z18,z19,z20,z21,z22,z23,z24,z25,z26,z27,z28,z29,z30,z31;
float t2_0,t1_0,u_0,t2_1,t1_1,u_1,t2_2,t1_2,u_2,t2_3,t1_3,u_3,t2_4,t1_4,u_4,t2_5,t1_5,u_5,t2_6,t1_6,u_6,t2_7,t1_7,u_7,t2_8,t1_8,u_8,t2_9,t1_9,u_9,t2_10,t1_10,u_10,t2_11,t1_11,u_11,t2_12,t1_12,u_12,t2_13,t1_13,u_13,t2_14,t1_14,u_14,t2_15,t1_15,u_15,t2_16,t1_16,u_16,t2_17,t1_17,u_17,t2_18,t1_18,u_18,t2_19,t1_19,u_19,t2_20,t1_20,u_20,t2_21,t1_21,u_21,t2_22,t1_22,u_22,t2_23,t1_23,u_23,t2_24,t1_24,u_24,t2_25,t1_25,u_25,t2_26,t1_26,u_26,t2_27,t1_27,u_27,t2_28,t1_28,u_28,t2_29,t1_29,u_29,t2_30,t1_30,u_30,t2_31,t1_31,u_31;
u32x4 yw[4];

LDB(0,0); LDB(1,0);
LDB(0,1); LDB(1,1);
LDB(0,2); LDB(1,2);
LDX(0);
SBAR();
MM0(z0,A[0],B0[0]); SBAR(); LDB(0,3); SBAR();
MM0(z1,A[0],B1[0]); SBAR(); LDB(1,3); SBAR();
MM0(z2,A[1],B0[0]); SBAR(); LDB(0,4); SBAR();
MM0(z3,A[1],B1[0]); SBAR(); LDB(1,4); SBAR();
MMA(z2,A[0],B0[1]); SBAR();  SBAR();
MMA(z3,A[0],B1[1]); SBAR();  SBAR();
MM0(z4,A[2],B0[0]); SBAR();  SBAR();
MM0(z5,A[2],B1[0]); SBAR();  SBAR();
MMA(z4,A[1],B0[1]); SBAR();  SBAR();
MMA(z5,A[1],B1[1]); SBAR();  SBAR();
MMA(z4,A[0],B0[2]); SBAR(); ROR2(t2_0,zf,z0[2],t2_1,z0[3],z1[2]); SBAR();
MMA(z5,A[0],B1[2]); SBAR(); LDB(0,5); SBAR();
MM0(z6,A[3],B0[0]); SBAR(); SHR2(t2_0,z0[3],z0[2],t2_1,z1[3],z1[2]); SBAR();
MM0(z7,A[3],B1[0]); SBAR(); LDB(1,5); SBAR();
MMA(z6,A[2],B0[1]); SBAR(); ROR2(t1_0,zf,z0[1],t1_1,t2_0,z1[1]); SBAR();
MMA(z7,A[2],B1[1]); SBAR(); SHX(); SBAR();
MMA(z6,A[1],B0[2]); SBAR(); SHR2(t1_0,t2_0,z0[1],t1_1,t2_1,z1[1]); SBAR();
MMA(z7,A[1],B1[2]); SBAR(); FMD(z0,xa_); FMD(z1,xb_); SBAR();
MMA(z6,A[0],B0[3]); SBAR(); ROR2(u_0,zf,z0[0],u_1,t1_0,z1[0]); SBAR();
MMA(z7,A[0],B1[3]); SBAR(); LDX(1); SBAR();
MM0(z8,A[4],B0[0]); SBAR(); SHR2(u_0,t1_0,z0[0],u_1,t1_1,z1[0]); SBAR();
MM0(z9,A[4],B1[0]); SBAR(); yw[0][0]=pk(u_0,u_1); PIN(yw[0]); SBAR();
MMA(z8,A[3],B0[1]); SBAR(); ROR2(t2_2,z1[3],z2[2],t2_3,z2[3],z3[2]); SBAR();
MMA(z9,A[3],B1[1]); SBAR(); LDB(0,6); SBAR();
MMA(z8,A[2],B0[2]); SBAR(); SHR2(t2_2,z2[3],z2[2],t2_3,z3[3],z3[2]); SBAR();
MMA(z9,A[2],B1[2]); SBAR(); LDB(1,6); SBAR();
MMA(z8,A[1],B0[3]); SBAR(); ROR2(t1_2,t2_1,z2[1],t1_3,t2_2,z3[1]); SBAR();
MMA(z9,A[1],B1[3]); SBAR(); SHX(); SBAR();
MMA(z8,A[0],B0[4]); SBAR(); SHR2(t1_2,t2_2,z2[1],t1_3,t2_3,z3[1]); SBAR();
MMA(z9,A[0],B1[4]); SBAR(); FMD(z2,xa_); FMD(z3,xb_); SBAR();
MM0(z10,A[5],B0[0]); SBAR(); ROR2(u_2,t1_1,z2[0],u_3,t1_2,z3[0]); SBAR();
MM0(z11,A[5],B1[0]); SBAR(); LDX(2); SBAR();
MMA(z10,A[4],B0[1]); SBAR(); SHR2(u_2,t1_2,z2[0],u_3,t1_3,z3[0]); SBAR();
MMA(z11,A[4],B1[1]); SBAR(); yw[0][1]=pk(u_2,u_3); PIN(yw[0]); SBAR();
MMA(z10,A[3],B0[2]); SBAR(); ROR2(t2_4,z3[3],z4[2],t2_5,z4[3],z5[2]); SBAR();
MMA(z11,A[3],B1[2]); SBAR(); LDB(0,7); SBAR();
MMA(z10,A[2],B0[3]); SBAR(); SHR2(t2_4,z4[3],z4[2],t2_5,z5[3],z5[2]); SBAR();
MMA(z11,A[2],B1[3]); SBAR(); LDB(1,7); SBAR();
MMA(z10,A[1],B0[4]); SBAR(); ROR2(t1_4,t2_3,z4[1],t1_5,t2_4,z5[1]); SBAR();
MMA(z11,A[1],B1[4]); SBAR(); SHX(); SBAR();
MMA(z10,A[0],B0[5]); SBAR(); SHR2(t1_4,t2_4,z4[1],t1_5,t2_5,z5[1]); SBAR();
MMA(z11,A[0],B1[5]); SBAR(); FMD(z4,xa_); FMD(z5,xb_); SBAR();
MM0(z12,A[6],B0[0]); SBAR(); ROR2(u_4,t1_3,z4[0],u_5,t1_4,z5[0]); SBAR();
MM0(z13,A[6],B1[0]); SBAR(); LDX(3); SBAR();
MMA(z12,A[5],B0[1]); SBAR(); SHR2(u_4,t1_4,z4[0],u_5,t1_5,z5[0]); SBAR();
MMA(z13,A[5],B1[1]); SBAR(); yw[0][2]=pk(u_4,u_5); PIN(yw[0]); SBAR();
MMA(z12,A[4],B0[2]); SBAR(); ROR2(t2_6,z5[3],z6[2],t2_7,z6[3],z7[2]); SBAR();
MMA(z13,A[4],B1[2]); SBAR(); LDB(0,8); SBAR();
MMA(z12,A[3],B0[3]); SBAR(); SHR2(t2_6,z6[3],z6[2],t2_7,z7[3],z7[2]); SBAR();
MMA(z13,A[3],B1[3]); SBAR(); LDB(1,8); SBAR();
MMA(z12,A[2],B0[4]); SBAR(); ROR2(t1_6,t2_5,z6[1],t1_7,t2_6,z7[1]); SBAR();
MMA(z13,A[2],B1[4]); SBAR(); SHX(); SBAR();
MMA(z12,A[1],B0[5]); SBAR(); SHR2(t1_6,t2_6,z6[1],t1_7,t2_7,z7[1]); SBAR();
MMA(z13,A[1],B1[5]); SBAR(); FMD(z6,xa_); FMD(z7,xb_); SBAR();
MMA(z12,A[0],B0[6]); SBAR(); ROR2(u_6,t1_5,z6[0],u_7,t1_6,z7[0]); SBAR();
MMA(z13,A[0],B1[6]); SBAR(); LDX(4); SBAR();
MM0(z14,A[7],B0[0]); SBAR(); SHR2(u_6,t1_6,z6[0],u_7,t1_7,z7[0]); SBAR();
MM0(z15,A[7],B1[0]); SBAR(); yw[0][3]=pk(u_6,u_7); PIN(yw[0]); STY(0); SBAR();
MMA(z14,A[6],B0[1]); SBAR(); ROR2(t2_8,z7[3],z8[2],t2_9,z8[3],z9[2]); SBAR();
MMA(z15,A[6],B1[1]); SBAR(); LDB(0,9); SBAR();
MMA(z14,A[5],B0[2]); SBAR(); SHR2(t2_8,z8[3],z8[2],t2_9,z9[3],z9[2]); SBAR();
MMA(z15,A[5],B1[2]); SBAR(); LDB(1,9); SBAR();
MMA(z14,A[4],B0[3]); SBAR(); ROR2(t1_8,t2_7,z8[1],t1_9,t2_8,z9[1]); SBAR();
MMA(z15,A[4],B1[3]); SBAR(); SHX(); SBAR();
MMA(z14,A[3],B0[4]); SBAR(); SHR2(t1_8,t2_8,z8[1],t1_9,t2_9,z9[1]); SBAR();
MMA(z15,A[3],B1[4]); SBAR(); FMD(z8,xa_); FMD(z9,xb_); SBAR();
MMA(z14,A[2],B0[5]); SBAR(); ROR2(u_8,t1_7,z8[0],u_9,t1_8,z9[0]); SBAR();
MMA(z15,A[2],B1[5]); SBAR(); LDX(5); SBAR();
MMA(z14,A[1],B0[6]); SBAR(); SHR2(u_8,t1_8,z8[0],u_9,t1_9,z9[0]); SBAR();
MMA(z15,A[1],B1[6]); SBAR(); yw[1][0]=pk(u_8,u_9); PIN(yw[1]); SBAR();
MMA(z14,A[0],B0[7]); SBAR(); ROR2(t2_10,z9[3],z10[2],t2_11,z10[3],z11[2]); SBAR();
MMA(z15,A[0],B1[7]); SBAR(); LDB(0,10); SBAR();
MM0(z16,A[8],B0[0]); SBAR(); SHR2(t2_10,z10[3],z10[2],t2_11,z11[3],z11[2]); SBAR();
MM0(z17,A[8],B1[0]); SBAR(); LDB(1,10); SBAR();
MMA(z16,A[7],B0[1]); SBAR(); ROR2(t1_10,t2_9,z10[1],t1_11,t2_10,z11[1]); SBAR();
MMA(z17,A[7],B1[1]); SBAR(); SHX(); SBAR();
MMA(z16,A[6],B0[2]); SBAR(); SHR2(t1_10,t2_10,z10[1],t1_11,t2_11,z11[1]); SBAR();
MMA(z17,A[6],B1[2]); SBAR(); FMD(z10,xa_); FMD(z11,xb_); SBAR();
MMA(z16,A[5],B0[3]); SBAR(); ROR2(u_10,t1_9,z10[0],u_11,t1_10,z11[0]); SBAR();
MMA(z17,A[5],B1[3]); SBAR(); LDX(6); SBAR();
MMA(z16,A[4],B0[4]); SBAR(); SHR2(u_10,t1_10,z10[0],u_11,t1_11,z11[0]); SBAR();
MMA(z17,A[4],B1[4]); SBAR(); yw[1][1]=pk(u_10,u_11); PIN(yw[1]); SBAR();
MMA(z16,A[3],B0[5]); SBAR(); ROR2(t2_12,z11[3],z12[2],t2_13,z12[3],z13[2]); SBAR();
MMA(z17,A[3],B1[5]); SBAR(); LDB(0,11); SBAR();
MMA(z16,A[2],B0[6]); SBAR(); SHR2(t2_12,z12[3],z12[2],t2_13,z13[3],z13[2]); SBAR();
MMA(z17,A[2],B1[6]); SBAR(); LDB(1,11); SBAR();
MMA(z16,A[1],B0[7]); SBAR(); ROR2(t1_12,t2_11,z12[1],t1_13,t2_12,z13[1]); SBAR();
MMA(z17,A[1],B1[7]); SBAR(); SHX(); SBAR();
MMA(z16,A[0],B0[8]); SBAR(); SHR2(t1_12,t2_12,z12[1],t1_13,t2_13,z13[1]); SBAR();
MMA(z17,A[0],B1[8]); SBAR(); FMD(z12,xa_); FMD(z13,xb_); SBAR();
MM0(z18,A[9],B0[0]); SBAR(); ROR2(u_12,t1_11,z12[0],u_13,t1_12,z13[0]); SBAR();
MM0(z19,A[9],B1[0]); SBAR(); LDX(7); SBAR();
MMA(z18,A[8],B0[1]); SBAR(); SHR2(u_12,t1_12,z12[0],u_13,t1_13,z13[0]); SBAR();
MMA(z19,A[8],B1[1]); SBAR(); yw[1][2]=pk(u_12,u_13); PIN(yw[1]); SBAR();
MMA(z18,A[7],B0[2]); SBAR(); ROR2(t2_14,z13[3],z14[2],t2_15,z14[3],z15[2]); SBAR();
MMA(z19,A[7],B1[2]); SBAR(); LDB(0,12); SBAR();
MMA(z18,A[6],B0[3]); SBAR(); SHR2(t2_14,z14[3],z14[2],t2_15,z15[3],z15[2]); SBAR();
MMA(z19,A[6],B1[3]); SBAR(); LDB(1,12); SBAR();
MMA(z18,A[5],B0[4]); SBAR(); ROR2(t1_14,t2_13,z14[1],t1_15,t2_14,z15[1]); SBAR();
MMA(z19,A[5],B1[4]); SBAR(); SHX(); SBAR();
MMA(z18,A[4],B0[5]); SBAR(); SHR2(t1_14,t2_14,z14[1],t1_15,t2_15,z15[1]); SBAR();
MMA(z19,A[4],B1[5]); SBAR(); FMD(z14,xa_); FMD(z15,xb_); SBAR();
MMA(z18,A[3],B0[6]); SBAR(); ROR2(u_14,t1_13,z14[0],u_15,t1_14,z15[0]); SBAR();
MMA(z19,A[3],B1[6]); SBAR(); LDX(8); SBAR();
MMA(z18,A[2],B0[7]); SBAR(); SHR2(u_14,t1_14,z14[0],u_15,t1_15,z15[0]); SBAR();
MMA(z19,A[2],B1[7]); SBAR(); yw[1][3]=pk(u_14,u_15); PIN(yw[1]); STY(1); SBAR();
MMA(z18,A[1],B0[8]); SBAR(); ROR2(t2_16,z15[3],z16[2],t2_17,z16[3],z17[2]); SBAR();
MMA(z19,A[1],B1[8]); SBAR(); LDB(0,13); SBAR();
MMA(z18,A[0],B0[9]); SBAR(); SHR2(t2_16,z16[3],z16[2],t2_17,z17[3],z17[2]); SBAR();
MMA(z19,A[0],B1[9]); SBAR(); LDB(1,13); SBAR();
MM0(z20,A[10],B0[0]); SBAR(); ROR2(t1_16,t2_15,z16[1],t1_17,t2_16,z17[1]); SBAR();
MM0(z21,A[10],B1[0]); SBAR(); SHX(); SBAR();
MMA(z20,A[9],B0[1]); SBAR(); SHR2(t1_16,t2_16,z16[1],t1_17,t2_17,z17[1]); SBAR();
MMA(z21,A[9],B1[1]); SBAR(); FMD(z16,xa_); FMD(z17,xb_); SBAR();
MMA(z20,A[8],B0[2]); SBAR(); ROR2(u_16,t1_15,z16[0],u_17,t1_16,z17[0]); SBAR();
MMA(z21,A[8],B1[2]); SBAR(); LDX(9); SBAR();
MMA(z20,A[7],B0[3]); SBAR(); SHR2(u_16,t1_16,z16[0],u_17,t1_17,z17[0]); SBAR();
MMA(z21,A[7],B1[3]); SBAR(); yw[2][0]=pk(u_16,u_17); PIN(yw[2]); SBAR();
MMA(z20,A[6],B0[4]); SBAR(); ROR2(t2_18,z17[3],z18[2],t2_19,z18[3],z19[2]); SBAR();
MMA(z21,A[6],B1[4]); SBAR(); LDB(0,14); SBAR();
MMA(z20,A[5],B0[5]); SBAR(); SHR2(t2_18,z18[3],z18[2],t2_19,z19[3],z19[2]); SBAR();
MMA(z21,A[5],B1[5]); SBAR(); LDB(1,14); SBAR();
MMA(z20,A[4],B0[6]); SBAR(); ROR2(t1_18,t2_17,z18[1],t1_19,t2_18,z19[1]); SBAR();
MMA(z21,A[4],B1[6]); SBAR(); SHX(); SBAR();
MMA(z20,A[3],B0[7]); SBAR(); SHR2(t1_18,t2_18,z18[1],t1_19,t2_19,z19[1]); SBAR();
MMA(z21,A[3],B1[7]); SBAR(); FMD(z18,xa_); FMD(z19,xb_); SBAR();
MMA(z20,A[2],B0[8]); SBAR(); ROR2(u_18,t1_17,z18[0],u_19,t1_18,z19[0]); SBAR();
MMA(z21,A[2],B1[8]); SBAR(); LDX(10); SBAR();
MMA(z20,A[1],B0[9]); SBAR(); SHR2(u_18,t1_18,z18[0],u_19,t1_19,z19[0]); SBAR();
MMA(z21,A[1],B1[9]); SBAR(); yw[2][1]=pk(u_18,u_19); PIN(yw[2]); SBAR();
MMA(z20,A[0],B0[10]); SBAR();  SBAR();
MMA(z21,A[0],B1[10]); SBAR();  SBAR();
MM0(z22,A[11],B0[0]); SBAR();  SBAR();
MM0(z23,A[11],B1[0]); SBAR();  SBAR();
MMA(z22,A[10],B0[1]); SBAR();  SBAR();
MMA(z23,A[10],B1[1]); SBAR();  SBAR();
MMA(z22,A[9],B0[2]); SBAR();  SBAR();
MMA(z23,A[9],B1[2]); SBAR();  SBAR();
MMA(z22,A[8],B0[3]); SBAR();  SBAR();
MMA(z23,A[8],B1[3]); SBAR();  SBAR();
MMA(z22,A[7],B0[4]); SBAR(); ROR2(t2_20,z19[3],z20[2],t2_21,z20[3],z21[2]); SBAR();
MMA(z23,A[7],B1[4]); SBAR(); LDB(0,15); SBAR();
MMA(z22,A[6],B0[5]); SBAR(); SHR2(t2_20,z20[3],z20[2],t2_21,z21[3],z21[2]); SBAR();
MMA(z23,A[6],B1[5]); SBAR(); LDB(1,15); SBAR();
MMA(z22,A[5],B0[6]); SBAR(); ROR2(t1_20,t2_19,z20[1],t1_21,t2_20,z21[1]); SBAR();
MMA(z23,A[5],B1[6]); SBAR(); SHX(); SBAR();
MMA(z22,A[4],B0[7]); SBAR(); SHR2(t1_20,t2_20,z20[1],t1_21,t2_21,z21[1]); SBAR();
MMA(z23,A[4],B1[7]); SBAR(); FMD(z20,xa_); FMD(z21,xb_); SBAR();
MMA(z22,A[3],B0[8]); SBAR(); ROR2(u_20,t1_19,z20[0],u_21,t1_20,z21[0]); SBAR();
MMA(z23,A[3],B1[8]); SBAR(); LDX(11); SBAR();
MMA(z22,A[2],B0[9]); SBAR(); SHR2(u_20,t1_20,z20[0],u_21,t1_21,z21[0]); SBAR();
MMA(z23,A[2],B1[9]); SBAR(); yw[2][2]=pk(u_20,u_21); PIN(yw[2]); SBAR();
MMA(z22,A[1],B0[10]); SBAR();  SBAR();
MMA(z23,A[1],B1[10]); SBAR();  SBAR();
MMA(z22,A[0],B0[11]); SBAR();  SBAR();
MMA(z23,A[0],B1[11]); SBAR();  SBAR();
MM0(z24,A[12],B0[0]); SBAR();  SBAR();
MM0(z25,A[12],B1[0]); SBAR();  SBAR();
MMA(z24,A[11],B0[1]); SBAR();  SBAR();
MMA(z25,A[11],B1[1]); SBAR();  SBAR();
MMA(z24,A[10],B0[2]); SBAR();  SBAR();
MMA(z25,A[10],B1[2]); SBAR();  SBAR();
MMA(z24,A[9],B0[3]); SBAR();  SBAR();
MMA(z25,A[9],B1[3]); SBAR();  SBAR();
MMA(z24,A[8],B0[4]); SBAR(); ROR2(t2_22,z21[3],z22[2],t2_23,z22[3],z23[2]); SBAR();
MMA(z25,A[8],B1[4]); SBAR(); VNOP(); SBAR();
MMA(z24,A[7],B0[5]); SBAR(); SHR2(t2_22,z22[3],z22[2],t2_23,z23[3],z23[2]); SBAR();
MMA(z25,A[7],B1[5]); SBAR(); VNOP(); SBAR();
MMA(z24,A[6],B0[6]); SBAR(); ROR2(t1_22,t2_21,z22[1],t1_23,t2_22,z23[1]); SBAR();
MMA(z25,A[6],B1[6]); SBAR(); SHX(); SBAR();
MMA(z24,A[5],B0[7]); SBAR(); SHR2(t1_22,t2_22,z22[1],t1_23,t2_23,z23[1]); SBAR();
MMA(z25,A[5],B1[7]); SBAR(); FMD(z22,xa_); FMD(z23,xb_); SBAR();
MMA(z24,A[4],B0[8]); SBAR(); ROR2(u_22,t1_21,z22[0],u_23,t1_22,z23[0]); SBAR();
MMA(z25,A[4],B1[8]); SBAR(); LDX(12); SBAR();
MMA(z24,A[3],B0[9]); SBAR(); SHR2(u_22,t1_22,z22[0],u_23,t1_23,z23[0]); SBAR();
MMA(z25,A[3],B1[9]); SBAR(); yw[2][3]=pk(u_22,u_23); PIN(yw[2]); STY(2); SBAR();
MMA(z24,A[2],B0[10]); SBAR();  SBAR();
MMA(z25,A[2],B1[10]); SBAR();  SBAR();
MMA(z24,A[1],B0[11]); SBAR();  SBAR();
MMA(z25,A[1],B1[11]); SBAR();  SBAR();
MMA(z24,A[0],B0[12]); SBAR();  SBAR();
MMA(z25,A[0],B1[12]); SBAR();  SBAR();
MM0(z26,A[13],B0[0]); SBAR();  SBAR();
MM0(z27,A[13],B1[0]); SBAR();  SBAR();
MMA(z26,A[12],B0[1]); SBAR();  SBAR();
MMA(z27,A[12],B1[1]); SBAR();  SBAR();
MMA(z26,A[11],B0[2]); SBAR();  SBAR();
MMA(z27,A[11],B1[2]); SBAR();  SBAR();
MMA(z26,A[10],B0[3]); SBAR();  SBAR();
MMA(z27,A[10],B1[3]); SBAR();  SBAR();
MMA(z26,A[9],B0[4]); SBAR(); ROR2(t2_24,z23[3],z24[2],t2_25,z24[3],z25[2]); SBAR();
MMA(z27,A[9],B1[4]); SBAR(); VNOP(); SBAR();
MMA(z26,A[8],B0[5]); SBAR(); SHR2(t2_24,z24[3],z24[2],t2_25,z25[3],z25[2]); SBAR();
MMA(z27,A[8],B1[5]); SBAR(); VNOP(); SBAR();
MMA(z26,A[7],B0[6]); SBAR(); ROR2(t1_24,t2_23,z24[1],t1_25,t2_24,z25[1]); SBAR();
MMA(z27,A[7],B1[6]); SBAR(); SHX(); SBAR();
MMA(z26,A[6],B0[7]); SBAR(); SHR2(t1_24,t2_24,z24[1],t1_25,t2_25,z25[1]); SBAR();
MMA(z27,A[6],B1[7]); SBAR(); FMD(z24,xa_); FMD(z25,xb_); SBAR();
MMA(z26,A[5],B0[8]); SBAR(); ROR2(u_24,t1_23,z24[0],u_25,t1_24,z25[0]); SBAR();
MMA(z27,A[5],B1[8]); SBAR(); LDX(13); SBAR();
MMA(z26,A[4],B0[9]); SBAR(); SHR2(u_24,t1_24,z24[0],u_25,t1_25,z25[0]); SBAR();
MMA(z27,A[4],B1[9]); SBAR(); yw[3][0]=pk(u_24,u_25); PIN(yw[3]); SBAR();
MMA(z26,A[3],B0[10]); SBAR();  SBAR();
MMA(z27,A[3],B1[10]); SBAR();  SBAR();
MMA(z26,A[2],B0[11]); SBAR();  SBAR();
MMA(z27,A[2],B1[11]); SBAR();  SBAR();
MMA(z26,A[1],B0[12]); SBAR();  SBAR();
MMA(z27,A[1],B1[12]); SBAR();  SBAR();
MMA(z26,A[0],B0[13]); SBAR();  SBAR();
MMA(z27,A[0],B1[13]); SBAR();  SBAR();
MM0(z28,A[14],B0[0]); SBAR();  SBAR();
MM0(z29,A[14],B1[0]); SBAR();  SBAR();
MMA(z28,A[13],B0[1]); SBAR();  SBAR();
MMA(z29,A[13],B1[1]); SBAR();  SBAR();
MMA(z28,A[12],B0[2]); SBAR();  SBAR();
MMA(z29,A[12],B1[2]); SBAR();  SBAR();
MMA(z28,A[11],B0[3]); SBAR();  SBAR();
MMA(z29,A[11],B1[3]); SBAR();  SBAR();
MMA(z28,A[10],B0[4]); SBAR(); ROR2(t2_26,z25[3],z26[2],t2_27,z26[3],z27[2]); SBAR();
MMA(z29,A[10],B1[4]); SBAR(); VNOP(); SBAR();
MMA(z28,A[9],B0[5]); SBAR(); SHR2(t2_26,z26[3],z26[2],t2_27,z27[3],z27[2]); SBAR();
MMA(z29,A[9],B1[5]); SBAR(); VNOP(); SBAR();
MMA(z28,A[8],B0[6]); SBAR(); ROR2(t1_26,t2_25,z26[1],t1_27,t2_26,z27[1]); SBAR();
MMA(z29,A[8],B1[6]); SBAR(); SHX(); SBAR();
MMA(z28,A[7],B0[7]); SBAR(); SHR2(t1_26,t2_26,z26[1],t1_27,t2_27,z27[1]); SBAR();
MMA(z29,A[7],B1[7]); SBAR(); FMD(z26,xa_); FMD(z27,xb_); SBAR();
MMA(z28,A[6],B0[8]); SBAR(); ROR2(u_26,t1_25,z26[0],u_27,t1_26,z27[0]); SBAR();
MMA(z29,A[6],B1[8]); SBAR(); LDX(14); SBAR();
MMA(z28,A[5],B0[9]); SBAR(); SHR2(u_26,t1_26,z26[0],u_27,t1_27,z27[0]); SBAR();
MMA(z29,A[5],B1[9]); SBAR(); yw[3][1]=pk(u_26,u_27); PIN(yw[3]); SBAR();
MMA(z28,A[4],B0[10]); SBAR();  SBAR();
MMA(z29,A[4],B1[10]); SBAR();  SBAR();
MMA(z28,A[3],B0[11]); SBAR();  SBAR();
MMA(z29,A[3],B1[11]); SBAR();  SBAR();
MMA(z28,A[2],B0[12]); SBAR();  SBAR();
MMA(z29,A[2],B1[12]); SBAR();  SBAR();
MMA(z28,A[1],B0[13]); SBAR();  SBAR();
MMA(z29,A[1],B1[13]); SBAR();  SBAR();
MMA(z28,A[0],B0[14]); SBAR();  SBAR();
MMA(z29,A[0],B1[14]); SBAR();  SBAR();
MM0(z30,A[15],B0[0]); SBAR();  SBAR();
MM0(z31,A[15],B1[0]); SBAR();  SBAR();
MMA(z30,A[14],B0[1]); SBAR();  SBAR();
MMA(z31,A[14],B1[1]); SBAR();  SBAR();
MMA(z30,A[13],B0[2]); SBAR();  SBAR();
MMA(z31,A[13],B1[2]); SBAR();  SBAR();
MMA(z30,A[12],B0[3]); SBAR();  SBAR();
MMA(z31,A[12],B1[3]); SBAR();  SBAR();
MMA(z30,A[11],B0[4]); SBAR(); ROR2(t2_28,z27[3],z28[2],t2_29,z28[3],z29[2]); SBAR();
MMA(z31,A[11],B1[4]); SBAR(); VNOP(); SBAR();
MMA(z30,A[10],B0[5]); SBAR(); SHR2(t2_28,z28[3],z28[2],t2_29,z29[3],z29[2]); SBAR();
HOOK1(); SBAR();
MMA(z31,A[10],B1[5]); SBAR(); VNOP(); SBAR();
MMA(z30,A[9],B0[6]); SBAR(); ROR2(t1_28,t2_27,z28[1],t1_29,t2_28,z29[1]); SBAR();
MMA(z31,A[9],B1[6]); SBAR(); SHX(); SBAR();
MMA(z30,A[8],B0[7]); SBAR(); SHR2(t1_28,t2_28,z28[1],t1_29,t2_29,z29[1]); SBAR();
MMA(z31,A[8],B1[7]); SBAR(); FMD(z28,xa_); FMD(z29,xb_); SBAR();
MMA(z30,A[7],B0[8]); SBAR(); ROR2(u_28,t1_27,z28[0],u_29,t1_28,z29[0]); SBAR();
MMA(z31,A[7],B1[8]); SBAR(); LDX(15); SBAR();
MMA(z30,A[6],B0[9]); SBAR(); SHR2(u_28,t1_28,z28[0],u_29,t1_29,z29[0]); SBAR();
MMA(z31,A[6],B1[9]); SBAR(); yw[3][2]=pk(u_28,u_29); PIN(yw[3]); SBAR();
MMA(z30,A[5],B0[10]); SBAR();  SBAR();
MMA(z31,A[5],B1[10]); SBAR();  SBAR();
MMA(z30,A[4],B0[11]); SBAR();  SBAR();
MMA(z31,A[4],B1[11]); SBAR();  SBAR();
MMA(z30,A[3],B0[12]); SBAR();  SBAR();
MMA(z31,A[3],B1[12]); SBAR();  SBAR();
MMA(z30,A[2],B0[13]); SBAR();  SBAR();
MMA(z31,A[2],B1[13]); SBAR();  SBAR();
MMA(z30,A[1],B0[14]); SBAR();  SBAR();
MMA(z31,A[1],B1[14]); SBAR();  SBAR();
MMA(z30,A[0],B0[15]); SBAR();  SBAR();
MMA(z31,A[0],B1[15]); SBAR();  SBAR();
NOP1(); SBAR();  SBAR();
NOP1(); SBAR();  SBAR();
NOP1(); SBAR();  SBAR();
NOP1(); SBAR();  SBAR();
NOP1(); SBAR();  SBAR();
NOP1(); SBAR();  SBAR();
NOP1(); SBAR();  SBAR();
NOP1(); SBAR();  SBAR();
NOP1(); SBAR(); ROR2(t2_30,z29[3],z30[2],t2_31,z30[3],z31[2]); SBAR();
NOP1(); SBAR(); VNOP(); SBAR();
NOP1(); SBAR(); SHR2(t2_30,z30[3],z30[2],t2_31,z31[3],z31[2]); SBAR();
NOP1(); SBAR(); VNOP(); SBAR();
NOP1(); SBAR(); ROR2(t1_30,t2_29,z30[1],t1_31,t2_30,z31[1]); SBAR();
NOP1(); SBAR(); SHX(); SBAR();
NOP1(); SBAR(); SHR2(t1_30,t2_30,z30[1],t1_31,t2_31,z31[1]); SBAR();
NOP1(); SBAR(); FMD(z30,xa_); FMD(z31,xb_); SBAR();
NOP1(); SBAR(); ROR2(u_30,t1_29,z30[0],u_31,t1_30,z31[0]); SBAR();
NOP1(); SBAR(); VNOP(); SBAR();
NOP1(); SBAR(); SHR2(u_30,t1_30,z30[0],u_31,t1_31,z31[0]); SBAR();
NOP1(); SBAR(); yw[3][3]=pk(u_30,u_31); PIN(yw[3]); STY(3); SBAR();
      if(v==3 && lane<32) touch((unsigned)((j*32+lane)*2048+((xcd&1)*8+((3-(int)((uintptr_t)WO>>7))&7))*128),WO,junk);
      HOOK2();
    }
    if(!h1){
      wait_flags<4>(kvflag(ws,R,C,v>>1,1,lane),nonce);
      lds_wait(cnt+C_KV1,2);
      kv_fetch(ws,lds0,R,C,v>>1,1,v&1,lane);
    }
    WAITV(0);
    arrive_publish(cnt+C_H1,4,flagp(ws,b,FK_Y),nonce,lane);
    lds_wait(cnt+C_H1,4); lds_wait(cnt+C_P1E,4);
#pragma unroll 1
    for(int i=v;i<WO_P1;i+=4) wo_block(i);
    if(v<2) touch((unsigned)((512*(j>>3)+8*(j&7)+64*(lane>>3)+(lane&7))*2048+(xcd*2+v)*128),Aq,junk);
    __builtin_amdgcn_s_setprio(0);
  } else {
    const int wr=wid>>1, wc=wid&1;
    const int r=lane&31, h=lane>>5;
    lptr L=(lptr)lds;
    lptr xa[4];
    { int row=wr*64+r; int sw=(row>>1)&7;
#pragma unroll
      for(int ks=0;ks<4;ks++) xa[ks]=L+row*128+(((2*ks+h)^sw)*16); }
    lptr wa;
    { int blk=(lane>>4)&1,q=(lane&15)>>2,p=lane&3; int c=(wc*4+2*blk+(p>>1))^(((q>>1)&1)<<2); wa=L+16384+(8*h+q)*128+c*16+8*(p&1); }
    f32x16 aq[2],ak[2],av[2];
#pragma unroll
    for(int i=0;i<2;i++){ aq[i]=f32x16{}; ak[i]=f32x16{}; av[i]=f32x16{}; }
    G1F F0,F1;
#define G1_RX(F,ks) do{ F.x0=ldsr128(xa[ks]+so); F.x1=ldsr128(xa[ks]+so+4096); }while(0)
#define G1_RW(F,ks,sg) do{ F.w[2*(sg)]=ldstr(wa+so+(sg)*8192+(16*(ks))*128); F.w[2*(sg)+1]=ldstr(wa+so+(sg)*8192+(16*(ks)+4)*128); }while(0)
#define G1_W(F,sg) cat(F.w[2*(sg)],F.w[2*(sg)+1])
#define G1_STEP(F,N,ks,LD) do{ \
    ak[0]=MFMA32(G1_W(F,1),F.x0,ak[0]); PIN(ak[0]); if(LD) G1_RX(N,ks); SBAR(); \
    ak[1]=MFMA32(G1_W(F,1),F.x1,ak[1]); PIN(ak[1]); if(LD) G1_RW(N,ks,1); SBAR(); \
    av[0]=MFMA32(F.x0,G1_W(F,2),av[0]); PIN(av[0]); if(LD) G1_RW(N,ks,2); SBAR(); \
    av[1]=MFMA32(F.x1,G1_W(F,2),av[1]); PIN(av[1]); if(LD) G1_RW(N,ks,0); SBAR(); \
    aq[0]=MFMA32(G1_W(F,0),F.x0,aq[0]); PIN(aq[0]); SBAR(); \
    aq[1]=MFMA32(G1_W(F,0),F.x1,aq[1]); PIN(aq[1]); SBAR(); }while(0)
    __builtin_amdgcn_s_setprio(3);
    asm volatile("":"+v"(aq[0]),"+v"(aq[1]),"+v"(ak[0]),"+v"(ak[1]),"+v"(av[0]),"+v"(av[1]));
    WAITV(6);
    BAR();
    int so=0;
    G1_RX(F0,0); G1_RW(F0,0,1); G1_RW(F0,0,2); G1_RW(F0,0,0); SBAR();
#pragma unroll 1
    for(int t=0;t<14;t++){
      G1_STEP(F0,F1,1,true);
      G1_STEP(F1,F0,2,true);
      G1_STEP(F0,F1,3,true);
      so=(so==2*G1_SLOT)?0:so+G1_SLOT;
      if(t==0) WAITV(0);
      WAITL(); BAR(); SBAR();
      G1_STEP(F1,F0,0,true);
    }
    G1F E[8]; E[0]=F0;
#define G1_KV(n,LD) do{ \
    ak[0]=MFMA32(G1_W(E[n],1),E[n].x0,ak[0]); PIN(ak[0]); if(LD) G1_RX(E[(n)+1],((n)+1)&3); SBAR(); \
    ak[1]=MFMA32(G1_W(E[n],1),E[n].x1,ak[1]); PIN(ak[1]); if(LD) G1_RW(E[(n)+1],((n)+1)&3,1); SBAR(); \
    av[0]=MFMA32(E[n].x0,G1_W(E[n],2),av[0]); PIN(av[0]); if(LD) G1_RW(E[(n)+1],((n)+1)&3,2); SBAR(); \
    av[1]=MFMA32(E[n].x1,G1_W(E[n],2),av[1]); PIN(av[1]); if(LD) G1_RW(E[(n)+1],((n)+1)&3,0); SBAR(); }while(0)
#define G1_Q(n) do{ aq[0]=MFMA32(G1_W(E[n],0),E[n].x0,aq[0]); PIN(aq[0]); aq[1]=MFMA32(G1_W(E[n],0),E[n].x1,aq[1]); PIN(aq[1]); SBAR(); }while(0)
    G1_KV(0,true); G1_KV(1,true); G1_KV(2,true);
    LOAD_NONCE();
    so=0;
    lds_wait(cnt+C_S15,1); SBAR();
    G1_KV(3,true); G1_KV(4,true); G1_KV(5,true); G1_KV(6,true);
    arrive(cnt+C_RD);
    SBAR();
    { const int n=7; ak[0]=MFMA32(G1_W(E[n],1),E[n].x0,ak[0]); ak[1]=MFMA32(G1_W(E[n],1),E[n].x1,ak[1]); av[0]=MFMA32(E[n].x0,G1_W(E[n],2),av[0]); av[1]=MFMA32(E[n].x1,G1_W(E[n],2),av[1]);
      PIN(ak[0]); PIN(ak[1]); PIN(av[0]); PIN(av[1]); SBAR(); }
    const float qs=0.17677669529663687f*1.4426950408889634f;
    u32x4 kf[2][2],vf[2][2];
    const unsigned go=WS_KV+(unsigned)b*32768u+wc*16384u+(2*wr)*2048u+lane*16u;
#define KV_STORE() _Pragma("unroll") for(int rt=0;rt<2;rt++) _Pragma("unroll") for(int s=0;s<2;s++) __builtin_amdgcn_raw_buffer_store_b128(kf[rt][s],wsr,go+rt*2048+s*1024,0,SC1); \
      _Pragma("unroll") for(int rt=0;rt<2;rt++) _Pragma("unroll") for(int s=0;s<2;s++) __builtin_amdgcn_raw_buffer_store_b128(vf[rt][s],wsr,go+8192+rt*2048+s*1024,0,SC1)
    G1_Q(0);
    kf[0][0]=pack8(ak[0],0); kf[0][1]=pack8(ak[0],1); PIN(kf[0][0]); PIN(kf[0][1]);
    kf[1][0]=pack8(ak[1],0); kf[1][1]=pack8(ak[1],1); PIN(kf[1][0]); PIN(kf[1][1]); SBAR(); G1_Q(1);
    vf[0][0]=pack8(av[0],0); vf[0][1]=pack8(av[0],1); PIN(vf[0][0]); PIN(vf[0][1]);
    vf[1][0]=pack8(av[1],0); vf[1][1]=pack8(av[1],1); PIN(vf[1][0]); PIN(vf[1][1]); SBAR();
    if(wc==0){ KV_STORE(); }
    SBAR();
    G1_Q(2); G1_Q(3); G1_Q(4); G1_Q(5); G1_Q(6); G1_Q(7);
    lds_wait(cnt+C_RD,4);
#pragma unroll
    for(int rt=0;rt<2;rt++){
      lwptr lq=(lwptr)lds+(1-wc)*HR+49152+(2*wr+rt)*2048+lane*16;
#pragma unroll
      for(int s=0;s<2;s++) *(LAS u32x4*)(lq+s*1024)=pack8(aq[rt],s,qs);
    }
    if(R<2){
#pragma unroll 1
      for(int i=0;i<8*(2-R);i++) *(LAS u32x4*)((lwptr)lds+(1-wc)*HR+wr*24576+i*1024+lane*16)=u32x4{0,0,0,0};
    }
    if(wc==1){ lds_wait(cnt+C_KV0,2); KV_STORE(); }
    WAITV(0);
    arrive_publish(cnt+C_KV0+wc,2,flagp(ws,b,FK_KV0+wc),nonce,lane);
    if(wid==0){
      lds_wait(cnt+C_KV0,2);
      const int kind=(lane>>2)&1, ri=lane&3;
      const gu64* fp=kvflag(ws,R,C,kind,0,ri);
      unsigned todo=(R>=2)?0x77u:((R==1)?0x66u:0x44u);
#pragma unroll 1
      for(unsigned sp=0;sp<SPIN_CAP && todo;sp++){
        u64 fv=0;
        if(lane<8 && ((todo>>lane)&1)) fv=__hip_atomic_load(fp,RLX_AGENT);
        unsigned rdy=(unsigned)__builtin_amdgcn_ballot_w64(fv==nonce)&todo;
        MEMFENCE();
        todo&=~rdy;
        if(!rdy) __builtin_amdgcn_s_sleep(2);
#pragma unroll 1
        while(rdy){
          const int bit=__builtin_ctz(rdy); rdy&=rdy-1;
          const int kd=bit>>2, i=bit&3;
          const char* src=ws+WS_KV+(size_t)blk_of(R-2+i,(C&12)|((C-1-kd)&3))*32768+kd*8192;
          const unsigned dst=lds0+HR+(1-kd)*24576+i*8192;
          dma4_sc1(lane*16,src,dst); dma4_sc1(lane*16,src+4096,dst+4096);
        }
      }
      arrive(cnt+C_HI);
      WAITV(0);
      arrive(cnt+C_H0K);
    }
    __builtin_amdgcn_s_setprio(1);
  }
  const int late=wid>>2;
#pragma unroll 1
  for(int rep=(wid==2)?0:1;rep<2;rep++){
    if(rep==1 && wid>0 && wid<4) lds_wait(cnt+C_H0K,1);
    if(R>=2) attention<false>(lds,cnt,ws,wsr,b,R,C,wid,rep==0,lane,nonce);
    else     attention<true >(lds,cnt,ws,wsr,b,R,C,wid,rep==0,lane,nonce);
  }
  if(!late){
    WAITV(0);
    arrive_publish(cnt+C_O0,4,flagp(ws,b,FK_O0),nonce,lane);
    if(wid<3){ lds_wait(cnt+C_E,4); lds_wait(cnt+C_P1L,4);
#pragma unroll 1
      for(int i=WO_P1+wid;i<32;i+=3) wo_block(i);
    }
    const int T2=R2*4+wid;
    const unsigned ob=WS_O+(unsigned)T2*65536u+lane*16u;
    u32x4 of[G2D][2];
#define G2_LDO(i,n) do{ const unsigned so_=(unsigned)((((n)+KO)&15)*4096); of[(i)%G2D][0]=__builtin_amdgcn_raw_buffer_load_b128(wsr,obv,so_,SC1); of[(i)%G2D][1]=__builtin_amdgcn_raw_buffer_load_b128(wsr,obv+1024,so_,SC1); }while(0)
    const int h=lane>>5,blk=(lane>>4)&1,q=(lane&15)>>2,p=lane&3;
    lptr wa[2];
#pragma unroll
    for(int ct=0;ct<2;ct++){ int c=(ct*4+2*blk+(p>>1))^(((q>>1)&1)<<2); wa[ct]=(lptr)lds+L_WO+(31-G2D)*4096+(4*h+q)*128+c*16+8*(p&1); }
    f32x16 acc[2]; acc[0]=f32x16{}; acc[1]=f32x16{};
    s16x4 wf[2][8];
#define G2_LDW(i) do{ _Pragma("unroll") for(int s_=0;s_<2;s_++) _Pragma("unroll") for(int ct_=0;ct_<2;ct_++){ lptr a_=wa[ct_]+(G2D-(i))*4096+s_*2048; \
      wf[(i)&1][4*s_+2*ct_]=ldstr(a_); wf[(i)&1][4*s_+2*ct_+1]=ldstr(a_+1024); } }while(0)
    lds_wait(cnt+C_W1,4);
    G2_LDW(0);
    PIN(acc[0]); PIN(acc[1]);
    if(wid==3){ wait_flags(flagp(ws,blk_of(R2,lane&15),FK_O0),nonce); arrive(cnt+C_EF); } else lds_wait(cnt+C_EF,1);
    unsigned obv=ob;
#pragma unroll
    for(int i=0;i<G2D;i++) G2_LDO(i,i);
    u64 f1=0;
    constexpr int NIT=32/G2D, ITL=16/G2D-1;
#pragma unroll 1
    for(int it=0;it<NIT;it++){
      if(it==1) arrive(cnt+C_W2);
      if(it==ITL){
        if(wid==3){ if(!allof(f1==nonce)) wait_flags(flagp(ws,blk_of(R2,lane&15),FK_O1),nonce); MEMFENCE(); arrive(cnt+C_LF); }
        else lds_wait(cnt+C_LF,1);
        __builtin_amdgcn_s_setprio(3);
      }
      if(it==ITL+1) lds_wait(cnt+C_W2,4);
      if(it==NIT-1) arrive(cnt+C_G2);
      PIN(wa[0]); PIN(wa[1]);
      const int nb=G2D*it+G2D;
      obv=ob+((it<ITL)?0u:((it==NIT-1)?0x40000000u:2048u)); PIN(obv);
#pragma unroll
      for(int i=0;i<G2D;i++){
        SBAR();
        if(i==G2D-4){ if(wid==3 && it==ITL-1) f1=__hip_atomic_load(flagp(ws,blk_of(R2,lane&15),FK_O1),RLX_AGENT); }
        G2_LDW(i+1);
#pragma unroll
        for(int s=0;s<2;s++)
#pragma unroll
          for(int ct=0;ct<2;ct++) acc[ct]=MFMA32(cat(wf[i&1][4*s+2*ct],wf[i&1][4*s+2*ct+1]),B8(of[i][s]),acc[ct]);
#pragma unroll
        for(int g_=0;g_<4;g_++){ __builtin_amdgcn_sched_group_barrier(0x100,2,0); __builtin_amdgcn_sched_group_barrier(0x8,1,0); }
        PIN(acc[0]); PIN(acc[1]);
        SBAR();
        G2_LDO(i,nb+i);
      }
      wa[0]-=G2D*4096; wa[1]-=G2D*4096;
    }
    lwptr stg=(lwptr)lds+L_WO+(31-wid)*4096;
#pragma unroll
    for(int ct=0;ct<2;ct++){
#pragma unroll
      for(int gp=0;gp<2;gp++){
        unsigned a0=pk(acc[ct][8*gp],acc[ct][8*gp+1]), a1=pk(acc[ct][8*gp+2],acc[ct][8*gp+3]);
        unsigned b0=pk(acc[ct][8*gp+4],acc[ct][8*gp+5]), b1=pk(acc[ct][8*gp+6],acc[ct][8*gp+7]);
        auto r0=__builtin_amdgcn_permlane32_swap(a0,b0,false,false); auto r1=__builtin_amdgcn_permlane32_swap(a1,b1,false,false);
        u32x4 vv={r0[0],r1[0],r0[1],r1[1]};
        *(LAS u32x4*)(stg+(lane&31)*128+(((ct*4+gp*2+h)^(lane&7))*16))=vv;
      }
    }
#pragma unroll
    for(int i=0;i<4;i++){
      const int row=8*i+(lane>>3);
      const u32x4 vv=*(const LAS u32x4*)(stg+row*128+(((lane&7)^(row&7))*16));
      __builtin_amdgcn_raw_buffer_store_b128(vv,outr,(unsigned)(T2*32+row)*4096u+2048u+C2*128u+(lane&7)*16u,0,SC1);
    }
  } else {
    const int v=wid-4;
    const int chb=xcd*128+(v&1)*64, rb=512*(j>>3)+8*(j&7)+4*(v>>1)+64*(lane>>3);
    u32x4 qq[4],yy[4];
    const bool yr=__builtin_amdgcn_readfirstlane(*(volatile LAS unsigned*)(cnt+C_YF))!=0;
    MEMFENCE();
    unsigned yo=WS_Y+(unsigned)(chb+(lane>>2))*4096u+(unsigned)(j*128+(v>>1)*64+(lane&3)*16);
    unsigned yo1=yr? yo : 0x40000000u;
#pragma unroll
    for(int i=0;i<4;i++){ qq[i]=*(const u32x4*)(Aq+(size_t)(rb+i)*1024+chb+(lane&7)*8); yy[i]=__builtin_amdgcn_raw_buffer_load_b128(wsr,yo1,i*65536,SC1); }
    WAITV(8);
    arrive_publish(cnt+C_O1,4,flagp(ws,b,FK_O1),nonce,lane);
    __builtin_amdgcn_s_setprio(0);
    if(!yr){ wait_flags<8>(flagp(ws,(lane&31)*8+xcd,FK_Y),nonce);
#pragma unroll
      for(int i=0;i<4;i++) yy[i]=__builtin_amdgcn_raw_buffer_load_b128(wsr,yo,i*65536,SC1); }
    lds_wait(cnt+C_L,4);
    lwptr stg=(lwptr)lds+v*4096;
#pragma unroll
    for(int i=0;i<4;i++) *(LAS u32x4*)(stg+i*1024+lane*16)=yy[i];
    { const int g=lane>>4,q=(lane&15)>>2,p=lane&3;
      bf16x8 tt[4];
#pragma unroll
      for(int n=0;n<4;n++){ lptr a=(lptr)stg+(8*(2*n+(g>>1))+q)*64+(g&1)*32+8*p; tt[n]=cat(ldstr(a),ldstr(a+256)); }
      MEMFENCE();
      const int srow=16*(g&1)+(lane&15);
#pragma unroll
      for(int n=0;n<4;n++) *(LAS bf16x8*)(stg+srow*128+(((2*n+(g>>1))^(srow&7))*16))=tt[n];
    }
    u32x4 ro[4];
#pragma unroll
    for(int i=0;i<4;i++){
      const int row=8*i+(lane>>3);
      const u32x4 yt=*(const LAS u32x4*)(stg+row*128+(((lane&7)^(row&7))*16));
#pragma unroll
      for(int d=0;d<4;d++) ro[i][d]=pk(lo16(qq[i][d])*lo16(yt[d]),hi16(qq[i][d])*hi16(yt[d]));
      PIN(ro[i]);
    }
    if(b==0 && wid==7){
#pragma unroll 1
      for(unsigned sp=0;sp<SPIN_CAP;sp++){
        u64 f[4];
#pragma unroll
        for(int i=0;i<4;i++) f[i]=__hip_atomic_load(flagp(ws,i*64+lane,FK_Y),RLX_AGENT);
        if(allof(f[0]==nonce && f[1]==nonce && f[2]==nonce && f[3]==nonce)) break;
        __builtin_amdgcn_s_sleep(16);
      }
      if(lane==0) __hip_atomic_store((gu64*)(ws+F_E),nonce,RLX_AGENT);
    }
    lds_wait(cnt+C_G2,4);
#pragma unroll
    for(int i=0;i<4;i++) __builtin_amdgcn_raw_buffer_store_b128(ro[i],outr,(unsigned)(rb+i)*4096u+chb*2u+(lane&7)*16u,0,SC1);
  }
}

extern "C" void kernel_launch(void* const* d_in, const int* in_sizes, int n_in, void* d_out, int out_size, void* d_ws, size_t ws_size, hipStream_t stream){
  (void)hipFuncSetAttribute((const void*)fused,hipFuncAttributeMaxDynamicSharedMemorySize,LDS_BYTES);
  fused<<<256,512,LDS_BYTES,stream>>>((const u16*)d_in[0],(const u16*)d_in[1],(const u16*)d_in[2],(const u16*)d_in[3],(const u16*)d_in[4],(const u16*)d_in[5],
                                      (const u16*)d_in[6],(const u16*)d_in[7],(const u16*)d_in[8],(u16*)d_out,(char*)d_ws);
}
```

```cpp
#include <hip/hip_runtime.h>
#include <hip/hip_bf16.h>
#include <stdint.h>
#if defined(__HIP_DEVICE_COMPILE__)
#define NOPK __attribute__((target("no-packed-fp32-ops")))
#else
#define NOPK
#endif
typedef unsigned short u16;
typedef unsigned long long u64;
typedef short bf16x8 __attribute__((ext_vector_type(8)));
typedef short s16x4 __attribute__((ext_vector_type(4)));
typedef float f32x16 __attribute__((ext_vector_type(16)));
typedef float f32x4 __attribute__((ext_vector_type(4)));
typedef float f32x2 __attribute__((ext_vector_type(2)));
typedef __bf16 bf2 __attribute__((ext_vector_type(2)));
typedef unsigned u32x4 __attribute__((ext_vector_type(4)));
typedef unsigned u32x2 __attribute__((ext_vector_type(2)));
#define LAS __attribute__((address_space(3)))
#define GAS __attribute__((address_space(1)))
typedef LAS const char* lptr;
typedef LAS char* lwptr;
typedef GAS u64 gu64;
#define DI __device__ __forceinline__ NOPK
#define MFMA32(a,b,c) __builtin_amdgcn_mfma_f32_32x32x16_bf16((a),(b),(c),0,0,0)
#define MFMA16(a,b,c) __builtin_amdgcn_mfma_f32_16x16x32_bf16((a),(b),(c),0,0,0)
#define SBAR() __builtin_amdgcn_sched_barrier(0)
#define PIN(x) asm volatile("":"+v"(x))
#define MEMFENCE() asm volatile("":::"memory")
#define BAR() asm volatile("s_barrier":::"memory")
#define WAITV(n) asm volatile("s_waitcnt vmcnt(" #n ")":::"memory")
#define WAITL() asm volatile("s_waitcnt lgkmcnt(0)":::"memory")
#define RLX_AGENT __ATOMIC_RELAXED, __HIP_MEMORY_SCOPE_AGENT
#define RLX_WG __ATOMIC_RELAXED, __HIP_MEMORY_SCOPE_WORKGROUP
#define SC1 16
#define B8(x) __builtin_bit_cast(bf16x8,(x))

constexpr unsigned MB=1u<<20;
constexpr unsigned WS_KV=0, WS_O=8*MB, WS_XT=12*MB, WS_Y=16*MB, WS_F=20*MB, WS_END=21*MB;
constexpr unsigned F_E=WS_F, F_W=WS_F+4096;
enum { FK_KV0=0, FK_KV1, FK_O0, FK_O1, FK_X, FK_Y };
constexpr int G1_SLOT=40960;
constexpr int HR=57344;
constexpr int L_CV=126976, CV_W=8480, CV_H=128, CV_X=128+4096+160;
constexpr int L_MISC=L_CV+4*CV_W;
constexpr int L_WO=L_MISC-131072;
constexpr int WO_P1=19;
static_assert(L_WO+(32-WO_P1)*4096>=HR+24576 && L_WO>=24576,"wo image");
constexpr int L_CNT=L_MISC;
constexpr int L_JUNK=L_CNT+128;
constexpr int L_MASK=L_JUNK+256;
constexpr int LDS_BYTES=L_MASK+2048;
static_assert(LDS_BYTES<=163840,"lds");
enum { C_RD=0, C_S15, C_KV0, C_KV1, C_H0K, C_H1, C_P1E, C_P1L, C_E, C_L, C_W1, C_W2, C_O0, C_O1, C_EF, C_LF, C_XF, C_YF, C_G2, C_NUM };
constexpr int CODE2=42;
constexpr int G2D=8;
constexpr unsigned SPIN_CAP=1u<<17, LSPIN_CAP=1u<<22;

DI unsigned pk(float a,float b){ f32x2 v={a,b}; bf2 r=__builtin_convertvector(v,bf2); return __builtin_bit_cast(unsigned,r); }
DI float asf(unsigned u){ return __builtin_bit_cast(float,u); }
DI unsigned asu(float f){ return __builtin_bit_cast(unsigned,f); }
DI bool allof(bool p){ return __builtin_amdgcn_ballot_w64(p)==~0ull; }
DI float lo16(unsigned u){ return asf(u<<16); }
DI float hi16(unsigned u){ return asf(u&0xffff0000u); }
DI bf16x8 ldsr128(lptr p){ return *(const LAS bf16x8*)p; }
DI s16x4 ldstr(lptr p){ return __builtin_amdgcn_ds_read_tr16_b64_v4i16((LAS s16x4*)p); }
DI bf16x8 cat(s16x4 a,s16x4 b){ return __builtin_shufflevector(a,b,0,1,2,3,4,5,6,7); }
DI u32x4 pack8(const f32x16& x,int s,float sc){ u32x4 r; r[0]=pk(x[8*s]*sc,x[8*s+1]*sc); r[1]=pk(x[8*s+2]*sc,x[8*s+3]*sc); r[2]=pk(x[8*s+4]*sc,x[8*s+5]*sc); r[3]=pk(x[8*s+6]*sc,x[8*s+7]*sc); return r; }
DI u32x4 pack8(const f32x16& x,int s){ u32x4 r; r[0]=pk(x[8*s],x[8*s+1]); r[1]=pk(x[8*s+2],x[8*s+3]); r[2]=pk(x[8*s+4],x[8*s+5]); r[3]=pk(x[8*s+6],x[8*s+7]); return r; }
#define DMA4_BODY(FL) "s_mov_b32 m0, %2\n\ts_nop 0\n\t" \
   "global_load_lds_dwordx4 %0, %1" FL "\n\tglobal_load_lds_dwordx4 %0, %1 offset:1024" FL "\n\tglobal_load_lds_dwordx4 %0, %1 offset:2048" FL "\n\tglobal_load_lds_dwordx4 %0, %1 offset:3072" FL
DI void dma4(unsigned voff,const void* sbase,unsigned ldsaddr){ asm volatile(DMA4_BODY(" nt") :: "v"(voff), "s"(sbase), "s"(ldsaddr) : "memory"); }
DI void dma4_sc1(unsigned voff,const void* sbase,unsigned ldsaddr){ asm volatile(DMA4_BODY(" sc1") :: "v"(voff), "s"(sbase), "s"(ldsaddr) : "memory"); }
DI void dma4v(unsigned v0,unsigned v1,unsigned v2,unsigned v3,const void* sbase,unsigned ldsaddr){
  asm volatile("s_mov_b32 m0, %5\n\ts_nop 0\n\t"
    "global_load_lds_dwordx4 %0, %4\n\tglobal_load_lds_dwordx4 %1, %4 offset:1024\n\tglobal_load_lds_dwordx4 %2, %4 offset:2048\n\tglobal_load_lds_dwordx4 %3, %4 offset:3072"
    :: "v"(v0),"v"(v1),"v"(v2),"v"(v3), "s"(sbase), "s"(ldsaddr) : "memory");
}
DI void touch(unsigned voff,const void* sbase,unsigned junk){
  asm volatile("s_mov_b32 m0, %2\n\ts_nop 0\n\tglobal_load_lds_dword %0, %1" :: "v"(voff), "s"(sbase), "s"(junk) : "memory");
}
DI void touch64(const void* a,unsigned junk){
  asm volatile("s_mov_b32 m0, %1\n\ts_nop 0\n\tglobal_load_lds_dword %0, off" :: "v"(a), "s"(junk) : "memory");
}
template<int SL=2>
DI void wait_flags(const gu64* p,u64 nonce){
#pragma unroll 1
  for(unsigned sp=0;sp<SPIN_CAP;sp++){
    u64 v=__hip_atomic_load(p,RLX_AGENT);
    if(allof(v==nonce)) break;
    __builtin_amdgcn_s_sleep(SL);
  }
  MEMFENCE();
}
DI void lds_wait(LAS unsigned* p,unsigned n){
#pragma unroll 1
  for(unsigned sp=0;sp<LSPIN_CAP;sp++){
    unsigned v=__hip_atomic_load(p,RLX_WG);
    if(__builtin_amdgcn_readfirstlane(v)>=n) break;
    __builtin_amdgcn_s_sleep(1);
  }
  MEMFENCE();
}
DI void arrive(LAS unsigned* cnt){
  unsigned one=1u;
  asm volatile("s_mov_b64 exec, 1\n\tds_add_u32 %0, %1\n\ts_mov_b64 exec, -1" :: "v"(cnt), "v"(one) : "memory");
}
DI void arrive_publish(LAS unsigned* cnt,unsigned n,gu64* flag,u64 nonce,int lane){
  unsigned one=1u, old;
  asm volatile("s_mov_b64 exec, 1\n\tds_add_rtn_u32 %0, %1, %2\n\ts_mov_b64 exec, -1\n\ts_waitcnt lgkmcnt(0)" : "=&v"(old) : "v"(cnt), "v"(one) : "memory");
  if(__builtin_amdgcn_readfirstlane(old)==n-1){ if(lane==0) __hip_atomic_store(flag,nonce,RLX_AGENT); }
  MEMFENCE();
}
DI gu64* flagp(char* ws,int blk,int k){ return (gu64*)(ws+F_W+(unsigned)(blk*8+k)*128u); }
DI int blk_of(int R,int C){ return (((R&7)*4+(C&3))<<3)|((R>>3)*4+(C>>2)); }
DI float fmax3(float a,float b,float c){ return __builtin_fmaxf(__builtin_fmaxf(a,b),c); }
DI float xhalf_max(float m){ auto rr=__builtin_amdgcn_permlane32_swap(asu(m),asu(m),false,false); return __builtin_fmaxf(asf(rr[0]),asf(rr[1])); }
struct G1F { bf16x8 x0,x1; s16x4 w[6]; };

DI const gu64* kvflag(char* ws,int R,int C,int kind,int head,int lane){
  const int Cs=(C&12)|((C-1-kind)&3); const int rr=(lane<3)? lane : 2; const int Rl=(R-2+rr<0)? R : R-2+rr;
  return flagp(ws,blk_of(Rl,Cs),FK_KV0+head);
}
DI void kv_fetch(char* ws,unsigned lds0,int R,int C,int kind,int head,int half,int lane){
  const int Cs=(C&12)|((C-1-kind)&3);
#pragma unroll 1
  for(int i=(R<2)?2-R:0;i<3;i++){
    const char* src=ws+WS_KV+(size_t)blk_of(R-2+i,Cs)*32768+head*16384+kind*8192+half*4096;
    const unsigned dst=lds0+(1-head)*HR+(1-kind)*24576+i*8192+half*4096;
    dma4_sc1(lane*16,src,dst);
  }
}
template<bool CHECK>
DI void attention(char* lds,LAS unsigned* cnt,char* ws,__amdgpu_buffer_rsrc_t wsr,int b,int R,int C,int wid,int lane,u64 nonce){
  const int late=wid>>2, jq=wid&3;
  const int T=4*R+jq;
  lptr VL=(lptr)lds+(1-late)*HR+jq*2048+lane*16;
  lptr KL=VL+24576;
  lptr QL=(lptr)lds+(1-late)*HR+49152+jq*2048+lane*16;
  const f32x16 zero=f32x16{};
  const float ninf=-__builtin_inff();
  const int dl=(lane&31)-4*(lane>>5);
  bf16x8 kf[9][2];
  const bf16x8 q0=ldsr128(QL), q1=ldsr128(QL+1024);
  kf[8][0]=ldsr128(KL+8*2048); kf[8][1]=ldsr128(KL+8*2048+1024);
#pragma unroll
  for(int u=0;u<4;u++){ kf[u][0]=ldsr128(KL+u*2048); kf[u][1]=ldsr128(KL+u*2048+1024); }
  f32x16 sd,sm,s[3];
  float m;
#pragma unroll
  for(int n=0;n<11;n++){
    SBAR();
    if(n==0){ sd=MFMA32(kf[8][0],q0,zero); sd=MFMA32(kf[8][1],q1,sd); PIN(sd); }
    else if(n==1){ sm=MFMA32(kf[0][0],q0,zero); sm=MFMA32(kf[0][1],q1,sm); PIN(sm); }
    else if(n<9){ const int u=n-1; s[u%3]=MFMA32(kf[u][0],q0,zero); s[u%3]=MFMA32(kf[u][1],q1,s[u%3]); PIN(s[u%3]); }
    if(n<4){ const int u=n+4; kf[u][0]=ldsr128(KL+u*2048); kf[u][1]=ldsr128(KL+u*2048+1024); }
    if(n==3){
#pragma unroll
      for(int i=0;i<16;i++){ const int a=(i&3)+8*(i>>2); sm[i]=(a<=dl)?sd[i]:((CHECK&&T<8)?ninf:sm[i]); }
    }
    if(n>=3){
      const int t=n-3; const f32x16& x=(t==0)?sm:s[t%3];
      float a=fmax3(x[0],x[1],x[2]), c=fmax3(x[3],x[4],x[5]);
      a=fmax3(a,x[6],x[7]); c=fmax3(c,x[8],x[9]); a=fmax3(a,x[10],x[11]); c=fmax3(c,x[12],x[13]); a=fmax3(a,x[14],x[15]);
      if(t==0) m=__builtin_fmaxf(a,c);
      else { const float mm=fmax3(m,a,c); m=(!CHECK||T-8+t>=0)?mm:m; }
      PIN(m);
    }
  }
  SBAR();
  arrive(cnt+(late?C_P1L:C_P1E));
  if(late){
    WAITV(0); arrive(cnt+C_W1);
    lds_wait(cnt+C_E,4); __builtin_amdgcn_s_setprio(2);
  }
  bf16x8 vfr[2][2];
  vfr[0][0]=ldsr128(VL+8*2048); vfr[0][1]=ldsr128(VL+8*2048+1024);
  vfr[1][0]=ldsr128(VL); vfr[1][1]=ldsr128(VL+1024);
  const u32x4 mk0=*(const LAS u32x4*)((lptr)lds+L_MASK+lane*16), mk1=*(const LAS u32x4*)((lptr)lds+L_MASK+1024+lane*16);
  m=xhalf_max(m);
  f32x16 negm;
#pragma unroll
  for(int i=0;i<16;i++) negm[i]=-m;
  PIN(negm);
  const unsigned one2=((((lane>>2)^(lane>>4))&1)==0)?0x3f803f80u:0u;
  const u32x4 ones={one2,one2,one2,one2};
  f32x16 o; f32x4 ls; u64 fy=0;
  u32x4 p0,p1;
#pragma unroll
  for(int u=0;u<=8;u++){
    SBAR();
    if(u==2){ if(wid==4) fy=__hip_atomic_load(flagp(ws,(lane&31)*8+(b&7),FK_Y),RLX_AGENT); SBAR(); }
    if(u==6){ if(wid==4){ PIN(fy); if(allof(fy==nonce)) arrive(cnt+C_YF); } SBAR(); }
    u32x4 n0,n1;
    if(u+1<8){ const int t=u+1; s[t&1]=MFMA32(kf[t][0],q0,negm); s[t&1]=MFMA32(kf[t][1],q1,s[t&1]); }
    if(u<8){
      f32x16& x=(u==0)?sm:s[u&1];
#pragma unroll
      for(int i=0;i<16;i++) x[i]=__builtin_amdgcn_exp2f((u==0)?x[i]-m:x[i]);
      n0=pack8(x,0); n1=pack8(x,1);
      if(CHECK && u>0){ if(T-8+u<0){ n0=u32x4{0,0,0,0}; n1=u32x4{0,0,0,0}; } }
    }
    if(u==1){
      const u32x4 d0=p0&mk0, d1=p1&mk1;
      o=MFMA32(vfr[0][0],B8(d0),zero); o=MFMA32(vfr[0][1],B8(d1),o);
      o=MFMA32(vfr[1][0],B8(p0^d0),o); o=MFMA32(vfr[1][1],B8(p1^d1),o);
      vfr[1][0]=ldsr128(VL+1*2048); vfr[1][1]=ldsr128(VL+1*2048+1024);
      vfr[0][0]=ldsr128(VL+2*2048); vfr[0][1]=ldsr128(VL+2*2048+1024);
      ls=MFMA16(B8(ones),B8(p0),(f32x4{0,0,0,0})); ls=MFMA16(B8(ones),B8(p1),ls);
    } else if(u>1){
      const int t=u-1;
      o=MFMA32(vfr[t&1][0],B8(p0),o); o=MFMA32(vfr[t&1][1],B8(p1),o);
      if(t+2<8){ vfr[t&1][0]=ldsr128(VL+(t+2)*2048); vfr[t&1][1]=ldsr128(VL+(t+2)*2048+1024); }
      ls=MFMA16(B8(ones),B8(p0),ls); ls=MFMA16(B8(ones),B8(p1),ls);
    }
    if(u>1 && u<7 && !CHECK){
#pragma unroll
      for(int g_=0;g_<6;g_++){ __builtin_amdgcn_sched_group_barrier(0x8,1,0); __builtin_amdgcn_sched_group_barrier(0x402,4,0); }
    }
    if(u+1<8) PIN(s[(u+1)&1]);
    if(u>0){ PIN(ls); PIN(o); }
    if(u<8){ PIN(n0); PIN(n1); p0=n0; p1=n1; }
  }
  SBAR();
  arrive(cnt+(late?C_L:C_E));
  const float il=__builtin_amdgcn_rcpf(ls[0]);
  const unsigned oo=WS_O+(unsigned)(T*64+2*(2*C+late))*1024u+lane*16u;
  __builtin_amdgcn_raw_buffer_store_b128(pack8(o,0,il),wsr,oo,0,SC1);
  __builtin_amdgcn_raw_buffer_store_b128(pack8(o,1,il),wsr,oo+1024,0,SC1);
}

__global__ __launch_bounds__(512,2) NOPK void fused(const u16* Aq,const u16* Ak,const u16* Av,const u16* Ask,const u16* Ah,const u16* AD,
                                               const u16* X,const u16* W,const u16* WO,u16* out,char* ws){
  extern __shared__ __attribute__((aligned(16))) char lds[];
  const char* kpc; asm volatile("s_getpc_b64 %0":"=s"(kpc));
  asm volatile(""::"s"(X),"s"(W),"s"(ws),"s"(WO),"s"(Ak));
  const int tid=threadIdx.x, lane=tid&63; const int wid=__builtin_amdgcn_readfirstlane(tid>>6);
  const int b=blockIdx.x, xcd=b&7, j=b>>3;
  const int R=(xcd>>2)*8+(j>>2), C=(xcd&3)*4+(j&3);
  const int wgi=xcd*32+j;
  const int R2=((xcd>>1)^2)*4+(j>>3), C2=(xcd&1)*8+(j&7), KO=2*(j&7)+4*(j>>3);
  const unsigned lds0=(unsigned)(uintptr_t)lds;
  u64 ew=__hip_atomic_load((gu64*)(ws+F_E),RLX_AGENT);
  const unsigned a0=(unsigned)((lane>>3)*2048+(((lane&7)^(lane>>4))*16)), a1=(a0^64u)+(16384-1024);
  const unsigned b0=(unsigned)((lane>>3)*6144+(((lane&7)^(((lane>>4)&1)<<2))*16));
  const char* xb=(const char*)X+(size_t)R*128*2048; const char* wb=(const char*)W+(C&12)*128;
  const int cs[3]={(C&3)*128,2048+((C+1)&3)*128,4096+((C+2)&3)*128};
#pragma unroll
  for(int k=0;k<2;k++){
    asm volatile("s_mov_b32 m0, %3\n\ts_nop 0\n\tglobal_load_lds_dwordx4 %0, %2\n\tglobal_load_lds_dwordx4 %1, %2 offset:1024" :: "v"(a0),"v"(a1),"s"(xb+wid*32768+k*128),"s"(lds0+k*G1_SLOT+wid*2048) : "memory");
#pragma unroll
    for(int g=0;g<3;g++)
      asm volatile("s_mov_b32 m0, %2\n\ts_nop 0\n\tglobal_load_lds_dwordx4 %0, %1" :: "v"(b0),"s"(wb+cs[g]+wid*49152+k*(64*6144)),"s"(lds0+k*G1_SLOT+16384+g*8192+wid*1024) : "memory");
    if(k==0){ if(lane<2) touch((unsigned)((lane? 256+((j*2+(wid&1)<CODE2)?j*2+(wid&1):CODE2) : j*8+wid)*128),kpc,lds0+L_JUNK); }
    SBAR();
  }
  LAS unsigned* cnt=(LAS unsigned*)((lwptr)lds+L_CNT);
  const __amdgpu_buffer_rsrc_t wsr=__builtin_amdgcn_make_buffer_rsrc(ws,0,WS_END,0x00020000);
  const __amdgpu_buffer_rsrc_t outr=__builtin_amdgcn_make_buffer_rsrc(out,0,8*MB,0x00020000);
  u64 nonce;
#define LOAD_NONCE() do{ PIN(ew); nonce=((u64)__builtin_amdgcn_readfirstlane((unsigned)(ew>>32))<<32)|(unsigned)__builtin_amdgcn_readfirstlane((unsigned)ew); \
    nonce=nonce*6364136223846793005ull+1442695040888963407ull; }while(0)
  auto wo_block=[&](int i) NOPK __attribute__((always_inline)) {
    const int hd=2*((i+KO)&15)+(i>>4);
    const unsigned vo=(unsigned)((lane>>3)*2048+C2*128+(((lane&7)^(((lane>>4)&1)<<2))*16));
    dma4v(vo,vo+16384-1024,vo+2*(16384-1024),vo+3*(16384-1024),(const char*)WO+(size_t)hd*65536,lds0+L_WO+(31-i)*4096);
  };
  if(__builtin_expect(wid>=4,1)){
    const int v=wid-4;
    const unsigned junk=lds0+L_JUNK;
    if(__builtin_expect(wid==4,1)){
      xb+=256; wb+=2*64*6144;
      unsigned la=lds0+2*G1_SLOT;
      WAITV(6);
#pragma unroll 1
      for(int k=0;k<14;k++){
        WAITV(40); BAR();
#pragma unroll
        for(int g=0;g<4;g++){
          dma4v(a0,a1,a0+2*(16384-1024),a1+2*(16384-1024),xb+g*65536,la+g*4096);
          if(g<3){
            dma4v(b0,b0+(49152-1024),b0+2*(49152-1024),b0+3*(49152-1024),wb+cs[g],la+16384+g*8192);
            dma4v(b0,b0+(49152-1024),b0+2*(49152-1024),b0+3*(49152-1024),wb+cs[g]+32*6144,la+16384+g*8192+4096);
          }
        }
        xb+=128; wb+=64*6144; la+=G1_SLOT; if(la==lds0+3*G1_SLOT) la=lds0;
      }
      WAITV(40); BAR();
      WAITV(0); arrive(cnt+C_S15);
      LOAD_NONCE();
    } else if(wid==5){
      __builtin_amdgcn_s_setprio(3);
      const char* ta; unsigned tstep;
      { const int sg=lane/20, i=lane-sg*20; const bool isx=i<8; const int rem=i-8, kr=(xcd>>2)*32+j;
        tstep=isx?128u:64u*6144u;
        ta=(isx? (const char*)X+(size_t)((xcd>>2)*1024+((xcd&3)*32+j)*8+i)*2048 : (const char*)W+(kr*6144+(rem>>2)*2048+((xcd&3)*4+(rem&3))*128))+(size_t)tstep*(2+sg);
        tstep*=3; }
#define STOUCH(n) do{ if(lane<(n)) touch64(ta,junk); ta+=tstep; }while(0)
      STOUCH(60);
      if(j==0) *(LAS u32x4*)((lwptr)lds+L_CV+lane*16)=u32x4{0,0,0,0};
      if(lane<C_NUM) cnt[lane]=0u;
      { const int bq=lane&31, hi=lane>>5; u32x4 mk[2];
#pragma unroll
        for(int d=0;d<8;d++){ const int i=2*d; const int a=(i&3)+8*(i>>2)+4*hi; mk[d>>2][d&3]=((a<=bq)?0xffffu:0u)|((a+1<=bq)?0xffff0000u:0u); }
        *(LAS u32x4*)((lwptr)lds+L_MASK+lane*16)=mk[0]; *(LAS u32x4*)((lwptr)lds+L_MASK+1024+lane*16)=mk[1]; }
      WAITL();
      WAITV(7);
      BAR();
      WAITV(1);
      if(lane<6) asm volatile("s_mov_b32 m0, %2\n\ts_nop 0\n\tglobal_load_lds_dword %0, %1 sc1" :: "v"((unsigned)(F_W+(b*8+lane)*128)), "s"(ws), "s"(junk) : "memory");
      const unsigned pv=(unsigned)((lane>>4)*2048+(lane&15)*16);
      const char* ksrc=(const char*)Ak+(size_t)j*64*2048+xcd*256; const char* vsrc=(const char*)Av+(size_t)j*64*2048+xcd*256;
      if(j>0) asm volatile("s_mov_b32 m0, %2\n\ts_nop 0\n\tglobal_load_lds_dwordx4 %0, %1" :: "v"(pv^(7u<<5)), "s"(ksrc-4*2048), "s"(lds0+L_CV) : "memory");
      STOUCH(60); STOUCH(60);
      BAR();
      STOUCH(60); STOUCH(40);
      touch((unsigned)((wgi*64+lane)*128),WO,junk);
#pragma unroll 1
      for(int g2=0;g2<4;g2++){
        const unsigned p0=pv^(unsigned)(g2<<6), p1=p0^32u;
        dma4v(p0,p0+(8192-1024),p1+2*(8192-1024),p1+3*(8192-1024),ksrc+g2*32768,lds0+L_CV+1024+g2*4096);
        dma4v(p0,p0+(8192-1024),p1+2*(8192-1024),p1+3*(8192-1024),vsrc+g2*32768,lds0+L_CV+17408+g2*4096);
      }
      BAR();
      WAITV(0);
#pragma unroll 1
      for(int i=3;i<=12;i++) BAR();
      u64 fxv=__hip_atomic_load(flagp(ws,(lane&31)*8+xcd,FK_X),RLX_AGENT);
      BAR();
      SBAR(); PIN(fxv);
      LOAD_NONCE();
      if(allof(fxv==nonce)){ arrive(cnt+C_XF); WAITL(); }
      BAR();
      __builtin_amdgcn_s_setprio(0);
    } else {
      __builtin_amdgcn_s_setprio(1);
      const int hv=wid-6, rg=lane&7;
      u32x4 sk[4];
      WAITV(6);
      BAR();
      WAITV(0);
      LOAD_NONCE();
#pragma unroll
      for(int i=0;i<4;i++) sk[i]=*(const u32x4*)(Ask+(xcd*128+(hv*16+(i>>1)*8+(lane>>3))*4)*4+(i&1)*8);
      BAR(); BAR(); BAR();
      SBAR();
      asm volatile("":"+v"(sk[0]),"+v"(sk[1]),"+v"(sk[2]),"+v"(sk[3]));
#pragma unroll 1
      for(int g=0;g<2;g++){
        const int c4=hv*16+g*8+(lane>>3);
        u32x2 kk[11], vv[8];
        lptr kb=(lptr)lds+L_CV+(8*rg+4)*256+(c4&1)*8;
#pragma unroll
        for(int i=0;i<3;i++) kk[i]=*(const LAS u32x2*)(kb+(i-3)*256+(((c4>>1)^(((rg-1)&7)<<1))*16));
#pragma unroll
        for(int i=0;i<8;i++){ kk[3+i]=*(const LAS u32x2*)(kb+i*256+(((c4>>1)^(rg<<1))*16)); vv[i]=*(const LAS u32x2*)(kb+16384+i*256+(((c4>>1)^(rg<<1))*16)); }
#pragma unroll
        for(int c=0;c<4;c++){
          const unsigned w01=sk[c>>1][(c&1)*2], w23=sk[c>>1][(c&1)*2+1];
          const float w0=lo16(w01),w1=hi16(w01),w2=lo16(w23),w3=hi16(w23);
          float kf[11];
#pragma unroll
          for(int i=0;i<11;i++) kf[i]=(c&1)?hi16(kk[i][c>>1]):lo16(kk[i][c>>1]);
          float o[8];
#pragma unroll
          for(int i=0;i<8;i++){ float vf=(c&1)?hi16(vv[i][c>>1]):lo16(vv[i][c>>1]); o[i]=(kf[i]*w3+kf[i+1]*w2+kf[i+2]*w1+kf[i+3]*w0)*vf; }
          u32x4 r; r[0]=pk(o[0],o[1]); r[1]=pk(o[2],o[3]); r[2]=pk(o[4],o[5]); r[3]=pk(o[6],o[7]);
          __builtin_amdgcn_raw_buffer_store_b128(r,wsr,WS_XT+(unsigned)(xcd*128+c4*4+c)*4096u+(unsigned)(j*128+rg*16),0,SC1);
          SBAR();
          if(c!=1){ BAR(); SBAR(); }
        }
        sk[0]=sk[2]; sk[1]=sk[3];
      }
      WAITV(0);
      BAR();
      if(hv==0 && lane==0) __hip_atomic_store(flagp(ws,b,FK_X),nonce,RLX_AGENT);
      BAR(); BAR(); BAR(); BAR();
    }
    const int ch=wgi*4+v;
    const float Dc=lo16(AD[ch]);
    const bool xr=__builtin_amdgcn_readfirstlane(cnt[C_XF])!=0;
    dma4(lane*16,(const char*)Ah+(size_t)ch*4096,lds0+L_CV+v*CV_W+CV_H);
    if(xr) dma4_sc1(lane*16,ws+WS_XT+(size_t)ch*4096,lds0+L_CV+v*CV_W+CV_X);
    __builtin_amdgcn_s_setprio(2);
    if(!xr){ wait_flags<4>(flagp(ws,(lane&31)*8+xcd,FK_X),nonce); dma4_sc1(lane*16,ws+WS_XT+(size_t)ch*4096,lds0+L_CV+v*CV_W+CV_X); }
    WAITV(4);
    if(lane<32) *(LAS unsigned*)((lwptr)lds+L_CV+v*CV_W+lane*4)=0u;
    if(lane<40) *(LAS unsigned*)((lwptr)lds+L_CV+v*CV_W+CV_H+4096+lane*4)=0u;
    bool h1;
    {
      lwptr LW=(lwptr)lds+L_CV+v*CV_W;
      const int g=lane>>4,q=(lane&15)>>2,p=lane&3;
      lptr ha=(lptr)LW+CV_H+8*(4*p+8*g+q-12);
      lptr xa=(lptr)LW+CV_X+8*(p+8-8*g-q);
      PIN(ha); PIN(xa);
      bf16x8 A[16],B0[16],B1[16];
#pragma unroll
      for(int d=0;d<16;d++) A[d]=cat(ldstr(ha+256*d),ldstr(ha+256*d+32));
      WAITV(0);
      float zf=0.f; PIN(zf);
#define MM0(z,a,b) asm volatile("v_mfma_f32_16x16x32_bf16 %0, %1, %2, 0" : "=&v"(z) : "v"(a), "v"(b))
#define MMA(z,a,b) asm volatile("v_mfma_f32_16x16x32_bf16 %0, %1, %2, %0" : "+v"(z) : "v"(a), "v"(b))
#define LDB(P,e) B##P[e]=cat(ldstr(xa+256*(e)+128*(P)+32),ldstr(xa+256*(e)+128*(P)))
#define ROR2(d,s,z,e,t,y) asm volatile("v_add_f32_dpp %0, %2, %3 wave_ror:1 row_mask:0xf bank_mask:0xf\n\tv_add_f32_dpp %1, %4, %5 wave_ror:1 row_mask:0xf bank_mask:0xf" : "=&v"(d), "=&v"(e) : "v"(s), "v"(z), "v"(t), "v"(y))
#define SHR2(d,s,z,e,t,y) asm volatile("v_add_f32_dpp %0, %2, %3 wave_shr:1 row_mask:0xf bank_mask:0xf\n\tv_add_f32_dpp %1, %4, %5 wave_shr:1 row_mask:0xf bank_mask:0xf" : "+v"(d), "+v"(e) : "v"(s), "v"(z), "v"(t), "v"(y))
      const unsigned yo=WS_Y+(unsigned)ch*4096u+lane*16u;
#define STY(C) __builtin_amdgcn_raw_buffer_store_b128(yw[C],wsr,yo+(C)*1024,0,SC1)
#define NOP1() asm volatile("s_nop 0")
#define VNOP() __builtin_amdgcn_s_setprio(2)
      unsigned xa_,xb_;
      lptr xe=(lptr)LW+CV_X+2*lane; PIN(xe);
#define LDX(c) do{ xa_=*(const LAS u16*)(xe+256*(c)); xb_=*(const LAS u16*)(xe+256*(c)+128); }while(0)
#define SHX() do{ xa_<<=16; xb_<<=16; PIN(xa_); PIN(xb_); }while(0)
#define FMD(z,x) do{ z[0]=__builtin_fmaf(asf(x),Dc,z[0]); PIN(z); }while(0)
      const gu64* kf1=kvflag(ws,R,C,v>>1,1,lane);
      u64 f1v; unsigned c1v;
#define HOOK1() do{ f1v=__hip_atomic_load(kf1,RLX_AGENT); c1v=*(volatile LAS unsigned*)(cnt+C_KV1); }while(0)
#define HOOK2() do{ h1=allof(f1v==nonce) && __builtin_amdgcn_readfirstlane(c1v)>=2; MEMFENCE(); if(h1) kv_fetch(ws,lds0,R,C,v>>1,1,v&1,lane); }while(0)
      SBAR();
f32x4 z0,z1,z2,z3,z4,z5,z6,z7,z8,z9,z10,z11,z12,z13,z14,z15,z16,z17,z18,z19,z20,z21,z22,z23,z24,z25,z26,z27,z28,z29,z30,z31;
float t2_0,t1_0,u_0,t2_1,t1_1,u_1,t2_2,t1_2,u_2,t2_3,t1_3,u_3,t2_4,t1_4,u_4,t2_5,t1_5,u_5,t2_6,t1_6,u_6,t2_7,t1_7,u_7,t2_8,t1_8,u_8,t2_9,t1_9,u_9,t2_10,t1_10,u_10,t2_11,t1_11,u_11,t2_12,t1_12,u_12,t2_13,t1_13,u_13,t2_14,t1_14,u_14,t2_15,t1_15,u_15,t2_16,t1_16,u_16,t2_17,t1_17,u_17,t2_18,t1_18,u_18,t2_19,t1_19,u_19,t2_20,t1_20,u_20,t2_21,t1_21,u_21,t2_22,t1_22,u_22,t2_23,t1_23,u_23,t2_24,t1_24,u_24,t2_25,t1_25,u_25,t2_26,t1_26,u_26,t2_27,t1_27,u_27,t2_28,t1_28,u_28,t2_29,t1_29,u_29,t2_30,t1_30,u_30,t2_31,t1_31,u_31;
u32x4 yw[4];

LDB(0,0); LDB(1,0);
LDB(0,1); LDB(1,1);
LDB(0,2); LDB(1,2);
LDX(0);
SBAR();
MM0(z0,A[0],B0[0]); SBAR(); LDB(0,3); SBAR();
MM0(z1,A[0],B1[0]); SBAR(); LDB(1,3); SBAR();
MM0(z2,A[1],B0[0]); SBAR(); LDB(0,4); SBAR();
MM0(z3,A[1],B1[0]); SBAR(); LDB(1,4); SBAR();
MMA(z2,A[0],B0[1]); SBAR();  SBAR();
MMA(z3,A[0],B1[1]); SBAR();  SBAR();
MM0(z4,A[2],B0[0]); SBAR();  SBAR();
MM0(z5,A[2],B1[0]); SBAR();  SBAR();
MMA(z4,A[1],B0[1]); SBAR();  SBAR();
MMA(z5,A[1],B1[1]); SBAR();  SBAR();
MMA(z4,A[0],B0[2]); SBAR(); ROR2(t2_0,zf,z0[2],t2_1,z0[3],z1[2]); SBAR();
MMA(z5,A[0],B1[2]); SBAR(); LDB(0,5); SBAR();
MM0(z6,A[3],B0[0]); SBAR(); SHR2(t2_0,z0[3],z0[2],t2_1,z1[3],z1[2]); SBAR();
MM0(z7,A[3],B1[0]); SBAR(); LDB(1,5); SBAR();
MMA(z6,A[2],B0[1]); SBAR(); ROR2(t1_0,zf,z0[1],t1_1,t2_0,z1[1]); SBAR();
MMA(z7,A[2],B1[1]); SBAR(); SHX(); SBAR();
MMA(z6,A[1],B0[2]); SBAR(); SHR2(t1_0,t2_0,z0[1],t1_1,t2_1,z1[1]); SBAR();
MMA(z7,A[1],B1[2]); SBAR(); FMD(z0,xa_); FMD(z1,xb_); SBAR();
MMA(z6,A[0],B0[3]); SBAR(); ROR2(u_0,zf,z0[0],u_1,t1_0,z1[0]); SBAR();
MMA(z7,A[0],B1[3]); SBAR(); LDX(1); SBAR();
MM0(z8,A[4],B0[0]); SBAR(); SHR2(u_0,t1_0,z0[0],u_1,t1_1,z1[0]); SBAR();
MM0(z9,A[4],B1[0]); SBAR(); yw[0][0]=pk(u_0,u_1); PIN(yw[0]); SBAR();
MMA(z8,A[3],B0[1]); SBAR(); ROR2(t2_2,z1[3],z2[2],t2_3,z2[3],z3[2]); SBAR();
MMA(z9,A[3],B1[1]); SBAR(); LDB(0,6); SBAR();
MMA(z8,A[2],B0[2]); SBAR(); SHR2(t2_2,z2[3],z2[2],t2_3,z3[3],z3[2]); SBAR();
MMA(z9,A[2],B1[2]); SBAR(); LDB(1,6); SBAR();
MMA(z8,A[1],B0[3]); SBAR(); ROR2(t1_2,t2_1,z2[1],t1_3,t2_2,z3[1]); SBAR();
MMA(z9,A[1],B1[3]); SBAR(); SHX(); SBAR();
MMA(z8,A[0],B0[4]); SBAR(); SHR2(t1_2,t2_2,z2[1],t1_3,t2_3,z3[1]); SBAR();
MMA(z9,A[0],B1[4]); SBAR(); FMD(z2,xa_); FMD(z3,xb_); SBAR();
MM0(z10,A[5],B0[0]); SBAR(); ROR2(u_2,t1_1,z2[0],u_3,t1_2,z3[0]); SBAR();
MM0(z11,A[5],B1[0]); SBAR(); LDX(2); SBAR();
MMA(z10,A[4],B0[1]); SBAR(); SHR2(u_2,t1_2,z2[0],u_3,t1_3,z3[0]); SBAR();
MMA(z11,A[4],B1[1]); SBAR(); yw[0][1]=pk(u_2,u_3); PIN(yw[0]); SBAR();
MMA(z10,A[3],B0[2]); SBAR(); ROR2(t2_4,z3[3],z4[2],t2_5,z4[3],z5[2]); SBAR();
MMA(z11,A[3],B1[2]); SBAR(); LDB(0,7); SBAR();
MMA(z10,A[2],B0[3]); SBAR(); SHR2(t2_4,z4[3],z4[2],t2_5,z5[3],z5[2]); SBAR();
MMA(z11,A[2],B1[3]); SBAR(); LDB(1,7); SBAR();
MMA(z10,A[1],B0[4]); SBAR(); ROR2(t1_4,t2_3,z4[1],t1_5,t2_4,z5[1]); SBAR();
MMA(z11,A[1],B1[4]); SBAR(); SHX(); SBAR();
MMA(z10,A[0],B0[5]); SBAR(); SHR2(t1_4,t2_4,z4[1],t1_5,t2_5,z5[1]); SBAR();
MMA(z11,A[0],B1[5]); SBAR(); FMD(z4,xa_); FMD(z5,xb_); SBAR();
MM0(z12,A[6],B0[0]); SBAR(); ROR2(u_4,t1_3,z4[0],u_5,t1_4,z5[0]); SBAR();
MM0(z13,A[6],B1[0]); SBAR(); LDX(3); SBAR();
MMA(z12,A[5],B0[1]); SBAR(); SHR2(u_4,t1_4,z4[0],u_5,t1_5,z5[0]); SBAR();
MMA(z13,A[5],B1[1]); SBAR(); yw[0][2]=pk(u_4,u_5); PIN(yw[0]); SBAR();
MMA(z12,A[4],B0[2]); SBAR(); ROR2(t2_6,z5[3],z6[2],t2_7,z6[3],z7[2]); SBAR();
MMA(z13,A[4],B1[2]); SBAR(); LDB(0,8); SBAR();
MMA(z12,A[3],B0[3]); SBAR(); SHR2(t2_6,z6[3],z6[2],t2_7,z7[3],z7[2]); SBAR();
MMA(z13,A[3],B1[3]); SBAR(); LDB(1,8); SBAR();
MMA(z12,A[2],B0[4]); SBAR(); ROR2(t1_6,t2_5,z6[1],t1_7,t2_6,z7[1]); SBAR();
MMA(z13,A[2],B1[4]); SBAR(); SHX(); SBAR();
MMA(z12,A[1],B0[5]); SBAR(); SHR2(t1_6,t2_6,z6[1],t1_7,t2_7,z7[1]); SBAR();
MMA(z13,A[1],B1[5]); SBAR(); FMD(z6,xa_); FMD(z7,xb_); SBAR();
MMA(z12,A[0],B0[6]); SBAR(); ROR2(u_6,t1_5,z6[0],u_7,t1_6,z7[0]); SBAR();
MMA(z13,A[0],B1[6]); SBAR(); LDX(4); SBAR();
MM0(z14,A[7],B0[0]); SBAR(); SHR2(u_6,t1_6,z6[0],u_7,t1_7,z7[0]); SBAR();
MM0(z15,A[7],B1[0]); SBAR(); yw[0][3]=pk(u_6,u_7); PIN(yw[0]); STY(0); SBAR();
MMA(z14,A[6],B0[1]); SBAR(); ROR2(t2_8,z7[3],z8[2],t2_9,z8[3],z9[2]); SBAR();
MMA(z15,A[6],B1[1]); SBAR(); LDB(0,9); SBAR();
MMA(z14,A[5],B0[2]); SBAR(); SHR2(t2_8,z8[3],z8[2],t2_9,z9[3],z9[2]); SBAR();
MMA(z15,A[5],B1[2]); SBAR(); LDB(1,9); SBAR();
MMA(z14,A[4],B0[3]); SBAR(); ROR2(t1_8,t2_7,z8[1],t1_9,t2_8,z9[1]); SBAR();
MMA(z15,A[4],B1[3]); SBAR(); SHX(); SBAR();
MMA(z14,A[3],B0[4]); SBAR(); SHR2(t1_8,t2_8,z8[1],t1_9,t2_9,z9[1]); SBAR();
MMA(z15,A[3],B1[4]); SBAR(); FMD(z8,xa_); FMD(z9,xb_); SBAR();
MMA(z14,A[2],B0[5]); SBAR(); ROR2(u_8,t1_7,z8[0],u_9,t1_8,z9[0]); SBAR();
MMA(z15,A[2],B1[5]); SBAR(); LDX(5); SBAR();
MMA(z14,A[1],B0[6]); SBAR(); SHR2(u_8,t1_8,z8[0],u_9,t1_9,z9[0]); SBAR();
MMA(z15,A[1],B1[6]); SBAR(); yw[1][0]=pk(u_8,u_9); PIN(yw[1]); SBAR();
MMA(z14,A[0],B0[7]); SBAR(); ROR2(t2_10,z9[3],z10[2],t2_11,z10[3],z11[2]); SBAR();
MMA(z15,A[0],B1[7]); SBAR(); LDB(0,10); SBAR();
MM0(z16,A[8],B0[0]); SBAR(); SHR2(t2_10,z10[3],z10[2],t2_11,z11[3],z11[2]); SBAR();
MM0(z17,A[8],B1[0]); SBAR(); LDB(1,10); SBAR();
MMA(z16,A[7],B0[1]); SBAR(); ROR2(t1_10,t2_9,z10[1],t1_11,t2_10,z11[1]); SBAR();
MMA(z17,A[7],B1[1]); SBAR(); SHX(); SBAR();
MMA(z16,A[6],B0[2]); SBAR(); SHR2(t1_10,t2_10,z10[1],t1_11,t2_11,z11[1]); SBAR();
MMA(z17,A[6],B1[2]); SBAR(); FMD(z10,xa_); FMD(z11,xb_); SBAR();
MMA(z16,A[5],B0[3]); SBAR(); ROR2(u_10,t1_9,z10[0],u_11,t1_10,z11[0]); SBAR();
MMA(z17,A[5],B1[3]); SBAR(); LDX(6); SBAR();
MMA(z16,A[4],B0[4]); SBAR(); SHR2(u_10,t1_10,z10[0],u_11,t1_11,z11[0]); SBAR();
MMA(z17,A[4],B1[4]); SBAR(); yw[1][1]=pk(u_10,u_11); PIN(yw[1]); SBAR();
MMA(z16,A[3],B0[5]); SBAR(); ROR2(t2_12,z11[3],z12[2],t2_13,z12[3],z13[2]); SBAR();
MMA(z17,A[3],B1[5]); SBAR(); LDB(0,11); SBAR();
MMA(z16,A[2],B0[6]); SBAR(); SHR2(t2_12,z12[3],z12[2],t2_13,z13[3],z13[2]); SBAR();
MMA(z17,A[2],B1[6]); SBAR(); LDB(1,11); SBAR();
MMA(z16,A[1],B0[7]); SBAR(); ROR2(t1_12,t2_11,z12[1],t1_13,t2_12,z13[1]); SBAR();
MMA(z17,A[1],B1[7]); SBAR(); SHX(); SBAR();
MMA(z16,A[0],B0[8]); SBAR(); SHR2(t1_12,t2_12,z12[1],t1_13,t2_13,z13[1]); SBAR();
MMA(z17,A[0],B1[8]); SBAR(); FMD(z12,xa_); FMD(z13,xb_); SBAR();
MM0(z18,A[9],B0[0]); SBAR(); ROR2(u_12,t1_11,z12[0],u_13,t1_12,z13[0]); SBAR();
MM0(z19,A[9],B1[0]); SBAR(); LDX(7); SBAR();
MMA(z18,A[8],B0[1]); SBAR(); SHR2(u_12,t1_12,z12[0],u_13,t1_13,z13[0]); SBAR();
MMA(z19,A[8],B1[1]); SBAR(); yw[1][2]=pk(u_12,u_13); PIN(yw[1]); SBAR();
MMA(z18,A[7],B0[2]); SBAR(); ROR2(t2_14,z13[3],z14[2],t2_15,z14[3],z15[2]); SBAR();
MMA(z19,A[7],B1[2]); SBAR(); LDB(0,12); SBAR();
MMA(z18,A[6],B0[3]); SBAR(); SHR2(t2_14,z14[3],z14[2],t2_15,z15[3],z15[2]); SBAR();
MMA(z19,A[6],B1[3]); SBAR(); LDB(1,12); SBAR();
MMA(z18,A[5],B0[4]); SBAR(); ROR2(t1_14,t2_13,z14[1],t1_15,t2_14,z15[1]); SBAR();
MMA(z19,A[5],B1[4]); SBAR(); SHX(); SBAR();
MMA(z18,A[4],B0[5]); SBAR(); SHR2(t1_14,t2_14,z14[1],t1_15,t2_15,z15[1]); SBAR();
MMA(z19,A[4],B1[5]); SBAR(); FMD(z14,xa_); FMD(z15,xb_); SBAR();
MMA(z18,A[3],B0[6]); SBAR(); ROR2(u_14,t1_13,z14[0],u_15,t1_14,z15[0]); SBAR();
MMA(z19,A[3],B1[6]); SBAR(); LDX(8); SBAR();
MMA(z18,A[2],B0[7]); SBAR(); SHR2(u_14,t1_14,z14[0],u_15,t1_15,z15[0]); SBAR();
MMA(z19,A[2],B1[7]); SBAR(); yw[1][3]=pk(u_14,u_15); PIN(yw[1]); STY(1); SBAR();
MMA(z18,A[1],B0[8]); SBAR(); ROR2(t2_16,z15[3],z16[2],t2_17,z16[3],z17[2]); SBAR();
MMA(z19,A[1],B1[8]); SBAR(); LDB(0,13); SBAR();
MMA(z18,A[0],B0[9]); SBAR(); SHR2(t2_16,z16[3],z16[2],t2_17,z17[3],z17[2]); SBAR();
MMA(z19,A[0],B1[9]); SBAR(); LDB(1,13); SBAR();
MM0(z20,A[10],B0[0]); SBAR(); ROR2(t1_16,t2_15,z16[1],t1_17,t2_16,z17[1]); SBAR();
MM0(z21,A[10],B1[0]); SBAR(); SHX(); SBAR();
MMA(z20,A[9],B0[1]); SBAR(); SHR2(t1_16,t2_16,z16[1],t1_17,t2_17,z17[1]); SBAR();
MMA(z21,A[9],B1[1]); SBAR(); FMD(z16,xa_); FMD(z17,xb_); SBAR();
MMA(z20,A[8],B0[2]); SBAR(); ROR2(u_16,t1_15,z16[0],u_17,t1_16,z17[0]); SBAR();
MMA(z21,A[8],B1[2]); SBAR(); LDX(9); SBAR();
MMA(z20,A[7],B0[3]); SBAR(); SHR2(u_16,t1_16,z16[0],u_17,t1_17,z17[0]); SBAR();
MMA(z21,A[7],B1[3]); SBAR(); yw[2][0]=pk(u_16,u_17); PIN(yw[2]); SBAR();
MMA(z20,A[6],B0[4]); SBAR(); ROR2(t2_18,z17[3],z18[2],t2_19,z18[3],z19[2]); SBAR();
MMA(z21,A[6],B1[4]); SBAR(); LDB(0,14); SBAR();
MMA(z20,A[5],B0[5]); SBAR(); SHR2(t2_18,z18[3],z18[2],t2_19,z19[3],z19[2]); SBAR();
MMA(z21,A[5],B1[5]); SBAR(); LDB(1,14); SBAR();
MMA(z20,A[4],B0[6]); SBAR(); ROR2(t1_18,t2_17,z18[1],t1_19,t2_18,z19[1]); SBAR();
MMA(z21,A[4],B1[6]); SBAR(); SHX(); SBAR();
MMA(z20,A[3],B0[7]); SBAR(); SHR2(t1_18,t2_18,z18[1],t1_19,t2_19,z19[1]); SBAR();
MMA(z21,A[3],B1[7]); SBAR(); FMD(z18,xa_); FMD(z19,xb_); SBAR();
MMA(z20,A[2],B0[8]); SBAR(); ROR2(u_18,t1_17,z18[0],u_19,t1_18,z19[0]); SBAR();
MMA(z21,A[2],B1[8]); SBAR(); LDX(10); SBAR();
MMA(z20,A[1],B0[9]); SBAR(); SHR2(u_18,t1_18,z18[0],u_19,t1_19,z19[0]); SBAR();
MMA(z21,A[1],B1[9]); SBAR(); yw[2][1]=pk(u_18,u_19); PIN(yw[2]); SBAR();
MMA(z20,A[0],B0[10]); SBAR();  SBAR();
MMA(z21,A[0],B1[10]); SBAR();  SBAR();
MM0(z22,A[11],B0[0]); SBAR();  SBAR();
MM0(z23,A[11],B1[0]); SBAR();  SBAR();
MMA(z22,A[10],B0[1]); SBAR();  SBAR();
MMA(z23,A[10],B1[1]); SBAR();  SBAR();
MMA(z22,A[9],B0[2]); SBAR();  SBAR();
MMA(z23,A[9],B1[2]); SBAR();  SBAR();
MMA(z22,A[8],B0[3]); SBAR();  SBAR();
MMA(z23,A[8],B1[3]); SBAR();  SBAR();
MMA(z22,A[7],B0[4]); SBAR(); ROR2(t2_20,z19[3],z20[2],t2_21,z20[3],z21[2]); SBAR();
MMA(z23,A[7],B1[4]); SBAR(); LDB(0,15); SBAR();
MMA(z22,A[6],B0[5]); SBAR(); SHR2(t2_20,z20[3],z20[2],t2_21,z21[3],z21[2]); SBAR();
MMA(z23,A[6],B1[5]); SBAR(); LDB(1,15); SBAR();
MMA(z22,A[5],B0[6]); SBAR(); ROR2(t1_20,t2_19,z20[1],t1_21,t2_20,z21[1]); SBAR();
MMA(z23,A[5],B1[6]); SBAR(); SHX(); SBAR();
MMA(z22,A[4],B0[7]); SBAR(); SHR2(t1_20,t2_20,z20[1],t1_21,t2_21,z21[1]); SBAR();
MMA(z23,A[4],B1[7]); SBAR(); FMD(z20,xa_); FMD(z21,xb_); SBAR();
MMA(z22,A[3],B0[8]); SBAR(); ROR2(u_20,t1_19,z20[0],u_21,t1_20,z21[0]); SBAR();
MMA(z23,A[3],B1[8]); SBAR(); LDX(11); SBAR();
MMA(z22,A[2],B0[9]); SBAR(); SHR2(u_20,t1_20,z20[0],u_21,t1_21,z21[0]); SBAR();
MMA(z23,A[2],B1[9]); SBAR(); yw[2][2]=pk(u_20,u_21); PIN(yw[2]); SBAR();
MMA(z22,A[1],B0[10]); SBAR();  SBAR();
MMA(z23,A[1],B1[10]); SBAR();  SBAR();
MMA(z22,A[0],B0[11]); SBAR();  SBAR();
MMA(z23,A[0],B1[11]); SBAR();  SBAR();
MM0(z24,A[12],B0[0]); SBAR();  SBAR();
MM0(z25,A[12],B1[0]); SBAR();  SBAR();
MMA(z24,A[11],B0[1]); SBAR();  SBAR();
MMA(z25,A[11],B1[1]); SBAR();  SBAR();
MMA(z24,A[10],B0[2]); SBAR();  SBAR();
MMA(z25,A[10],B1[2]); SBAR();  SBAR();
MMA(z24,A[9],B0[3]); SBAR();  SBAR();
MMA(z25,A[9],B1[3]); SBAR();  SBAR();
MMA(z24,A[8],B0[4]); SBAR(); ROR2(t2_22,z21[3],z22[2],t2_23,z22[3],z23[2]); SBAR();
MMA(z25,A[8],B1[4]); SBAR(); VNOP(); SBAR();
MMA(z24,A[7],B0[5]); SBAR(); SHR2(t2_22,z22[3],z22[2],t2_23,z23[3],z23[2]); SBAR();
MMA(z25,A[7],B1[5]); SBAR(); VNOP(); SBAR();
MMA(z24,A[6],B0[6]); SBAR(); ROR2(t1_22,t2_21,z22[1],t1_23,t2_22,z23[1]); SBAR();
MMA(z25,A[6],B1[6]); SBAR(); SHX(); SBAR();
MMA(z24,A[5],B0[7]); SBAR(); SHR2(t1_22,t2_22,z22[1],t1_23,t2_23,z23[1]); SBAR();
MMA(z25,A[5],B1[7]); SBAR(); FMD(z22,xa_); FMD(z23,xb_); SBAR();
MMA(z24,A[4],B0[8]); SBAR(); ROR2(u_22,t1_21,z22[0],u_23,t1_22,z23[0]); SBAR();
MMA(z25,A[4],B1[8]); SBAR(); LDX(12); SBAR();
MMA(z24,A[3],B0[9]); SBAR(); SHR2(u_22,t1_22,z22[0],u_23,t1_23,z23[0]); SBAR();
MMA(z25,A[3],B1[9]); SBAR(); yw[2][3]=pk(u_22,u_23); PIN(yw[2]); STY(2); SBAR();
MMA(z24,A[2],B0[10]); SBAR();  SBAR();
MMA(z25,A[2],B1[10]); SBAR();  SBAR();
MMA(z24,A[1],B0[11]); SBAR();  SBAR();
MMA(z25,A[1],B1[11]); SBAR();  SBAR();
MMA(z24,A[0],B0[12]); SBAR();  SBAR();
MMA(z25,A[0],B1[12]); SBAR();  SBAR();
MM0(z26,A[13],B0[0]); SBAR();  SBAR();
MM0(z27,A[13],B1[0]); SBAR();  SBAR();
MMA(z26,A[12],B0[1]); SBAR();  SBAR();
MMA(z27,A[12],B1[1]); SBAR();  SBAR();
MMA(z26,A[11],B0[2]); SBAR();  SBAR();
MMA(z27,A[11],B1[2]); SBAR();  SBAR();
MMA(z26,A[10],B0[3]); SBAR();  SBAR();
MMA(z27,A[10],B1[3]); SBAR();  SBAR();
MMA(z26,A[9],B0[4]); SBAR(); ROR2(t2_24,z23[3],z24[2],t2_25,z24[3],z25[2]); SBAR();
MMA(z27,A[9],B1[4]); SBAR(); VNOP(); SBAR();
MMA(z26,A[8],B0[5]); SBAR(); SHR2(t2_24,z24[3],z24[2],t2_25,z25[3],z25[2]); SBAR();
MMA(z27,A[8],B1[5]); SBAR(); VNOP(); SBAR();
MMA(z26,A[7],B0[6]); SBAR(); ROR2(t1_24,t2_23,z24[1],t1_25,t2_24,z25[1]); SBAR();
MMA(z27,A[7],B1[6]); SBAR(); SHX(); SBAR();
MMA(z26,A[6],B0[7]); SBAR(); SHR2(t1_24,t2_24,z24[1],t1_25,t2_25,z25[1]); SBAR();
MMA(z27,A[6],B1[7]); SBAR(); FMD(z24,xa_); FMD(z25,xb_); SBAR();
MMA(z26,A[5],B0[8]); SBAR(); ROR2(u_24,t1_23,z24[0],u_25,t1_24,z25[0]); SBAR();
MMA(z27,A[5],B1[8]); SBAR(); LDX(13); SBAR();
MMA(z26,A[4],B0[9]); SBAR(); SHR2(u_24,t1_24,z24[0],u_25,t1_25,z25[0]); SBAR();
MMA(z27,A[4],B1[9]); SBAR(); yw[3][0]=pk(u_24,u_25); PIN(yw[3]); SBAR();
MMA(z26,A[3],B0[10]); SBAR();  SBAR();
MMA(z27,A[3],B1[10]); SBAR();  SBAR();
MMA(z26,A[2],B0[11]); SBAR();  SBAR();
MMA(z27,A[2],B1[11]); SBAR();  SBAR();
MMA(z26,A[1],B0[12]); SBAR();  SBAR();
MMA(z27,A[1],B1[12]); SBAR();  SBAR();
MMA(z26,A[0],B0[13]); SBAR();  SBAR();
MMA(z27,A[0],B1[13]); SBAR();  SBAR();
MM0(z28,A[14],B0[0]); SBAR();  SBAR();
MM0(z29,A[14],B1[0]); SBAR();  SBAR();
MMA(z28,A[13],B0[1]); SBAR();  SBAR();
MMA(z29,A[13],B1[1]); SBAR();  SBAR();
MMA(z28,A[12],B0[2]); SBAR();  SBAR();
MMA(z29,A[12],B1[2]); SBAR();  SBAR();
MMA(z28,A[11],B0[3]); SBAR();  SBAR();
MMA(z29,A[11],B1[3]); SBAR();  SBAR();
MMA(z28,A[10],B0[4]); SBAR(); ROR2(t2_26,z25[3],z26[2],t2_27,z26[3],z27[2]); SBAR();
MMA(z29,A[10],B1[4]); SBAR(); VNOP(); SBAR();
MMA(z28,A[9],B0[5]); SBAR(); SHR2(t2_26,z26[3],z26[2],t2_27,z27[3],z27[2]); SBAR();
MMA(z29,A[9],B1[5]); SBAR(); VNOP(); SBAR();
MMA(z28,A[8],B0[6]); SBAR(); ROR2(t1_26,t2_25,z26[1],t1_27,t2_26,z27[1]); SBAR();
MMA(z29,A[8],B1[6]); SBAR(); SHX(); SBAR();
MMA(z28,A[7],B0[7]); SBAR(); SHR2(t1_26,t2_26,z26[1],t1_27,t2_27,z27[1]); SBAR();
MMA(z29,A[7],B1[7]); SBAR(); FMD(z26,xa_); FMD(z27,xb_); SBAR();
MMA(z28,A[6],B0[8]); SBAR(); ROR2(u_26,t1_25,z26[0],u_27,t1_26,z27[0]); SBAR();
MMA(z29,A[6],B1[8]); SBAR(); LDX(14); SBAR();
MMA(z28,A[5],B0[9]); SBAR(); SHR2(u_26,t1_26,z26[0],u_27,t1_27,z27[0]); SBAR();
MMA(z29,A[5],B1[9]); SBAR(); yw[3][1]=pk(u_26,u_27); PIN(yw[3]); SBAR();
MMA(z28,A[4],B0[10]); SBAR();  SBAR();
MMA(z29,A[4],B1[10]); SBAR();  SBAR();
MMA(z28,A[3],B0[11]); SBAR();  SBAR();
MMA(z29,A[3],B1[11]); SBAR();  SBAR();
MMA(z28,A[2],B0[12]); SBAR();  SBAR();
MMA(z29,A[2],B1[12]); SBAR();  SBAR();
MMA(z28,A[1],B0[13]); SBAR();  SBAR();
MMA(z29,A[1],B1[13]); SBAR();  SBAR();
MMA(z28,A[0],B0[14]); SBAR();  SBAR();
MMA(z29,A[0],B1[14]); SBAR();  SBAR();
MM0(z30,A[15],B0[0]); SBAR();  SBAR();
MM0(z31,A[15],B1[0]); SBAR();  SBAR();
MMA(z30,A[14],B0[1]); SBAR();  SBAR();
MMA(z31,A[14],B1[1]); SBAR();  SBAR();
MMA(z30,A[13],B0[2]); SBAR();  SBAR();
MMA(z31,A[13],B1[2]); SBAR();  SBAR();
MMA(z30,A[12],B0[3]); SBAR();  SBAR();
MMA(z31,A[12],B1[3]); SBAR();  SBAR();
MMA(z30,A[11],B0[4]); SBAR(); ROR2(t2_28,z27[3],z28[2],t2_29,z28[3],z29[2]); SBAR();
MMA(z31,A[11],B1[4]); SBAR(); VNOP(); SBAR();
MMA(z30,A[10],B0[5]); SBAR(); SHR2(t2_28,z28[3],z28[2],t2_29,z29[3],z29[2]); SBAR();
HOOK1(); SBAR();
MMA(z31,A[10],B1[5]); SBAR(); VNOP(); SBAR();
MMA(z30,A[9],B0[6]); SBAR(); ROR2(t1_28,t2_27,z28[1],t1_29,t2_28,z29[1]); SBAR();
MMA(z31,A[9],B1[6]); SBAR(); SHX(); SBAR();
MMA(z30,A[8],B0[7]); SBAR(); SHR2(t1_28,t2_28,z28[1],t1_29,t2_29,z29[1]); SBAR();
MMA(z31,A[8],B1[7]); SBAR(); FMD(z28,xa_); FMD(z29,xb_); SBAR();
MMA(z30,A[7],B0[8]); SBAR(); ROR2(u_28,t1_27,z28[0],u_29,t1_28,z29[0]); SBAR();
MMA(z31,A[7],B1[8]); SBAR(); LDX(15); SBAR();
MMA(z30,A[6],B0[9]); SBAR(); SHR2(u_28,t1_28,z28[0],u_29,t1_29,z29[0]); SBAR();
MMA(z31,A[6],B1[9]); SBAR(); yw[3][2]=pk(u_28,u_29); PIN(yw[3]); SBAR();
MMA(z30,A[5],B0[10]); SBAR();  SBAR();
MMA(z31,A[5],B1[10]); SBAR();  SBAR();
MMA(z30,A[4],B0[11]); SBAR();  SBAR();
MMA(z31,A[4],B1[11]); SBAR();  SBAR();
MMA(z30,A[3],B0[12]); SBAR();  SBAR();
MMA(z31,A[3],B1[12]); SBAR();  SBAR();
MMA(z30,A[2],B0[13]); SBAR();  SBAR();
MMA(z31,A[2],B1[13]); SBAR();  SBAR();
MMA(z30,A[1],B0[14]); SBAR();  SBAR();
MMA(z31,A[1],B1[14]); SBAR();  SBAR();
MMA(z30,A[0],B0[15]); SBAR();  SBAR();
MMA(z31,A[0],B1[15]); SBAR();  SBAR();
NOP1(); SBAR();  SBAR();
NOP1(); SBAR();  SBAR();
NOP1(); SBAR();  SBAR();
NOP1(); SBAR();  SBAR();
NOP1(); SBAR();  SBAR();
NOP1(); SBAR();  SBAR();
NOP1(); SBAR();  SBAR();
NOP1(); SBAR();  SBAR();
NOP1(); SBAR(); ROR2(t2_30,z29[3],z30[2],t2_31,z30[3],z31[2]); SBAR();
NOP1(); SBAR(); VNOP(); SBAR();
NOP1(); SBAR(); SHR2(t2_30,z30[3],z30[2],t2_31,z31[3],z31[2]); SBAR();
NOP1(); SBAR(); VNOP(); SBAR();
NOP1(); SBAR(); ROR2(t1_30,t2_29,z30[1],t1_31,t2_30,z31[1]); SBAR();
NOP1(); SBAR(); SHX(); SBAR();
NOP1(); SBAR(); SHR2(t1_30,t2_30,z30[1],t1_31,t2_31,z31[1]); SBAR();
NOP1(); SBAR(); FMD(z30,xa_); FMD(z31,xb_); SBAR();
NOP1(); SBAR(); ROR2(u_30,t1_29,z30[0],u_31,t1_30,z31[0]); SBAR();
NOP1(); SBAR(); VNOP(); SBAR();
NOP1(); SBAR(); SHR2(u_30,t1_30,z30[0],u_31,t1_31,z31[0]); SBAR();
NOP1(); SBAR(); yw[3][3]=pk(u_30,u_31); PIN(yw[3]); STY(3); SBAR();
      if(v==3 && lane<32) touch((unsigned)((j*32+lane)*2048+((xcd&1)*8+((3-(int)((uintptr_t)WO>>7))&7))*128),WO,junk);
      HOOK2();
    }
    if(!h1){
      wait_flags<4>(kvflag(ws,R,C,v>>1,1,lane),nonce);
      lds_wait(cnt+C_KV1,2);
      kv_fetch(ws,lds0,R,C,v>>1,1,v&1,lane);
    }
    WAITV(0);
    arrive_publish(cnt+C_H1,4,flagp(ws,b,FK_Y),nonce,lane);
    lds_wait(cnt+C_H1,4); lds_wait(cnt+C_P1E,4);
#pragma unroll 1
    for(int i=v;i<WO_P1;i+=4) wo_block(i);
    if(v<2) touch((unsigned)((512*(j>>3)+8*(j&7)+64*(lane>>3)+(lane&7))*2048+(xcd*2+v)*128),Aq,junk);
    __builtin_amdgcn_s_setprio(0);
  } else {
    const int wr=wid>>1, wc=wid&1;
    const int r=lane&31, h=lane>>5;
    lptr L=(lptr)lds;
    lptr xa[4];
    { int row=wr*64+r; int sw=(row>>1)&7;
#pragma unroll
      for(int ks=0;ks<4;ks++) xa[ks]=L+row*128+(((2*ks+h)^sw)*16); }
    lptr wa;
    { int blk=(lane>>4)&1,q=(lane&15)>>2,p=lane&3; int c=(wc*4+2*blk+(p>>1))^(((q>>1)&1)<<2); wa=L+16384+(8*h+q)*128+c*16+8*(p&1); }
    f32x16 aq[2],ak[2],av[2];
#pragma unroll
    for(int i=0;i<2;i++){ aq[i]=f32x16{}; ak[i]=f32x16{}; av[i]=f32x16{}; }
    G1F F0,F1;
#define G1_RX(F,ks) do{ F.x0=ldsr128(xa[ks]+so); F.x1=ldsr128(xa[ks]+so+4096); }while(0)
#define G1_RW(F,ks,sg) do{ F.w[2*(sg)]=ldstr(wa+so+(sg)*8192+(16*(ks))*128); F.w[2*(sg)+1]=ldstr(wa+so+(sg)*8192+(16*(ks)+4)*128); }while(0)
#define G1_W(F,sg) cat(F.w[2*(sg)],F.w[2*(sg)+1])
#define G1_STEP(F,N,ks,LD) do{ \
    ak[0]=MFMA32(G1_W(F,1),F.x0,ak[0]); PIN(ak[0]); if(LD) G1_RX(N,ks); SBAR(); \
    ak[1]=MFMA32(G1_W(F,1),F.x1,ak[1]); PIN(ak[1]); if(LD) G1_RW(N,ks,1); SBAR(); \
    av[0]=MFMA32(F.x0,G1_W(F,2),av[0]); PIN(av[0]); if(LD) G1_RW(N,ks,2); SBAR(); \
    av[1]=MFMA32(F.x1,G1_W(F,2),av[1]); PIN(av[1]); if(LD) G1_RW(N,ks,0); SBAR(); \
    aq[0]=MFMA32(G1_W(F,0),F.x0,aq[0]); PIN(aq[0]); SBAR(); \
    aq[1]=MFMA32(G1_W(F,0),F.x1,aq[1]); PIN(aq[1]); SBAR(); }while(0)
    __builtin_amdgcn_s_setprio(3);
    asm volatile("":"+v"(aq[0]),"+v"(aq[1]),"+v"(ak[0]),"+v"(ak[1]),"+v"(av[0]),"+v"(av[1]));
    WAITV(6);
    BAR();
    int so=0;
    G1_RX(F0,0); G1_RW(F0,0,1); G1_RW(F0,0,2); G1_RW(F0,0,0); SBAR();
#pragma unroll 1
    for(int t=0;t<14;t++){
      G1_STEP(F0,F1,1,true);
      G1_STEP(F1,F0,2,true);
      G1_STEP(F0,F1,3,true);
      so=(so==2*G1_SLOT)?0:so+G1_SLOT;
      if(t==0) WAITV(0);
      WAITL(); BAR(); SBAR();
      G1_STEP(F1,F0,0,true);
    }
    G1F E[8]; E[0]=F0;
#define G1_KV(n,LD) do{ \
    ak[0]=MFMA32(G1_W(E[n],1),E[n].x0,ak[0]); PIN(ak[0]); if(LD) G1_RX(E[(n)+1],((n)+1)&3); SBAR(); \
    ak[1]=MFMA32(G1_W(E[n],1),E[n].x1,ak[1]); PIN(ak[1]); if(LD) G1_RW(E[(n)+1],((n)+1)&3,1); SBAR(); \
    av[0]=MFMA32(E[n].x0,G1_W(E[n],2),av[0]); PIN(av[0]); if(LD) G1_RW(E[(n)+1],((n)+1)&3,2); SBAR(); \
    av[1]=MFMA32(E[n].x1,G1_W(E[n],2),av[1]); PIN(av[1]); if(LD) G1_RW(E[(n)+1],((n)+1)&3,0); SBAR(); }while(0)
#define G1_Q(n) do{ aq[0]=MFMA32(G1_W(E[n],0),E[n].x0,aq[0]); PIN(aq[0]); aq[1]=MFMA32(G1_W(E[n],0),E[n].x1,aq[1]); PIN(aq[1]); SBAR(); }while(0)
    G1_KV(0,true); G1_KV(1,true); G1_KV(2,true);
    LOAD_NONCE();
    so=0;
    lds_wait(cnt+C_S15,1); SBAR();
    G1_KV(3,true); G1_KV(4,true); G1_KV(5,true); G1_KV(6,true);
    arrive(cnt+C_RD);
    SBAR();
    { const int n=7; ak[0]=MFMA32(G1_W(E[n],1),E[n].x0,ak[0]); ak[1]=MFMA32(G1_W(E[n],1),E[n].x1,ak[1]); av[0]=MFMA32(E[n].x0,G1_W(E[n],2),av[0]); av[1]=MFMA32(E[n].x1,G1_W(E[n],2),av[1]);
      PIN(ak[0]); PIN(ak[1]); PIN(av[0]); PIN(av[1]); SBAR(); }
    const float qs=0.17677669529663687f*1.4426950408889634f;
    u32x4 kf[2][2],vf[2][2];
    const unsigned go=WS_KV+(unsigned)b*32768u+wc*16384u+(2*wr)*2048u+lane*16u;
#define KV_STORE() _Pragma("unroll") for(int rt=0;rt<2;rt++) _Pragma("unroll") for(int s=0;s<2;s++) __builtin_amdgcn_raw_buffer_store_b128(kf[rt][s],wsr,go+rt*2048+s*1024,0,SC1); \
      _Pragma("unroll") for(int rt=0;rt<2;rt++) _Pragma("unroll") for(int s=0;s<2;s++) __builtin_amdgcn_raw_buffer_store_b128(vf[rt][s],wsr,go+8192+rt*2048+s*1024,0,SC1)
    G1_Q(0);
    kf[0][0]=pack8(ak[0],0); kf[0][1]=pack8(ak[0],1); PIN(kf[0][0]); PIN(kf[0][1]);
    kf[1][0]=pack8(ak[1],0); kf[1][1]=pack8(ak[1],1); PIN(kf[1][0]); PIN(kf[1][1]); SBAR(); G1_Q(1);
    vf[0][0]=pack8(av[0],0); vf[0][1]=pack8(av[0],1); PIN(vf[0][0]); PIN(vf[0][1]);
    vf[1][0]=pack8(av[1],0); vf[1][1]=pack8(av[1],1); PIN(vf[1][0]); PIN(vf[1][1]); SBAR();
    if(wc==0){ KV_STORE(); }
    SBAR();
    G1_Q(2); G1_Q(3); G1_Q(4); G1_Q(5); G1_Q(6); G1_Q(7);
    lds_wait(cnt+C_RD,4);
#pragma unroll
    for(int rt=0;rt<2;rt++){
      lwptr lq=(lwptr)lds+(1-wc)*HR+49152+(2*wr+rt)*2048+lane*16;
#pragma unroll
      for(int s=0;s<2;s++) *(LAS u32x4*)(lq+s*1024)=pack8(aq[rt],s,qs);
    }
    if(R<2){
#pragma unroll 1
      for(int i=0;i<8*(2-R);i++) *(LAS u32x4*)((lwptr)lds+(1-wc)*HR+wr*24576+i*1024+lane*16)=u32x4{0,0,0,0};
    }
    if(wc==1){ lds_wait(cnt+C_KV0,2); KV_STORE(); }
    WAITV(0);
    arrive_publish(cnt+C_KV0+wc,2,flagp(ws,b,FK_KV0+wc),nonce,lane);
    if(wid==0){
      lds_wait(cnt+C_KV0,2);
      const int kind=(lane>>2)&1, ri=lane&3;
      const gu64* fp=kvflag(ws,R,C,kind,0,ri);
      unsigned todo=(R>=2)?0x77u:((R==1)?0x66u:0x44u);
#pragma unroll 1
      for(unsigned sp=0;sp<SPIN_CAP && todo;sp++){
        u64 fv=0;
        if(lane<8 && ((todo>>lane)&1)) fv=__hip_atomic_load(fp,RLX_AGENT);
        unsigned rdy=(unsigned)__builtin_amdgcn_ballot_w64(fv==nonce)&todo;
        MEMFENCE();
        todo&=~rdy;
        if(!rdy) __builtin_amdgcn_s_sleep(2);
#pragma unroll 1
        while(rdy){
          const int bit=__builtin_ctz(rdy); rdy&=rdy-1;
          const int kd=bit>>2, i=bit&3;
          const char* src=ws+WS_KV+(size_t)blk_of(R-2+i,(C&12)|((C-1-kd)&3))*32768+kd*8192;
          const unsigned dst=lds0+HR+(1-kd)*24576+i*8192;
          dma4_sc1(lane*16,src,dst); dma4_sc1(lane*16,src+4096,dst+4096);
        }
      }
      WAITV(0);
      arrive(cnt+C_H0K);
    } else lds_wait(cnt+C_H0K,1);
    __builtin_amdgcn_s_setprio(1);
  }
  const int late=wid>>2;
  if(R>=2) attention<false>(lds,cnt,ws,wsr,b,R,C,wid,lane,nonce);
  else     attention<true >(lds,cnt,ws,wsr,b,R,C,wid,lane,nonce);
  if(!late){
    WAITV(0);
    arrive_publish(cnt+C_O0,4,flagp(ws,b,FK_O0),nonce,lane);
    if(wid<3){ lds_wait(cnt+C_E,4); lds_wait(cnt+C_P1L,4);
#pragma unroll 1
      for(int i=WO_P1+wid;i<32;i+=3) wo_block(i);
    }
    const int T2=R2*4+wid;
    const unsigned ob=WS_O+(unsigned)T2*65536u+lane*16u;
    u32x4 of[G2D][2];
#define G2_LDO(i,n) do{ const unsigned so_=(unsigned)((((n)+KO)&15)*4096); of[(i)%G2D][0]=__builtin_amdgcn_raw_buffer_load_b128(wsr,obv,so_,SC1); of[(i)%G2D][1]=__builtin_amdgcn_raw_buffer_load_b128(wsr,obv+1024,so_,SC1); }while(0)
    const int h=lane>>5,blk=(lane>>4)&1,q=(lane&15)>>2,p=lane&3;
    lptr wa[2];
#pragma unroll
    for(int ct=0;ct<2;ct++){ int c=(ct*4+2*blk+(p>>1))^(((q>>1)&1)<<2); wa[ct]=(lptr)lds+L_WO+(31-G2D)*4096+(4*h+q)*128+c*16+8*(p&1); }
    f32x16 acc[2]; acc[0]=f32x16{}; acc[1]=f32x16{};
    s16x4 wf[2][8];
#define G2_LDW(i) do{ _Pragma("unroll") for(int s_=0;s_<2;s_++) _Pragma("unroll") for(int ct_=0;ct_<2;ct_++){ lptr a_=wa[ct_]+(G2D-(i))*4096+s_*2048; \
      wf[(i)&1][4*s_+2*ct_]=ldstr(a_); wf[(i)&1][4*s_+2*ct_+1]=ldstr(a_+1024); } }while(0)
    lds_wait(cnt+C_W1,4);
    G2_LDW(0);
    PIN(acc[0]); PIN(acc[1]);
    if(wid==3){ wait_flags(flagp(ws,blk_of(R2,lane&15),FK_O0),nonce); arrive(cnt+C_EF); } else lds_wait(cnt+C_EF,1);
    unsigned obv=ob;
#pragma unroll
    for(int i=0;i<G2D;i++) G2_LDO(i,i);
    u64 f1=0;
    constexpr int NIT=32/G2D, ITL=16/G2D-1;
#pragma unroll 1
    for(int it=0;it<NIT;it++){
      if(it==1) arrive(cnt+C_W2);
      if(it==ITL){
        if(wid==3){ if(!allof(f1==nonce)) wait_flags(flagp(ws,blk_of(R2,lane&15),FK_O1),nonce); MEMFENCE(); arrive(cnt+C_LF); }
        else lds_wait(cnt+C_LF,1);
        __builtin_amdgcn_s_setprio(3);
      }
      if(it==ITL+1) lds_wait(cnt+C_W2,4);
      if(it==NIT-1) arrive(cnt+C_G2);
      PIN(wa[0]); PIN(wa[1]);
      const int nb=G2D*it+G2D;
      obv=ob+((it<ITL)?0u:((it==NIT-1)?0x40000000u:2048u)); PIN(obv);
#pragma unroll
      for(int i=0;i<G2D;i++){
        SBAR();
        if(i==G2D-4){ if(wid==3 && it==ITL-1) f1=__hip_atomic_load(flagp(ws,blk_of(R2,lane&15),FK_O1),RLX_AGENT); }
        G2_LDW(i+1);
#pragma unroll
        for(int s=0;s<2;s++)
#pragma unroll
          for(int ct=0;ct<2;ct++) acc[ct]=MFMA32(cat(wf[i&1][4*s+2*ct],wf[i&1][4*s+2*ct+1]),B8(of[i][s]),acc[ct]);
#pragma unroll
        for(int g_=0;g_<4;g_++){ __builtin_amdgcn_sched_group_barrier(0x100,2,0); __builtin_amdgcn_sched_group_barrier(0x8,1,0); }
        PIN(acc[0]); PIN(acc[1]);
        SBAR();
        G2_LDO(i,nb+i);
      }
      wa[0]-=G2D*4096; wa[1]-=G2D*4096;
    }
    lwptr stg=(lwptr)lds+L_WO+(31-wid)*4096;
#pragma unroll
    for(int ct=0;ct<2;ct++){
#pragma unroll
      for(int gp=0;gp<2;gp++){
        unsigned a0=pk(acc[ct][8*gp],acc[ct][8*gp+1]), a1=pk(acc[ct][8*gp+2],acc[ct][8*gp+3]);
        unsigned b0=pk(acc[ct][8*gp+4],acc[ct][8*gp+5]), b1=pk(acc[ct][8*gp+6],acc[ct][8*gp+7]);
        auto r0=__builtin_amdgcn_permlane32_swap(a0,b0,false,false); auto r1=__builtin_amdgcn_permlane32_swap(a1,b1,false,false);
        u32x4 vv={r0[0],r1[0],r0[1],r1[1]};
        *(LAS u32x4*)(stg+(lane&31)*128+(((ct*4+gp*2+h)^(lane&7))*16))=vv;
      }
    }
#pragma unroll
    for(int i=0;i<4;i++){
      const int row=8*i+(lane>>3);
      const u32x4 vv=*(const LAS u32x4*)(stg+row*128+(((lane&7)^(row&7))*16));
      __builtin_amdgcn_raw_buffer_store_b128(vv,outr,(unsigned)(T2*32+row)*4096u+2048u+C2*128u+(lane&7)*16u,0,SC1);
    }
  } else {
    const int v=wid-4;
    const int chb=xcd*128+(v&1)*64, rb=512*(j>>3)+8*(j&7)+4*(v>>1)+64*(lane>>3);
    u32x4 qq[4],yy[4];
    const bool yr=__builtin_amdgcn_readfirstlane(*(volatile LAS unsigned*)(cnt+C_YF))!=0;
    MEMFENCE();
    unsigned yo=WS_Y+(unsigned)(chb+(lane>>2))*4096u+(unsigned)(j*128+(v>>1)*64+(lane&3)*16);
    unsigned yo1=yr? yo : 0x40000000u;
#pragma unroll
    for(int i=0;i<4;i++){ qq[i]=*(const u32x4*)(Aq+(size_t)(rb+i)*1024+chb+(lane&7)*8); yy[i]=__builtin_amdgcn_raw_buffer_load_b128(wsr,yo1,i*65536,SC1); }
    WAITV(8);
    arrive_publish(cnt+C_O1,4,flagp(ws,b,FK_O1),nonce,lane);
    __builtin_amdgcn_s_setprio(0);
    if(!yr){ wait_flags<8>(flagp(ws,(lane&31)*8+xcd,FK_Y),nonce);
#pragma unroll
      for(int i=0;i<4;i++) yy[i]=__builtin_amdgcn_raw_buffer_load_b128(wsr,yo,i*65536,SC1); }
    lds_wait(cnt+C_L,4);
    lwptr stg=(lwptr)lds+v*4096;
#pragma unroll
    for(int i=0;i<4;i++) *(LAS u32x4*)(stg+i*1024+lane*16)=yy[i];
    { const int g=lane>>4,q=(lane&15)>>2,p=lane&3;
      bf16x8 tt[4];
#pragma unroll
      for(int n=0;n<4;n++){ lptr a=(lptr)stg+(8*(2*n+(g>>1))+q)*64+(g&1)*32+8*p; tt[n]=cat(ldstr(a),ldstr(a+256)); }
      MEMFENCE();
      const int srow=16*(g&1)+(lane&15);
#pragma unroll
      for(int n=0;n<4;n++) *(LAS bf16x8*)(stg+srow*128+(((2*n+(g>>1))^(srow&7))*16))=tt[n];
    }
    u32x4 ro[4];
#pragma unroll
    for(int i=0;i<4;i++){
      const int row=8*i+(lane>>3);
      const u32x4 yt=*(const LAS u32x4*)(stg+row*128+(((lane&7)^(row&7))*16));
#pragma unroll
      for(int d=0;d<4;d++) ro[i][d]=pk(lo16(qq[i][d])*lo16(yt[d]),hi16(qq[i][d])*hi16(yt[d]));
      PIN(ro[i]);
    }
    if(b==0 && wid==7){
#pragma unroll 1
      for(unsigned sp=0;sp<SPIN_CAP;sp++){
        u64 f[4];
#pragma unroll
        for(int i=0;i<4;i++) f[i]=__hip_atomic_load(flagp(ws,i*64+lane,FK_Y),RLX_AGENT);
        if(allof(f[0]==nonce && f[1]==nonce && f[2]==nonce && f[3]==nonce)) break;
        __builtin_amdgcn_s_sleep(16);
      }
      if(lane==0) __hip_atomic_store((gu64*)(ws+F_E),nonce,RLX_AGENT);
    }
    lds_wait(cnt+C_G2,4);
#pragma unroll
    for(int i=0;i<4;i++) __builtin_amdgcn_raw_buffer_store_b128(ro[i],outr,(unsigned)(rb+i)*4096u+chb*2u+(lane&7)*16u,0,SC1);
  }
}

extern "C" void kernel_launch(void* const* d_in, const int* in_sizes, int n_in, void* d_out, int out_size, void* d_ws, size_t ws_size, hipStream_t stream){
  (void)hipFuncSetAttribute((const void*)fused,hipFuncAttributeMaxDynamicSharedMemorySize,LDS_BYTES);
  fused<<<256,512,LDS_BYTES,stream>>>((const u16*)d_in[0],(const u16*)d_in[1],(const u16*)d_in[2],(const u16*)d_in[3],(const u16*)d_in[4],(const u16*)d_in[5],
                                      (const u16*)d_in[6],(const u16*)d_in[7],(const u16*)d_in[8],(u16*)d_out,(char*)d_ws);
}
```

```cpp
#include <hip/hip_runtime.h>
#include <hip/hip_bf16.h>
#include <stdint.h>
#if defined(__HIP_DEVICE_COMPILE__)
#define NOPK __attribute__((target("no-packed-fp32-ops")))
#else
#define NOPK
#endif
typedef unsigned short u16;
typedef unsigned long long u64;
typedef short bf16x8 __attribute__((ext_vector_type(8)));
typedef short s16x4 __attribute__((ext_vector_type(4)));
typedef float f32x16 __attribute__((ext_vector_type(16)));
typedef float f32x4 __attribute__((ext_vector_type(4)));
typedef float f32x2 __attribute__((ext_vector_type(2)));
typedef __bf16 bf2 __attribute__((ext_vector_type(2)));
typedef unsigned u32x4 __attribute__((ext_vector_type(4)));
typedef unsigned u32x2 __attribute__((ext_vector_type(2)));
#define LAS __attribute__((address_space(3)))
#define GAS __attribute__((address_space(1)))
typedef LAS const char* lptr;
typedef LAS char* lwptr;
typedef GAS u64 gu64;
#define DI __device__ __forceinline__ NOPK
#define MFMA32(a,b,c) __builtin_amdgcn_mfma_f32_32x32x16_bf16((a),(b),(c),0,0,0)
#define MFMA16(a,b,c) __builtin_amdgcn_mfma_f32_16x16x32_bf16((a),(b),(c),0,0,0)
#define SBAR() __builtin_amdgcn_sched_barrier(0)
#define PIN(x) asm volatile("":"+v"(x))
#define MEMFENCE() asm volatile("":::"memory")
#define BAR() asm volatile("s_barrier":::"memory")
#define WAITV(n) asm volatile("s_waitcnt vmcnt(" #n ")":::"memory")
#define WAITL() asm volatile("s_waitcnt lgkmcnt(0)":::"memory")
#define RLX_AGENT __ATOMIC_RELAXED, __HIP_MEMORY_SCOPE_AGENT
#define RLX_WG __ATOMIC_RELAXED, __HIP_MEMORY_SCOPE_WORKGROUP
#define SC1 16
#define B8(x) __builtin_bit_cast(bf16x8,(x))

constexpr unsigned MB=1u<<20;
constexpr unsigned WS_KV=0, WS_O=8*MB, WS_XT=12*MB, WS_Y=16*MB, WS_F=20*MB, WS_END=21*MB;
constexpr unsigned F_E=WS_F, F_W=WS_F+4096;
enum { FK_KV0=0, FK_KV1, FK_O0, FK_O1, FK_X, FK_Y };
constexpr int G1_SLOT=40960;
constexpr int HR=57344;
constexpr int L_CV=126976, CV_W=8480, CV_H=128, CV_X=128+4096+160;
constexpr int L_MISC=L_CV+4*CV_W;
constexpr int L_WO=L_MISC-131072;
constexpr int WO_P1=19;
static_assert(L_WO+(32-WO_P1)*4096>=HR+24576 && L_WO>=24576,"wo image");
constexpr int L_CNT=L_MISC;
constexpr int L_JUNK=L_CNT+128;
constexpr int L_MASK=L_JUNK+256;
constexpr int LDS_BYTES=L_MASK+2048;
static_assert(LDS_BYTES<=163840,"lds");
enum { C_RD=0, C_S15, C_KV0, C_KV1, C_H0K, C_H1, C_P1E, C_P1L, C_E, C_L, C_W1, C_W2, C_O0, C_O1, C_EF, C_LF, C_XF, C_YF, C_G2, C_NUM };
constexpr int CODEL=247;
constexpr int G2D=8;
constexpr unsigned SPIN_CAP=1u<<17, LSPIN_CAP=1u<<22;

DI unsigned pk(float a,float b){ f32x2 v={a,b}; bf2 r=__builtin_convertvector(v,bf2); return __builtin_bit_cast(unsigned,r); }
DI float asf(unsigned u){ return __builtin_bit_cast(float,u); }
DI unsigned asu(float f){ return __builtin_bit_cast(unsigned,f); }
DI bool allof(bool p){ return __builtin_amdgcn_ballot_w64(p)==~0ull; }
DI float lo16(unsigned u){ return asf(u<<16); }
DI float hi16(unsigned u){ return asf(u&0xffff0000u); }
DI bf16x8 ldsr128(lptr p){ return *(const LAS bf16x8*)p; }
DI s16x4 ldstr(lptr p){ return __builtin_amdgcn_ds_read_tr16_b64_v4i16((LAS s16x4*)p); }
DI bf16x8 cat(s16x4 a,s16x4 b){ return __builtin_shufflevector(a,b,0,1,2,3,4,5,6,7); }
DI u32x4 pack8(const f32x16& x,int s,float sc){ u32x4 r; r[0]=pk(x[8*s]*sc,x[8*s+1]*sc); r[1]=pk(x[8*s+2]*sc,x[8*s+3]*sc); r[2]=pk(x[8*s+4]*sc,x[8*s+5]*sc); r[3]=pk(x[8*s+6]*sc,x[8*s+7]*sc); return r; }
DI u32x4 pack8(const f32x16& x,int s){ u32x4 r; r[0]=pk(x[8*s],x[8*s+1]); r[1]=pk(x[8*s+2],x[8*s+3]); r[2]=pk(x[8*s+4],x[8*s+5]); r[3]=pk(x[8*s+6],x[8*s+7]); return r; }
#define DMA4_BODY(FL) "s_mov_b32 m0, %2\n\ts_nop 0\n\t" \
   "global_load_lds_dwordx4 %0, %1" FL "\n\tglobal_load_lds_dwordx4 %0, %1 offset:1024" FL "\n\tglobal_load_lds_dwordx4 %0, %1 offset:2048" FL "\n\tglobal_load_lds_dwordx4 %0, %1 offset:3072" FL
DI void dma4(unsigned voff,const void* sbase,unsigned ldsaddr){ asm volatile(DMA4_BODY(" nt") :: "v"(voff), "s"(sbase), "s"(ldsaddr) : "memory"); }
DI void dma4_sc1(unsigned voff,const void* sbase,unsigned ldsaddr){ asm volatile(DMA4_BODY(" sc1") :: "v"(voff), "s"(sbase), "s"(ldsaddr) : "memory"); }
DI void dma4v(unsigned v0,unsigned v1,unsigned v2,unsigned v3,const void* sbase,unsigned ldsaddr){
  asm volatile("s_mov_b32 m0, %5\n\ts_nop 0\n\t"
    "global_load_lds_dwordx4 %0, %4\n\tglobal_load_lds_dwordx4 %1, %4 offset:1024\n\tglobal_load_lds_dwordx4 %2, %4 offset:2048\n\tglobal_load_lds_dwordx4 %3, %4 offset:3072"
    :: "v"(v0),"v"(v1),"v"(v2),"v"(v3), "s"(sbase), "s"(ldsaddr) : "memory");
}
DI void touch(unsigned voff,const void* sbase,unsigned junk){
  asm volatile("s_mov_b32 m0, %2\n\ts_nop 0\n\tglobal_load_lds_dword %0, %1" :: "v"(voff), "s"(sbase), "s"(junk) : "memory");
}
DI void touch64(const void* a,unsigned junk){
  asm volatile("s_mov_b32 m0, %1\n\ts_nop 0\n\tglobal_load_lds_dword %0, off" :: "v"(a), "s"(junk) : "memory");
}
template<int SL=2>
DI void wait_flags(const gu64* p,u64 nonce){
#pragma unroll 1
  for(unsigned sp=0;sp<SPIN_CAP;sp++){
    u64 v=__hip_atomic_load(p,RLX_AGENT);
    if(allof(v==nonce)) break;
    __builtin_amdgcn_s_sleep(SL);
  }
  MEMFENCE();
}
DI void lds_wait(LAS unsigned* p,unsigned n){
#pragma unroll 1
  for(unsigned sp=0;sp<LSPIN_CAP;sp++){
    unsigned v=__hip_atomic_load(p,RLX_WG);
    if(__builtin_amdgcn_readfirstlane(v)>=n) break;
    __builtin_amdgcn_s_sleep(1);
  }
  MEMFENCE();
}
DI void arrive(LAS unsigned* cnt){
  unsigned one=1u;
  asm volatile("s_mov_b64 exec, 1\n\tds_add_u32 %0, %1\n\ts_mov_b64 exec, -1" :: "v"(cnt), "v"(one) : "memory");
}
DI void arrive_publish(LAS unsigned* cnt,unsigned n,gu64* flag,u64 nonce,int lane){
  unsigned one=1u, old;
  asm volatile("s_mov_b64 exec, 1\n\tds_add_rtn_u32 %0, %1, %2\n\ts_mov_b64 exec, -1\n\ts_waitcnt lgkmcnt(0)" : "=&v"(old) : "v"(cnt), "v"(one) : "memory");
  if(__builtin_amdgcn_readfirstlane(old)==n-1){ if(lane==0) __hip_atomic_store(flag,nonce,RLX_AGENT); }
  MEMFENCE();
}
DI gu64* flagp(char* ws,int blk,int k){ return (gu64*)(ws+F_W+(unsigned)(blk*8+k)*128u); }
DI int blk_of(int R,int C){ return (((R&7)*4+(C&3))<<3)|((R>>3)*4+(C>>2)); }
DI float fmax3(float a,float b,float c){ return __builtin_fmaxf(__builtin_fmaxf(a,b),c); }
DI float xhalf_max(float m){ auto rr=__builtin_amdgcn_permlane32_swap(asu(m),asu(m),false,false); return __builtin_fmaxf(asf(rr[0]),asf(rr[1])); }
struct G1F { bf16x8 x0,x1; s16x4 w[6]; };

DI const gu64* kvflag(char* ws,int R,int C,int kind,int head,int lane){
  const int Cs=(C&12)|((C-1-kind)&3); const int rr=(lane<3)? lane : 2; const int Rl=(R-2+rr<0)? R : R-2+rr;
  return flagp(ws,blk_of(Rl,Cs),FK_KV0+head);
}
DI void kv_fetch(char* ws,unsigned lds0,int R,int C,int kind,int head,int half,int lane){
  const int Cs=(C&12)|((C-1-kind)&3);
#pragma unroll 1
  for(int i=(R<2)?2-R:0;i<3;i++){
    const char* src=ws+WS_KV+(size_t)blk_of(R-2+i,Cs)*32768+head*16384+kind*8192+half*4096;
    const unsigned dst=lds0+(1-head)*HR+(1-kind)*24576+i*8192+half*4096;
    dma4_sc1(lane*16,src,dst);
  }
}
template<bool CHECK>
DI void attention(char* lds,LAS unsigned* cnt,char* ws,__amdgpu_buffer_rsrc_t wsr,int b,int R,int C,int wid,int lane,u64 nonce){
  const int late=wid>>2, jq=wid&3;
  const int T=4*R+jq;
  lptr VL=(lptr)lds+(1-late)*HR+jq*2048+lane*16;
  lptr KL=VL+24576;
  lptr QL=(lptr)lds+(1-late)*HR+49152+jq*2048+lane*16;
  const f32x16 zero=f32x16{};
  const float ninf=-__builtin_inff();
  const int dl=(lane&31)-4*(lane>>5);
  bf16x8 kf[9][2];
  const bf16x8 q0=ldsr128(QL), q1=ldsr128(QL+1024);
  kf[8][0]=ldsr128(KL+8*2048); kf[8][1]=ldsr128(KL+8*2048+1024);
#pragma unroll
  for(int u=0;u<4;u++){ kf[u][0]=ldsr128(KL+u*2048); kf[u][1]=ldsr128(KL+u*2048+1024); }
  f32x16 sd,sm,s[3];
  float m;
#pragma unroll
  for(int n=0;n<11;n++){
    SBAR();
    if(n==0){ sd=MFMA32(kf[8][0],q0,zero); sd=MFMA32(kf[8][1],q1,sd); PIN(sd); }
    else if(n==1){ sm=MFMA32(kf[0][0],q0,zero); sm=MFMA32(kf[0][1],q1,sm); PIN(sm); }
    else if(n<9){ const int u=n-1; s[u%3]=MFMA32(kf[u][0],q0,zero); s[u%3]=MFMA32(kf[u][1],q1,s[u%3]); PIN(s[u%3]); }
    if(n<4){ const int u=n+4; kf[u][0]=ldsr128(KL+u*2048); kf[u][1]=ldsr128(KL+u*2048+1024); }
    if(n==3){
#pragma unroll
      for(int i=0;i<16;i++){ const int a=(i&3)+8*(i>>2); sm[i]=(a<=dl)?sd[i]:((CHECK&&T<8)?ninf:sm[i]); }
    }
    if(n>=3){
      const int t=n-3; const f32x16& x=(t==0)?sm:s[t%3];
      float a=fmax3(x[0],x[1],x[2]), c=fmax3(x[3],x[4],x[5]);
      a=fmax3(a,x[6],x[7]); c=fmax3(c,x[8],x[9]); a=fmax3(a,x[10],x[11]); c=fmax3(c,x[12],x[13]); a=fmax3(a,x[14],x[15]);
      if(t==0) m=__builtin_fmaxf(a,c);
      else { const float mm=fmax3(m,a,c); m=(!CHECK||T-8+t>=0)?mm:m; }
      PIN(m);
    }
  }
  SBAR();
  arrive(cnt+(late?C_P1L:C_P1E));
  if(late){
    WAITV(0); arrive(cnt+C_W1);
    lds_wait(cnt+C_E,4); __builtin_amdgcn_s_setprio(2);
  }
  bf16x8 vfr[2][2];
  vfr[0][0]=ldsr128(VL+8*2048); vfr[0][1]=ldsr128(VL+8*2048+1024);
  vfr[1][0]=ldsr128(VL); vfr[1][1]=ldsr128(VL+1024);
  const u32x4 mk0=*(const LAS u32x4*)((lptr)lds+L_MASK+lane*16), mk1=*(const LAS u32x4*)((lptr)lds+L_MASK+1024+lane*16);
  m=xhalf_max(m);
  f32x16 negm;
#pragma unroll
  for(int i=0;i<16;i++) negm[i]=-m;
  PIN(negm);
  const unsigned one2=((((lane>>2)^(lane>>4))&1)==0)?0x3f803f80u:0u;
  const u32x4 ones={one2,one2,one2,one2};
  f32x16 o; f32x4 ls; u64 fy=0;
  u32x4 p0,p1;
#pragma unroll
  for(int u=0;u<=8;u++){
    SBAR();
    if(u==2){ if(wid==4) fy=__hip_atomic_load(flagp(ws,(lane&31)*8+(b&7),FK_Y),RLX_AGENT); SBAR(); }
    if(u==6){ if(wid==4){ PIN(fy); if(allof(fy==nonce)) arrive(cnt+C_YF); } SBAR(); }
    u32x4 n0,n1;
    if(u+1<8){ const int t=u+1; s[t&1]=MFMA32(kf[t][0],q0,negm); s[t&1]=MFMA32(kf[t][1],q1,s[t&1]); }
    if(u<8){
      f32x16& x=(u==0)?sm:s[u&1];
#pragma unroll
      for(int i=0;i<16;i++) x[i]=__builtin_amdgcn_exp2f((u==0)?x[i]-m:x[i]);
      n0=pack8(x,0); n1=pack8(x,1);
      if(CHECK && u>0){ if(T-8+u<0){ n0=u32x4{0,0,0,0}; n1=u32x4{0,0,0,0}; } }
    }
    if(u==1){
      const u32x4 d0=p0&mk0, d1=p1&mk1;
      o=MFMA32(vfr[0][0],B8(d0),zero); o=MFMA32(vfr[0][1],B8(d1),o);
      o=MFMA32(vfr[1][0],B8(p0^d0),o); o=MFMA32(vfr[1][1],B8(p1^d1),o);
      vfr[1][0]=ldsr128(VL+1*2048); vfr[1][1]=ldsr128(VL+1*2048+1024);
      vfr[0][0]=ldsr128(VL+2*2048); vfr[0][1]=ldsr128(VL+2*2048+1024);
      ls=MFMA16(B8(ones),B8(p0),(f32x4{0,0,0,0})); ls=MFMA16(B8(ones),B8(p1),ls);
    } else if(u>1){
      const int t=u-1;
      o=MFMA32(vfr[t&1][0],B8(p0),o); o=MFMA32(vfr[t&1][1],B8(p1),o);
      if(t+2<8){ vfr[t&1][0]=ldsr128(VL+(t+2)*2048); vfr[t&1][1]=ldsr128(VL+(t+2)*2048+1024); }
      ls=MFMA16(B8(ones),B8(p0),ls); ls=MFMA16(B8(ones),B8(p1),ls);
    }
    if(u>1 && u<7 && !CHECK){
#pragma unroll
      for(int g_=0;g_<6;g_++){ __builtin_amdgcn_sched_group_barrier(0x8,1,0); __builtin_amdgcn_sched_group_barrier(0x402,4,0); }
    }
    if(u+1<8) PIN(s[(u+1)&1]);
    if(u>0){ PIN(ls); PIN(o); }
    if(u<8){ PIN(n0); PIN(n1); p0=n0; p1=n1; }
  }
  SBAR();
  arrive(cnt+(late?C_L:C_E));
  const float il=__builtin_amdgcn_rcpf(ls[0]);
  const unsigned oo=WS_O+(unsigned)(T*64+2*(2*C+late))*1024u+lane*16u;
  __builtin_amdgcn_raw_buffer_store_b128(pack8(o,0,il),wsr,oo,0,SC1);
  __builtin_amdgcn_raw_buffer_store_b128(pack8(o,1,il),wsr,oo+1024,0,SC1);
}

__global__ __launch_bounds__(512,2) NOPK void fused(const u16* Aq,const u16* Ak,const u16* Av,const u16* Ask,const u16* Ah,const u16* AD,
                                               const u16* X,const u16* W,const u16* WO,u16* out,char* ws){
  extern __shared__ __attribute__((aligned(16))) char lds[];
  const char* kpc; asm volatile("s_getpc_b64 %0":"=s"(kpc));
  asm volatile(""::"s"(X),"s"(W),"s"(ws),"s"(WO),"s"(Ak));
  const int tid=threadIdx.x, lane=tid&63; const int wid=__builtin_amdgcn_readfirstlane(tid>>6);
  const int b=blockIdx.x, xcd=b&7, j=b>>3;
  const int R=(xcd>>2)*8+(j>>2), C=(xcd&3)*4+(j&3);
  const int wgi=xcd*32+j;
  const int R2=((xcd>>1)^2)*4+(j>>3), C2=(xcd&1)*8+(j&7), KO=2*(j&7)+4*(j>>3);
  const unsigned lds0=(unsigned)(uintptr_t)lds;
  u64 ew=__hip_atomic_load((gu64*)(ws+F_E),RLX_AGENT);
  const unsigned a0=(unsigned)((lane>>3)*2048+(((lane&7)^(lane>>4))*16)), a1=(a0^64u)+(16384-1024);
  const unsigned b0=(unsigned)((lane>>3)*6144+(((lane&7)^(((lane>>4)&1)<<2))*16));
  const char* xb=(const char*)X+(size_t)R*128*2048; const char* wb=(const char*)W+(C&12)*128;
  const int cs[3]={(C&3)*128,2048+((C+1)&3)*128,4096+((C+2)&3)*128};
#pragma unroll
  for(int k=0;k<2;k++){
    asm volatile("s_mov_b32 m0, %3\n\ts_nop 0\n\tglobal_load_lds_dwordx4 %0, %2\n\tglobal_load_lds_dwordx4 %1, %2 offset:1024" :: "v"(a0),"v"(a1),"s"(xb+wid*32768+k*128),"s"(lds0+k*G1_SLOT+wid*2048) : "memory");
#pragma unroll
    for(int g=0;g<3;g++)
      asm volatile("s_mov_b32 m0, %2\n\ts_nop 0\n\tglobal_load_lds_dwordx4 %0, %1" :: "v"(b0),"s"(wb+cs[g]+wid*49152+k*(64*6144)),"s"(lds0+k*G1_SLOT+16384+g*8192+wid*1024) : "memory");
    if(k==0){ if(lane==0) touch((unsigned)(((j*8+wid<CODEL)?j*8+wid:CODEL-1)*128),kpc,lds0+L_JUNK); }
    SBAR();
  }
  LAS unsigned* cnt=(LAS unsigned*)((lwptr)lds+L_CNT);
  const __amdgpu_buffer_rsrc_t wsr=__builtin_amdgcn_make_buffer_rsrc(ws,0,WS_END,0x00020000);
  const __amdgpu_buffer_rsrc_t outr=__builtin_amdgcn_make_buffer_rsrc(out,0,8*MB,0x00020000);
  u64 nonce;
#define LOAD_NONCE() do{ PIN(ew); nonce=((u64)__builtin_amdgcn_readfirstlane((unsigned)(ew>>32))<<32)|(unsigned)__builtin_amdgcn_readfirstlane((unsigned)ew); \
    nonce=nonce*6364136223846793005ull+1442695040888963407ull; }while(0)
  auto wo_block=[&](int i) NOPK __attribute__((always_inline)) {
    const int hd=2*((i+KO)&15)+(i>>4);
    const unsigned vo=(unsigned)((lane>>3)*2048+C2*128+(((lane&7)^(((lane>>4)&1)<<2))*16));
    dma4v(vo,vo+16384-1024,vo+2*(16384-1024),vo+3*(16384-1024),(const char*)WO+(size_t)hd*65536,lds0+L_WO+(31-i)*4096);
  };
  if(__builtin_expect(wid>=4,1)){
    const int v=wid-4;
    const unsigned junk=lds0+L_JUNK;
    if(__builtin_expect(wid==4,1)){
      xb+=256; wb+=2*64*6144;
      unsigned la=lds0+2*G1_SLOT;
      WAITV(6);
#pragma unroll 1
      for(int k=0;k<14;k++){
        WAITV(40); BAR();
#pragma unroll
        for(int g=0;g<4;g++){
          dma4v(a0,a1,a0+2*(16384-1024),a1+2*(16384-1024),xb+g*65536,la+g*4096);
          if(g<3){
            dma4v(b0,b0+(49152-1024),b0+2*(49152-1024),b0+3*(49152-1024),wb+cs[g],la+16384+g*8192);
            dma4v(b0,b0+(49152-1024),b0+2*(49152-1024),b0+3*(49152-1024),wb+cs[g]+32*6144,la+16384+g*8192+4096);
          }
        }
        xb+=128; wb+=64*6144; la+=G1_SLOT; if(la==lds0+3*G1_SLOT) la=lds0;
      }
      WAITV(40); BAR();
      WAITV(0); arrive(cnt+C_S15);
      LOAD_NONCE();
    } else if(wid==5){
      __builtin_amdgcn_s_setprio(3);
      const char* ta; unsigned tstep;
      { const int sg=lane/20, i=lane-sg*20; const bool isx=i<8; const int rem=i-8, kr=(xcd>>2)*32+j;
        tstep=isx?128u:64u*6144u;
        ta=(isx? (const char*)X+(size_t)((xcd>>2)*1024+((xcd&3)*32+j)*8+i)*2048 : (const char*)W+(kr*6144+(rem>>2)*2048+((xcd&3)*4+(rem&3))*128))+(size_t)tstep*(2+sg);
        tstep*=3; }
#define STOUCH(n) do{ if(lane<(n)) touch64(ta,junk); ta+=tstep; }while(0)
      STOUCH(60);
      if(j==0) *(LAS u32x4*)((lwptr)lds+L_CV+lane*16)=u32x4{0,0,0,0};
      if(lane<C_NUM) cnt[lane]=0u;
      { const int bq=lane&31, hi=lane>>5; u32x4 mk[2];
#pragma unroll
        for(int d=0;d<8;d++){ const int i=2*d; const int a=(i&3)+8*(i>>2)+4*hi; mk[d>>2][d&3]=((a<=bq)?0xffffu:0u)|((a+1<=bq)?0xffff0000u:0u); }
        *(LAS u32x4*)((lwptr)lds+L_MASK+lane*16)=mk[0]; *(LAS u32x4*)((lwptr)lds+L_MASK+1024+lane*16)=mk[1]; }
      WAITL();
      WAITV(7);
      BAR();
      WAITV(1);
      if(lane<6) asm volatile("s_mov_b32 m0, %2\n\ts_nop 0\n\tglobal_load_lds_dword %0, %1 sc1" :: "v"((unsigned)(F_W+(b*8+lane)*128)), "s"(ws), "s"(junk) : "memory");
      const unsigned pv=(unsigned)((lane>>4)*2048+(lane&15)*16);
      const char* ksrc=(const char*)Ak+(size_t)j*64*2048+xcd*256; const char* vsrc=(const char*)Av+(size_t)j*64*2048+xcd*256;
      if(j>0) asm volatile("s_mov_b32 m0, %2\n\ts_nop 0\n\tglobal_load_lds_dwordx4 %0, %1" :: "v"(pv^(7u<<5)), "s"(ksrc-4*2048), "s"(lds0+L_CV) : "memory");
      STOUCH(60); STOUCH(60);
      BAR();
      STOUCH(60); STOUCH(40);
      touch((unsigned)((wgi*64+lane)*128),WO,junk);
#pragma unroll 1
      for(int g2=0;g2<4;g2++){
        const unsigned p0=pv^(unsigned)(g2<<6), p1=p0^32u;
        dma4v(p0,p0+(8192-1024),p1+2*(8192-1024),p1+3*(8192-1024),ksrc+g2*32768,lds0+L_CV+1024+g2*4096);
        dma4v(p0,p0+(8192-1024),p1+2*(8192-1024),p1+3*(8192-1024),vsrc+g2*32768,lds0+L_CV+17408+g2*4096);
      }
      BAR();
      WAITV(0);
#pragma unroll 1
      for(int i=3;i<=12;i++) BAR();
      u64 fxv=__hip_atomic_load(flagp(ws,(lane&31)*8+xcd,FK_X),RLX_AGENT);
      BAR();
      SBAR(); PIN(fxv);
      LOAD_NONCE();
      if(allof(fxv==nonce)){ arrive(cnt+C_XF); WAITL(); }
      BAR();
      __builtin_amdgcn_s_setprio(0);
    } else {
      __builtin_amdgcn_s_setprio(1);
      const int hv=wid-6, rg=lane&7;
      u32x4 sk[4];
      WAITV(6);
      BAR();
      WAITV(0);
      LOAD_NONCE();
#pragma unroll
      for(int i=0;i<4;i++) sk[i]=*(const u32x4*)(Ask+(xcd*128+(hv*16+(i>>1)*8+(lane>>3))*4)*4+(i&1)*8);
      BAR(); BAR(); BAR();
      SBAR();
      asm volatile("":"+v"(sk[0]),"+v"(sk[1]),"+v"(sk[2]),"+v"(sk[3]));
#pragma unroll 1
      for(int g=0;g<2;g++){
        const int c4=hv*16+g*8+(lane>>3);
        u32x2 kk[11], vv[8];
        lptr kb=(lptr)lds+L_CV+(8*rg+4)*256+(c4&1)*8;
#pragma unroll
        for(int i=0;i<3;i++) kk[i]=*(const LAS u32x2*)(kb+(i-3)*256+(((c4>>1)^(((rg-1)&7)<<1))*16));
#pragma unroll
        for(int i=0;i<8;i++){ kk[3+i]=*(const LAS u32x2*)(kb+i*256+(((c4>>1)^(rg<<1))*16)); vv[i]=*(const LAS u32x2*)(kb+16384+i*256+(((c4>>1)^(rg<<1))*16)); }
#pragma unroll
        for(int c=0;c<4;c++){
          const unsigned w01=sk[c>>1][(c&1)*2], w23=sk[c>>1][(c&1)*2+1];
          const float w0=lo16(w01),w1=hi16(w01),w2=lo16(w23),w3=hi16(w23);
          float kf[11];
#pragma unroll
          for(int i=0;i<11;i++) kf[i]=(c&1)?hi16(kk[i][c>>1]):lo16(kk[i][c>>1]);
          float o[8];
#pragma unroll
          for(int i=0;i<8;i++){ float vf=(c&1)?hi16(vv[i][c>>1]):lo16(vv[i][c>>1]); o[i]=(kf[i]*w3+kf[i+1]*w2+kf[i+2]*w1+kf[i+3]*w0)*vf; }
          u32x4 r; r[0]=pk(o[0],o[1]); r[1]=pk(o[2],o[3]); r[2]=pk(o[4],o[5]); r[3]=pk(o[6],o[7]);
          __builtin_amdgcn_raw_buffer_store_b128(r,wsr,WS_XT+(unsigned)(xcd*128+c4*4+c)*4096u+(unsigned)(j*128+rg*16),0,SC1);
          SBAR();
          if(c!=1){ BAR(); SBAR(); }
        }
        sk[0]=sk[2]; sk[1]=sk[3];
      }
      WAITV(0);
      BAR();
      if(hv==0 && lane==0) __hip_atomic_store(flagp(ws,b,FK_X),nonce,RLX_AGENT);
      BAR(); BAR(); BAR(); BAR();
    }
    const int ch=wgi*4+v;
    const float Dc=lo16(AD[ch]);
    const bool xr=__builtin_amdgcn_readfirstlane(cnt[C_XF])!=0;
    dma4(lane*16,(const char*)Ah+(size_t)ch*4096,lds0+L_CV+v*CV_W+CV_H);
    if(xr) dma4_sc1(lane*16,ws+WS_XT+(size_t)ch*4096,lds0+L_CV+v*CV_W+CV_X);
    __builtin_amdgcn_s_setprio(2);
    if(!xr){ wait_flags<4>(flagp(ws,(lane&31)*8+xcd,FK_X),nonce); dma4_sc1(lane*16,ws+WS_XT+(size_t)ch*4096,lds0+L_CV+v*CV_W+CV_X); }
    WAITV(4);
    if(lane<32) *(LAS unsigned*)((lwptr)lds+L_CV+v*CV_W+lane*4)=0u;
    if(lane<40) *(LAS unsigned*)((lwptr)lds+L_CV+v*CV_W+CV_H+4096+lane*4)=0u;
    bool h1;
    {
      lwptr LW=(lwptr)lds+L_CV+v*CV_W;
      const int g=lane>>4,q=(lane&15)>>2,p=lane&3;
      lptr ha=(lptr)LW+CV_H+8*(4*p+8*g+q-12);
      lptr xa=(lptr)LW+CV_X+8*(p+8-8*g-q);
      PIN(ha); PIN(xa);
      bf16x8 A[16],B0[16],B1[16];
#pragma unroll
      for(int d=0;d<16;d++) A[d]=cat(ldstr(ha+256*d),ldstr(ha+256*d+32));
      WAITV(0);
      float zf=0.f; PIN(zf);
#define MM0(z,a,b) asm volatile("v_mfma_f32_16x16x32_bf16 %0, %1, %2, 0" : "=&v"(z) : "v"(a), "v"(b))
#define MMA(z,a,b) asm volatile("v_mfma_f32_16x16x32_bf16 %0, %1, %2, %0" : "+v"(z) : "v"(a), "v"(b))
#define LDB(P,e) B##P[e]=cat(ldstr(xa+256*(e)+128*(P)+32),ldstr(xa+256*(e)+128*(P)))
#define ROR2(d,s,z,e,t,y) asm volatile("v_add_f32_dpp %0, %2, %3 wave_ror:1 row_mask:0xf bank_mask:0xf\n\tv_add_f32_dpp %1, %4, %5 wave_ror:1 row_mask:0xf bank_mask:0xf" : "=&v"(d), "=&v"(e) : "v"(s), "v"(z), "v"(t), "v"(y))
#define SHR2(d,s,z,e,t,y) asm volatile("v_add_f32_dpp %0, %2, %3 wave_shr:1 row_mask:0xf bank_mask:0xf\n\tv_add_f32_dpp %1, %4, %5 wave_shr:1 row_mask:0xf bank_mask:0xf" : "+v"(d), "+v"(e) : "v"(s), "v"(z), "v"(t), "v"(y))
      const unsigned yo=WS_Y+(unsigned)ch*4096u+lane*16u;
#define STY(C) __builtin_amdgcn_raw_buffer_store_b128(yw[C],wsr,yo+(C)*1024,0,SC1)
#define NOP1() asm volatile("s_nop 0")
#define VNOP() __builtin_amdgcn_s_setprio(2)
      unsigned xa_,xb_;
      lptr xe=(lptr)LW+CV_X+2*lane; PIN(xe);
#define LDX(c) do{ xa_=*(const LAS u16*)(xe+256*(c)); xb_=*(const LAS u16*)(xe+256*(c)+128); }while(0)
#define SHX() do{ xa_<<=16; xb_<<=16; PIN(xa_); PIN(xb_); }while(0)
#define FMD(z,x) do{ z[0]=__builtin_fmaf(asf(x),Dc,z[0]); PIN(z); }while(0)
      const gu64* kf1=kvflag(ws,R,C,v>>1,1,lane);
      u64 f1v; unsigned c1v;
#define HOOK1() do{ f1v=__hip_atomic_load(kf1,RLX_AGENT); c1v=*(volatile LAS unsigned*)(cnt+C_KV1); }while(0)
#define HOOK2() do{ h1=allof(f1v==nonce) && __builtin_amdgcn_readfirstlane(c1v)>=2; MEMFENCE(); if(h1) kv_fetch(ws,lds0,R,C,v>>1,1,v&1,lane); }while(0)
      SBAR();
f32x4 z0,z1,z2,z3,z4,z5,z6,z7,z8,z9,z10,z11,z12,z13,z14,z15,z16,z17,z18,z19,z20,z21,z22,z23,z24,z25,z26,z27,z28,z29,z30,z31;
float t2_0,t1_0,u_0,t2_1,t1_1,u_1,t2_2,t1_2,u_2,t2_3,t1_3,u_3,t2_4,t1_4,u_4,t2_5,t1_5,u_5,t2_6,t1_6,u_6,t2_7,t1_7,u_7,t2_8,t1_8,u_8,t2_9,t1_9,u_9,t2_10,t1_10,u_10,t2_11,t1_11,u_11,t2_12,t1_12,u_12,t2_13,t1_13,u_13,t2_14,t1_14,u_14,t2_15,t1_15,u_15,t2_16,t1_16,u_16,t2_17,t1_17,u_17,t2_18,t1_18,u_18,t2_19,t1_19,u_19,t2_20,t1_20,u_20,t2_21,t1_21,u_21,t2_22,t1_22,u_22,t2_23,t1_23,u_23,t2_24,t1_24,u_24,t2_25,t1_25,u_25,t2_26,t1_26,u_26,t2_27,t1_27,u_27,t2_28,t1_28,u_28,t2_29,t1_29,u_29,t2_30,t1_30,u_30,t2_31,t1_31,u_31;
u32x4 yw[4];

LDB(0,0); LDB(1,0);
LDB(0,1); LDB(1,1);
LDB(0,2); LDB(1,2);
LDX(0);
SBAR();
      MM0(z0,A[0],B0[0]); SBAR(); LDB(0,3); SBAR();
      MM0(z1,A[0],B1[0]); SBAR(); LDB(1,3); SBAR();
      MM0(z2,A[1],B0[0]); SBAR(); LDB(0,4); SBAR();
      MM0(z3,A[1],B1[0]); SBAR(); LDB(1,4); SBAR();
      MMA(z2,A[0],B0[1]); SBAR();
      MMA(z3,A[0],B1[1]); SBAR();
      MM0(z4,A[2],B0[0]); SBAR();
      MM0(z5,A[2],B1[0]); SBAR();
      MMA(z4,A[1],B0[1]); SBAR();
      MMA(z5,A[1],B1[1]); SBAR();
      MMA(z4,A[0],B0[2]); SBAR(); ROR2(t2_0,zf,z0[2],t2_1,z0[3],z1[2]); SBAR();
      MMA(z5,A[0],B1[2]); SBAR(); LDB(0,5); SBAR();
      MM0(z6,A[3],B0[0]); SBAR(); SHR2(t2_0,z0[3],z0[2],t2_1,z1[3],z1[2]); SBAR();
      MM0(z7,A[3],B1[0]); SBAR(); LDB(1,5); SBAR();
      MMA(z6,A[2],B0[1]); SBAR(); ROR2(t1_0,zf,z0[1],t1_1,t2_0,z1[1]); SBAR();
      MMA(z7,A[2],B1[1]); SBAR(); SHX(); SBAR();
      MMA(z6,A[1],B0[2]); SBAR(); SHR2(t1_0,t2_0,z0[1],t1_1,t2_1,z1[1]); SBAR();
      MMA(z7,A[1],B1[2]); SBAR(); FMD(z0,xa_); FMD(z1,xb_); SBAR();
      MMA(z6,A[0],B0[3]); SBAR(); ROR2(u_0,zf,z0[0],u_1,t1_0,z1[0]); SBAR();
      MMA(z7,A[0],B1[3]); SBAR(); LDX(1); SBAR();
      MM0(z8,A[4],B0[0]); SBAR(); SHR2(u_0,t1_0,z0[0],u_1,t1_1,z1[0]); SBAR();
      MM0(z9,A[4],B1[0]); SBAR(); yw[0][0]=pk(u_0,u_1); PIN(yw[0]); SBAR();
      MMA(z8,A[3],B0[1]); SBAR(); ROR2(t2_2,z1[3],z2[2],t2_3,z2[3],z3[2]); SBAR();
      MMA(z9,A[3],B1[1]); SBAR(); LDB(0,6); SBAR();
      MMA(z8,A[2],B0[2]); SBAR(); SHR2(t2_2,z2[3],z2[2],t2_3,z3[3],z3[2]); SBAR();
      MMA(z9,A[2],B1[2]); SBAR(); LDB(1,6); SBAR();
      MMA(z8,A[1],B0[3]); SBAR(); ROR2(t1_2,t2_1,z2[1],t1_3,t2_2,z3[1]); SBAR();
      MMA(z9,A[1],B1[3]); SBAR(); SHX(); SBAR();
      MMA(z8,A[0],B0[4]); SBAR(); SHR2(t1_2,t2_2,z2[1],t1_3,t2_3,z3[1]); SBAR();
      MMA(z9,A[0],B1[4]); SBAR(); FMD(z2,xa_); FMD(z3,xb_); SBAR();
      MM0(z10,A[5],B0[0]); SBAR(); ROR2(u_2,t1_1,z2[0],u_3,t1_2,z3[0]); SBAR();
      MM0(z11,A[5],B1[0]); SBAR(); LDX(2); SBAR();
      MMA(z10,A[4],B0[1]); SBAR(); SHR2(u_2,t1_2,z2[0],u_3,t1_3,z3[0]); SBAR();
      MMA(z11,A[4],B1[1]); SBAR(); yw[0][1]=pk(u_2,u_3); PIN(yw[0]); SBAR();
      MMA(z10,A[3],B0[2]); SBAR(); ROR2(t2_4,z3[3],z4[2],t2_5,z4[3],z5[2]); SBAR();
      MMA(z11,A[3],B1[2]); SBAR(); LDB(0,7); SBAR();
      MMA(z10,A[2],B0[3]); SBAR(); SHR2(t2_4,z4[3],z4[2],t2_5,z5[3],z5[2]); SBAR();
      MMA(z11,A[2],B1[3]); SBAR(); LDB(1,7); SBAR();
      MMA(z10,A[1],B0[4]); SBAR(); ROR2(t1_4,t2_3,z4[1],t1_5,t2_4,z5[1]); SBAR();
      MMA(z11,A[1],B1[4]); SBAR(); SHX(); SBAR();
      MMA(z10,A[0],B0[5]); SBAR(); SHR2(t1_4,t2_4,z4[1],t1_5,t2_5,z5[1]); SBAR();
      MMA(z11,A[0],B1[5]); SBAR(); FMD(z4,xa_); FMD(z5,xb_); SBAR();
      MM0(z12,A[6],B0[0]); SBAR(); ROR2(u_4,t1_3,z4[0],u_5,t1_4,z5[0]); SBAR();
      MM0(z13,A[6],B1[0]); SBAR(); LDX(3); SBAR();
      MMA(z12,A[5],B0[1]); SBAR(); SHR2(u_4,t1_4,z4[0],u_5,t1_5,z5[0]); SBAR();
      MMA(z13,A[5],B1[1]); SBAR(); yw[0][2]=pk(u_4,u_5); PIN(yw[0]); SBAR();
      MMA(z12,A[4],B0[2]); SBAR(); ROR2(t2_6,z5[3],z6[2],t2_7,z6[3],z7[2]); SBAR();
      MMA(z13,A[4],B1[2]); SBAR(); LDB(0,8); SBAR();
      MMA(z12,A[3],B0[3]); SBAR(); SHR2(t2_6,z6[3],z6[2],t2_7,z7[3],z7[2]); SBAR();
      MMA(z13,A[3],B1[3]); SBAR(); LDB(1,8); SBAR();
      MMA(z12,A[2],B0[4]); SBAR(); ROR2(t1_6,t2_5,z6[1],t1_7,t2_6,z7[1]); SBAR();
      MMA(z13,A[2],B1[4]); SBAR(); SHX(); SBAR();
      MMA(z12,A[1],B0[5]); SBAR(); SHR2(t1_6,t2_6,z6[1],t1_7,t2_7,z7[1]); SBAR();
      MMA(z13,A[1],B1[5]); SBAR(); FMD(z6,xa_); FMD(z7,xb_); SBAR();
      MMA(z12,A[0],B0[6]); SBAR(); ROR2(u_6,t1_5,z6[0],u_7,t1_6,z7[0]); SBAR();
      MMA(z13,A[0],B1[6]); SBAR(); LDX(4); SBAR();
      MM0(z14,A[7],B0[0]); SBAR(); SHR2(u_6,t1_6,z6[0],u_7,t1_7,z7[0]); SBAR();
      MM0(z15,A[7],B1[0]); SBAR(); yw[0][3]=pk(u_6,u_7); PIN(yw[0]); STY(0); SBAR();
      MMA(z14,A[6],B0[1]); SBAR(); ROR2(t2_8,z7[3],z8[2],t2_9,z8[3],z9[2]); SBAR();
      MMA(z15,A[6],B1[1]); SBAR(); LDB(0,9); SBAR();
      MMA(z14,A[5],B0[2]); SBAR(); SHR2(t2_8,z8[3],z8[2],t2_9,z9[3],z9[2]); SBAR();
      MMA(z15,A[5],B1[2]); SBAR(); LDB(1,9); SBAR();
      MMA(z14,A[4],B0[3]); SBAR(); ROR2(t1_8,t2_7,z8[1],t1_9,t2_8,z9[1]); SBAR();
      MMA(z15,A[4],B1[3]); SBAR(); SHX(); SBAR();
      MMA(z14,A[3],B0[4]); SBAR(); SHR2(t1_8,t2_8,z8[1],t1_9,t2_9,z9[1]); SBAR();
      MMA(z15,A[3],B1[4]); SBAR(); FMD(z8,xa_); FMD(z9,xb_); SBAR();
      MMA(z14,A[2],B0[5]); SBAR(); ROR2(u_8,t1_7,z8[0],u_9,t1_8,z9[0]); SBAR();
      MMA(z15,A[2],B1[5]); SBAR(); LDX(5); SBAR();
      MMA(z14,A[1],B0[6]); SBAR(); SHR2(u_8,t1_8,z8[0],u_9,t1_9,z9[0]); SBAR();
      MMA(z15,A[1],B1[6]); SBAR(); yw[1][0]=pk(u_8,u_9); PIN(yw[1]); SBAR();
      MMA(z14,A[0],B0[7]); SBAR(); ROR2(t2_10,z9[3],z10[2],t2_11,z10[3],z11[2]); SBAR();
      MMA(z15,A[0],B1[7]); SBAR(); LDB(0,10); SBAR();
      MM0(z16,A[8],B0[0]); SBAR(); SHR2(t2_10,z10[3],z10[2],t2_11,z11[3],z11[2]); SBAR();
      MM0(z17,A[8],B1[0]); SBAR(); LDB(1,10); SBAR();
      MMA(z16,A[7],B0[1]); SBAR(); ROR2(t1_10,t2_9,z10[1],t1_11,t2_10,z11[1]); SBAR();
      MMA(z17,A[7],B1[1]); SBAR(); SHX(); SBAR();
      MMA(z16,A[6],B0[2]); SBAR(); SHR2(t1_10,t2_10,z10[1],t1_11,t2_11,z11[1]); SBAR();
      MMA(z17,A[6],B1[2]); SBAR(); FMD(z10,xa_); FMD(z11,xb_); SBAR();
      MMA(z16,A[5],B0[3]); SBAR(); ROR2(u_10,t1_9,z10[0],u_11,t1_10,z11[0]); SBAR();
      MMA(z17,A[5],B1[3]); SBAR(); LDX(6); SBAR();
      MMA(z16,A[4],B0[4]); SBAR(); SHR2(u_10,t1_10,z10[0],u_11,t1_11,z11[0]); SBAR();
      MMA(z17,A[4],B1[4]); SBAR(); yw[1][1]=pk(u_10,u_11); PIN(yw[1]); SBAR();
      MMA(z16,A[3],B0[5]); SBAR(); ROR2(t2_12,z11[3],z12[2],t2_13,z12[3],z13[2]); SBAR();
      MMA(z17,A[3],B1[5]); SBAR(); LDB(0,11); SBAR();
      MMA(z16,A[2],B0[6]); SBAR(); SHR2(t2_12,z12[3],z12[2],t2_13,z13[3],z13[2]); SBAR();
      MMA(z17,A[2],B1[6]); SBAR(); LDB(1,11); SBAR();
      MMA(z16,A[1],B0[7]); SBAR(); ROR2(t1_12,t2_11,z12[1],t1_13,t2_12,z13[1]); SBAR();
      MMA(z17,A[1],B1[7]); SBAR(); SHX(); SBAR();
      MMA(z16,A[0],B0[8]); SBAR(); SHR2(t1_12,t2_12,z12[1],t1_13,t2_13,z13[1]); SBAR();
      MMA(z17,A[0],B1[8]); SBAR(); FMD(z12,xa_); FMD(z13,xb_); SBAR();
      MM0(z18,A[9],B0[0]); SBAR(); ROR2(u_12,t1_11,z12[0],u_13,t1_12,z13[0]); SBAR();
      MM0(z19,A[9],B1[0]); SBAR(); LDX(7); SBAR();
      MMA(z18,A[8],B0[1]); SBAR(); SHR2(u_12,t1_12,z12[0],u_13,t1_13,z13[0]); SBAR();
      MMA(z19,A[8],B1[1]); SBAR(); yw[1][2]=pk(u_12,u_13); PIN(yw[1]); SBAR();
      MMA(z18,A[7],B0[2]); SBAR(); ROR2(t2_14,z13[3],z14[2],t2_15,z14[3],z15[2]); SBAR();
      MMA(z19,A[7],B1[2]); SBAR(); LDB(0,12); SBAR();
      MMA(z18,A[6],B0[3]); SBAR(); SHR2(t2_14,z14[3],z14[2],t2_15,z15[3],z15[2]); SBAR();
      MMA(z19,A[6],B1[3]); SBAR(); LDB(1,12); SBAR();
      MMA(z18,A[5],B0[4]); SBAR(); ROR2(t1_14,t2_13,z14[1],t1_15,t2_14,z15[1]); SBAR();
      MMA(z19,A[5],B1[4]); SBAR(); SHX(); SBAR();
      MMA(z18,A[4],B0[5]); SBAR(); SHR2(t1_14,t2_14,z14[1],t1_15,t2_15,z15[1]); SBAR();
      MMA(z19,A[4],B1[5]); SBAR(); FMD(z14,xa_); FMD(z15,xb_); SBAR();
      MMA(z18,A[3],B0[6]); SBAR(); ROR2(u_14,t1_13,z14[0],u_15,t1_14,z15[0]); SBAR();
      MMA(z19,A[3],B1[6]); SBAR(); LDX(8); SBAR();
      MMA(z18,A[2],B0[7]); SBAR(); SHR2(u_14,t1_14,z14[0],u_15,t1_15,z15[0]); SBAR();
      MMA(z19,A[2],B1[7]); SBAR(); yw[1][3]=pk(u_14,u_15); PIN(yw[1]); STY(1); SBAR();
      MMA(z18,A[1],B0[8]); SBAR(); ROR2(t2_16,z15[3],z16[2],t2_17,z16[3],z17[2]); SBAR();
      MMA(z19,A[1],B1[8]); SBAR(); LDB(0,13); SBAR();
      MMA(z18,A[0],B0[9]); SBAR(); SHR2(t2_16,z16[3],z16[2],t2_17,z17[3],z17[2]); SBAR();
      MMA(z19,A[0],B1[9]); SBAR(); LDB(1,13); SBAR();
      MM0(z20,A[10],B0[0]); SBAR(); ROR2(t1_16,t2_15,z16[1],t1_17,t2_16,z17[1]); SBAR();
      MM0(z21,A[10],B1[0]); SBAR(); SHX(); SBAR();
      MMA(z20,A[9],B0[1]); SBAR(); SHR2(t1_16,t2_16,z16[1],t1_17,t2_17,z17[1]); SBAR();
      MMA(z21,A[9],B1[1]); SBAR(); FMD(z16,xa_); FMD(z17,xb_); SBAR();
      MMA(z20,A[8],B0[2]); SBAR(); ROR2(u_16,t1_15,z16[0],u_17,t1_16,z17[0]); SBAR();
      MMA(z21,A[8],B1[2]); SBAR(); LDX(9); SBAR();
      MMA(z20,A[7],B0[3]); SBAR(); SHR2(u_16,t1_16,z16[0],u_17,t1_17,z17[0]); SBAR();
      MMA(z21,A[7],B1[3]); SBAR(); yw[2][0]=pk(u_16,u_17); PIN(yw[2]); SBAR();
      MMA(z20,A[6],B0[4]); SBAR(); ROR2(t2_18,z17[3],z18[2],t2_19,z18[3],z19[2]); SBAR();
      MMA(z21,A[6],B1[4]); SBAR(); LDB(0,14); SBAR();
      MMA(z20,A[5],B0[5]); SBAR(); SHR2(t2_18,z18[3],z18[2],t2_19,z19[3],z19[2]); SBAR();
      MMA(z21,A[5],B1[5]); SBAR(); LDB(1,14); SBAR();
      MMA(z20,A[4],B0[6]); SBAR(); ROR2(t1_18,t2_17,z18[1],t1_19,t2_18,z19[1]); SBAR();
      MMA(z21,A[4],B1[6]); SBAR(); SHX(); SBAR();
      MMA(z20,A[3],B0[7]); SBAR(); SHR2(t1_18,t2_18,z18[1],t1_19,t2_19,z19[1]); SBAR();
      MMA(z21,A[3],B1[7]); SBAR(); FMD(z18,xa_); FMD(z19,xb_); SBAR();
      MMA(z20,A[2],B0[8]); SBAR(); ROR2(u_18,t1_17,z18[0],u_19,t1_18,z19[0]); SBAR();
      MMA(z21,A[2],B1[8]); SBAR(); LDX(10); SBAR();
      MMA(z20,A[1],B0[9]); SBAR(); SHR2(u_18,t1_18,z18[0],u_19,t1_19,z19[0]); SBAR();
      MMA(z21,A[1],B1[9]); SBAR(); yw[2][1]=pk(u_18,u_19); PIN(yw[2]); SBAR();
      MMA(z20,A[0],B0[10]); SBAR();
      MMA(z21,A[0],B1[10]); SBAR();
      MM0(z22,A[11],B0[0]); SBAR();
      MM0(z23,A[11],B1[0]); SBAR();
      MMA(z22,A[10],B0[1]); SBAR();
      MMA(z23,A[10],B1[1]); SBAR();
      MMA(z22,A[9],B0[2]); SBAR();
      MMA(z23,A[9],B1[2]); SBAR();
      MMA(z22,A[8],B0[3]); SBAR();
      MMA(z23,A[8],B1[3]); SBAR();
      MMA(z22,A[7],B0[4]); SBAR(); ROR2(t2_20,z19[3],z20[2],t2_21,z20[3],z21[2]); SBAR();
      MMA(z23,A[7],B1[4]); SBAR(); LDB(0,15); SBAR();
      MMA(z22,A[6],B0[5]); SBAR(); SHR2(t2_20,z20[3],z20[2],t2_21,z21[3],z21[2]); SBAR();
      MMA(z23,A[6],B1[5]); SBAR(); LDB(1,15); SBAR();
      MMA(z22,A[5],B0[6]); SBAR(); ROR2(t1_20,t2_19,z20[1],t1_21,t2_20,z21[1]); SBAR();
      MMA(z23,A[5],B1[6]); SBAR(); SHX(); SBAR();
      MMA(z22,A[4],B0[7]); SBAR(); SHR2(t1_20,t2_20,z20[1],t1_21,t2_21,z21[1]); SBAR();
      MMA(z23,A[4],B1[7]); SBAR(); FMD(z20,xa_); FMD(z21,xb_); SBAR();
      MMA(z22,A[3],B0[8]); SBAR(); ROR2(u_20,t1_19,z20[0],u_21,t1_20,z21[0]); SBAR();
      MMA(z23,A[3],B1[8]); SBAR(); LDX(11); SBAR();
      MMA(z22,A[2],B0[9]); SBAR(); SHR2(u_20,t1_20,z20[0],u_21,t1_21,z21[0]); SBAR();
      MMA(z23,A[2],B1[9]); SBAR(); yw[2][2]=pk(u_20,u_21); PIN(yw[2]); SBAR();
      MMA(z22,A[1],B0[10]); SBAR();
      MMA(z23,A[1],B1[10]); SBAR();
      MMA(z22,A[0],B0[11]); SBAR();
      MMA(z23,A[0],B1[11]); SBAR();
      MM0(z24,A[12],B0[0]); SBAR();
      MM0(z25,A[12],B1[0]); SBAR();
      MMA(z24,A[11],B0[1]); SBAR();
      MMA(z25,A[11],B1[1]); SBAR();
      MMA(z24,A[10],B0[2]); SBAR();
      MMA(z25,A[10],B1[2]); SBAR();
      MMA(z24,A[9],B0[3]); SBAR();
      MMA(z25,A[9],B1[3]); SBAR();
      MMA(z24,A[8],B0[4]); SBAR(); ROR2(t2_22,z21[3],z22[2],t2_23,z22[3],z23[2]); SBAR();
      MMA(z25,A[8],B1[4]); SBAR(); VNOP(); SBAR();
      MMA(z24,A[7],B0[5]); SBAR(); SHR2(t2_22,z22[3],z22[2],t2_23,z23[3],z23[2]); SBAR();
      MMA(z25,A[7],B1[5]); SBAR(); VNOP(); SBAR();
      MMA(z24,A[6],B0[6]); SBAR(); ROR2(t1_22,t2_21,z22[1],t1_23,t2_22,z23[1]); SBAR();
      MMA(z25,A[6],B1[6]); SBAR(); SHX(); SBAR();
      MMA(z24,A[5],B0[7]); SBAR(); SHR2(t1_22,t2_22,z22[1],t1_23,t2_23,z23[1]); SBAR();
      MMA(z25,A[5],B1[7]); SBAR(); FMD(z22,xa_); FMD(z23,xb_); SBAR();
      MMA(z24,A[4],B0[8]); SBAR(); ROR2(u_22,t1_21,z22[0],u_23,t1_22,z23[0]); SBAR();
      MMA(z25,A[4],B1[8]); SBAR(); LDX(12); SBAR();
      MMA(z24,A[3],B0[9]); SBAR(); SHR2(u_22,t1_22,z22[0],u_23,t1_23,z23[0]); SBAR();
      MMA(z25,A[3],B1[9]); SBAR(); yw[2][3]=pk(u_22,u_23); PIN(yw[2]); STY(2); SBAR();
      MMA(z24,A[2],B0[10]); SBAR();
      MMA(z25,A[2],B1[10]); SBAR();
      MMA(z24,A[1],B0[11]); SBAR();
      MMA(z25,A[1],B1[11]); SBAR();
      MMA(z24,A[0],B0[12]); SBAR();
      MMA(z25,A[0],B1[12]); SBAR();
      MM0(z26,A[13],B0[0]); SBAR();
      MM0(z27,A[13],B1[0]); SBAR();
      MMA(z26,A[12],B0[1]); SBAR();
      MMA(z27,A[12],B1[1]); SBAR();
      MMA(z26,A[11],B0[2]); SBAR();
      MMA(z27,A[11],B1[2]); SBAR();
      MMA(z26,A[10],B0[3]); SBAR();
      MMA(z27,A[10],B1[3]); SBAR();
      MMA(z26,A[9],B0[4]); SBAR(); ROR2(t2_24,z23[3],z24[2],t2_25,z24[3],z25[2]); SBAR();
      MMA(z27,A[9],B1[4]); SBAR(); VNOP(); SBAR();
      MMA(z26,A[8],B0[5]); SBAR(); SHR2(t2_24,z24[3],z24[2],t2_25,z25[3],z25[2]); SBAR();
      MMA(z27,A[8],B1[5]); SBAR(); VNOP(); SBAR();
      MMA(z26,A[7],B0[6]); SBAR(); ROR2(t1_24,t2_23,z24[1],t1_25,t2_24,z25[1]); SBAR();
      MMA(z27,A[7],B1[6]); SBAR(); SHX(); SBAR();
      MMA(z26,A[6],B0[7]); SBAR(); SHR2(t1_24,t2_24,z24[1],t1_25,t2_25,z25[1]); SBAR();
      MMA(z27,A[6],B1[7]); SBAR(); FMD(z24,xa_); FMD(z25,xb_); SBAR();
      MMA(z26,A[5],B0[8]); SBAR(); ROR2(u_24,t1_23,z24[0],u_25,t1_24,z25[0]); SBAR();
      MMA(z27,A[5],B1[8]); SBAR(); LDX(13); SBAR();
      MMA(z26,A[4],B0[9]); SBAR(); SHR2(u_24,t1_24,z24[0],u_25,t1_25,z25[0]); SBAR();
      MMA(z27,A[4],B1[9]); SBAR(); yw[3][0]=pk(u_24,u_25); PIN(yw[3]); SBAR();
      MMA(z26,A[3],B0[10]); SBAR();
      MMA(z27,A[3],B1[10]); SBAR();
      MMA(z26,A[2],B0[11]); SBAR();
      MMA(z27,A[2],B1[11]); SBAR();
      MMA(z26,A[1],B0[12]); SBAR();
      MMA(z27,A[1],B1[12]); SBAR();
      MMA(z26,A[0],B0[13]); SBAR();
      MMA(z27,A[0],B1[13]); SBAR();
      MM0(z28,A[14],B0[0]); SBAR();
      MM0(z29,A[14],B1[0]); SBAR();
      MMA(z28,A[13],B0[1]); SBAR();
      MMA(z29,A[13],B1[1]); SBAR();
      MMA(z28,A[12],B0[2]); SBAR();
      MMA(z29,A[12],B1[2]); SBAR();
      MMA(z28,A[11],B0[3]); SBAR();
      MMA(z29,A[11],B1[3]); SBAR();
      MMA(z28,A[10],B0[4]); SBAR(); ROR2(t2_26,z25[3],z26[2],t2_27,z26[3],z27[2]); SBAR();
      MMA(z29,A[10],B1[4]); SBAR(); VNOP(); SBAR();
      MMA(z28,A[9],B0[5]); SBAR(); SHR2(t2_26,z26[3],z26[2],t2_27,z27[3],z27[2]); SBAR();
      MMA(z29,A[9],B1[5]); SBAR(); VNOP(); SBAR();
      MMA(z28,A[8],B0[6]); SBAR(); ROR2(t1_26,t2_25,z26[1],t1_27,t2_26,z27[1]); SBAR();
      MMA(z29,A[8],B1[6]); SBAR(); SHX(); SBAR();
      MMA(z28,A[7],B0[7]); SBAR(); SHR2(t1_26,t2_26,z26[1],t1_27,t2_27,z27[1]); SBAR();
      MMA(z29,A[7],B1[7]); SBAR(); FMD(z26,xa_); FMD(z27,xb_); SBAR();
      MMA(z28,A[6],B0[8]); SBAR(); ROR2(u_26,t1_25,z26[0],u_27,t1_26,z27[0]); SBAR();
      MMA(z29,A[6],B1[8]); SBAR(); LDX(14); SBAR();
      MMA(z28,A[5],B0[9]); SBAR(); SHR2(u_26,t1_26,z26[0],u_27,t1_27,z27[0]); SBAR();
      MMA(z29,A[5],B1[9]); SBAR(); yw[3][1]=pk(u_26,u_27); PIN(yw[3]); SBAR();
      MMA(z28,A[4],B0[10]); SBAR();
      MMA(z29,A[4],B1[10]); SBAR();
      MMA(z28,A[3],B0[11]); SBAR();
      MMA(z29,A[3],B1[11]); SBAR();
      MMA(z28,A[2],B0[12]); SBAR();
      MMA(z29,A[2],B1[12]); SBAR();
      MMA(z28,A[1],B0[13]); SBAR();
      MMA(z29,A[1],B1[13]); SBAR();
      MMA(z28,A[0],B0[14]); SBAR();
      MMA(z29,A[0],B1[14]); SBAR();
      MM0(z30,A[15],B0[0]); SBAR();
      MM0(z31,A[15],B1[0]); SBAR();
      MMA(z30,A[14],B0[1]); SBAR();
      MMA(z31,A[14],B1[1]); SBAR();
      MMA(z30,A[13],B0[2]); SBAR();
      MMA(z31,A[13],B1[2]); SBAR();
      MMA(z30,A[12],B0[3]); SBAR();
      MMA(z31,A[12],B1[3]); SBAR();
      MMA(z30,A[11],B0[4]); SBAR(); ROR2(t2_28,z27[3],z28[2],t2_29,z28[3],z29[2]); SBAR();
      MMA(z31,A[11],B1[4]); SBAR(); VNOP(); SBAR();
      MMA(z30,A[10],B0[5]); SBAR(); SHR2(t2_28,z28[3],z28[2],t2_29,z29[3],z29[2]); SBAR();
HOOK1(); SBAR();
      MMA(z31,A[10],B1[5]); SBAR(); VNOP(); SBAR();
      MMA(z30,A[9],B0[6]); SBAR(); ROR2(t1_28,t2_27,z28[1],t1_29,t2_28,z29[1]); SBAR();
      MMA(z31,A[9],B1[6]); SBAR(); SHX(); SBAR();
      MMA(z30,A[8],B0[7]); SBAR(); SHR2(t1_28,t2_28,z28[1],t1_29,t2_29,z29[1]); SBAR();
      MMA(z31,A[8],B1[7]); SBAR(); FMD(z28,xa_); FMD(z29,xb_); SBAR();
      MMA(z30,A[7],B0[8]); SBAR(); ROR2(u_28,t1_27,z28[0],u_29,t1_28,z29[0]); SBAR();
      MMA(z31,A[7],B1[8]); SBAR(); LDX(15); SBAR();
      MMA(z30,A[6],B0[9]); SBAR(); SHR2(u_28,t1_28,z28[0],u_29,t1_29,z29[0]); SBAR();
      MMA(z31,A[6],B1[9]); SBAR(); yw[3][2]=pk(u_28,u_29); PIN(yw[3]); SBAR();
      MMA(z30,A[5],B0[10]); SBAR();
      MMA(z31,A[5],B1[10]); SBAR();
      MMA(z30,A[4],B0[11]); SBAR();
      MMA(z31,A[4],B1[11]); SBAR();
      MMA(z30,A[3],B0[12]); SBAR();
      MMA(z31,A[3],B1[12]); SBAR();
      MMA(z30,A[2],B0[13]); SBAR();
      MMA(z31,A[2],B1[13]); SBAR();
      MMA(z30,A[1],B0[14]); SBAR();
      MMA(z31,A[1],B1[14]); SBAR();
      MMA(z30,A[0],B0[15]); SBAR();
      MMA(z31,A[0],B1[15]); SBAR();
      NOP1(); SBAR();
      NOP1(); SBAR();
      NOP1(); SBAR();
      NOP1(); SBAR();
      NOP1(); SBAR();
      NOP1(); SBAR();
      NOP1(); SBAR();
      NOP1(); SBAR();
      NOP1(); SBAR(); ROR2(t2_30,z29[3],z30[2],t2_31,z30[3],z31[2]); SBAR();
      NOP1(); SBAR(); VNOP(); SBAR();
      NOP1(); SBAR(); SHR2(t2_30,z30[3],z30[2],t2_31,z31[3],z31[2]); SBAR();
      NOP1(); SBAR(); VNOP(); SBAR();
      NOP1(); SBAR(); ROR2(t1_30,t2_29,z30[1],t1_31,t2_30,z31[1]); SBAR();
      NOP1(); SBAR(); SHX(); SBAR();
      NOP1(); SBAR(); SHR2(t1_30,t2_30,z30[1],t1_31,t2_31,z31[1]); SBAR();
      NOP1(); SBAR(); FMD(z30,xa_); FMD(z31,xb_); SBAR();
      NOP1(); SBAR(); ROR2(u_30,t1_29,z30[0],u_31,t1_30,z31[0]); SBAR();
      NOP1(); SBAR(); VNOP(); SBAR();
      NOP1(); SBAR(); SHR2(u_30,t1_30,z30[0],u_31,t1_31,z31[0]); SBAR();
      NOP1(); SBAR(); yw[3][3]=pk(u_30,u_31); PIN(yw[3]); STY(3); SBAR();
      if(v==3 && lane<32) touch((unsigned)((j*32+lane)*2048+((xcd&1)*8+((3-(int)((uintptr_t)WO>>7))&7))*128),WO,junk);
      HOOK2();
    }
    if(!h1){
      wait_flags<4>(kvflag(ws,R,C,v>>1,1,lane),nonce);
      lds_wait(cnt+C_KV1,2);
      kv_fetch(ws,lds0,R,C,v>>1,1,v&1,lane);
    }
    WAITV(0);
    arrive_publish(cnt+C_H1,4,flagp(ws,b,FK_Y),nonce,lane);
    lds_wait(cnt+C_H1,4); lds_wait(cnt+C_P1E,4);
#pragma unroll 1
    for(int i=v;i<WO_P1;i+=4) wo_block(i);
    if(v<2) touch((unsigned)((512*(j>>3)+8*(j&7)+64*(lane>>3)+(lane&7))*2048+(xcd*2+v)*128),Aq,junk);
    __builtin_amdgcn_s_setprio(0);
  } else {
    const int wr=wid>>1, wc=wid&1;
    const int r=lane&31, h=lane>>5;
    lptr L=(lptr)lds;
    lptr xa[4];
    { int row=wr*64+r; int sw=(row>>1)&7;
#pragma unroll
      for(int ks=0;ks<4;ks++) xa[ks]=L+row*128+(((2*ks+h)^sw)*16); }
    lptr wa;
    { int blk=(lane>>4)&1,q=(lane&15)>>2,p=lane&3; int c=(wc*4+2*blk+(p>>1))^(((q>>1)&1)<<2); wa=L+16384+(8*h+q)*128+c*16+8*(p&1); }
    f32x16 aq[2],ak[2],av[2];
#pragma unroll
    for(int i=0;i<2;i++){ aq[i]=f32x16{}; ak[i]=f32x16{}; av[i]=f32x16{}; }
    G1F F0,F1;
#define G1_RX(F,ks) do{ F.x0=ldsr128(xa[ks]+so); F.x1=ldsr128(xa[ks]+so+4096); }while(0)
#define G1_RW(F,ks,sg) do{ F.w[2*(sg)]=ldstr(wa+so+(sg)*8192+(16*(ks))*128); F.w[2*(sg)+1]=ldstr(wa+so+(sg)*8192+(16*(ks)+4)*128); }while(0)
#define G1_W(F,sg) cat(F.w[2*(sg)],F.w[2*(sg)+1])
#define G1_STEP(F,N,ks,LD) do{ \
    ak[0]=MFMA32(G1_W(F,1),F.x0,ak[0]); PIN(ak[0]); if(LD) G1_RX(N,ks); SBAR(); \
    ak[1]=MFMA32(G1_W(F,1),F.x1,ak[1]); PIN(ak[1]); if(LD) G1_RW(N,ks,1); SBAR(); \
    av[0]=MFMA32(F.x0,G1_W(F,2),av[0]); PIN(av[0]); if(LD) G1_RW(N,ks,2); SBAR(); \
    av[1]=MFMA32(F.x1,G1_W(F,2),av[1]); PIN(av[1]); if(LD) G1_RW(N,ks,0); SBAR(); \
    aq[0]=MFMA32(G1_W(F,0),F.x0,aq[0]); PIN(aq[0]); SBAR(); \
    aq[1]=MFMA32(G1_W(F,0),F.x1,aq[1]); PIN(aq[1]); SBAR(); }while(0)
    __builtin_amdgcn_s_setprio(3);
    asm volatile("":"+v"(aq[0]),"+v"(aq[1]),"+v"(ak[0]),"+v"(ak[1]),"+v"(av[0]),"+v"(av[1]));
    WAITV(6);
    BAR();
    int so=0;
    G1_RX(F0,0); G1_RW(F0,0,1); G1_RW(F0,0,2); G1_RW(F0,0,0); SBAR();
#pragma unroll 1
    for(int t=0;t<14;t++){
      G1_STEP(F0,F1,1,true);
      G1_STEP(F1,F0,2,true);
      G1_STEP(F0,F1,3,true);
      so=(so==2*G1_SLOT)?0:so+G1_SLOT;
      if(t==0) WAITV(0);
      WAITL(); BAR(); SBAR();
      G1_STEP(F1,F0,0,true);
    }
    G1F E[8]; E[0]=F0;
#define G1_KV(n,LD) do{ \
    ak[0]=MFMA32(G1_W(E[n],1),E[n].x0,ak[0]); PIN(ak[0]); if(LD) G1_RX(E[(n)+1],((n)+1)&3); SBAR(); \
    ak[1]=MFMA32(G1_W(E[n],1),E[n].x1,ak[1]); PIN(ak[1]); if(LD) G1_RW(E[(n)+1],((n)+1)&3,1); SBAR(); \
    av[0]=MFMA32(E[n].x0,G1_W(E[n],2),av[0]); PIN(av[0]); if(LD) G1_RW(E[(n)+1],((n)+1)&3,2); SBAR(); \
    av[1]=MFMA32(E[n].x1,G1_W(E[n],2),av[1]); PIN(av[1]); if(LD) G1_RW(E[(n)+1],((n)+1)&3,0); SBAR(); }while(0)
#define G1_Q(n) do{ aq[0]=MFMA32(G1_W(E[n],0),E[n].x0,aq[0]); PIN(aq[0]); aq[1]=MFMA32(G1_W(E[n],0),E[n].x1,aq[1]); PIN(aq[1]); SBAR(); }while(0)
    G1_KV(0,true); G1_KV(1,true); G1_KV(2,true);
    LOAD_NONCE();
    so=0;
    lds_wait(cnt+C_S15,1); SBAR();
    G1_KV(3,true); G1_KV(4,true); G1_KV(5,true); G1_KV(6,true);
    arrive(cnt+C_RD);
    SBAR();
    { const int n=7; ak[0]=MFMA32(G1_W(E[n],1),E[n].x0,ak[0]); ak[1]=MFMA32(G1_W(E[n],1),E[n].x1,ak[1]); av[0]=MFMA32(E[n].x0,G1_W(E[n],2),av[0]); av[1]=MFMA32(E[n].x1,G1_W(E[n],2),av[1]);
      PIN(ak[0]); PIN(ak[1]); PIN(av[0]); PIN(av[1]); SBAR(); }
    const float qs=0.17677669529663687f*1.4426950408889634f;
    u32x4 kf[2][2],vf[2][2];
    const unsigned go=WS_KV+(unsigned)b*32768u+wc*16384u+(2*wr)*2048u+lane*16u;
#define KV_STORE() _Pragma("unroll") for(int rt=0;rt<2;rt++) _Pragma("unroll") for(int s=0;s<2;s++) __builtin_amdgcn_raw_buffer_store_b128(kf[rt][s],wsr,go+rt*2048+s*1024,0,SC1); \
      _Pragma("unroll") for(int rt=0;rt<2;rt++) _Pragma("unroll") for(int s=0;s<2;s++) __builtin_amdgcn_raw_buffer_store_b128(vf[rt][s],wsr,go+8192+rt*2048+s*1024,0,SC1)
    G1_Q(0);
    kf[0][0]=pack8(ak[0],0); kf[0][1]=pack8(ak[0],1); PIN(kf[0][0]); PIN(kf[0][1]);
    kf[1][0]=pack8(ak[1],0); kf[1][1]=pack8(ak[1],1); PIN(kf[1][0]); PIN(kf[1][1]); SBAR(); G1_Q(1);
    vf[0][0]=pack8(av[0],0); vf[0][1]=pack8(av[0],1); PIN(vf[0][0]); PIN(vf[0][1]);
    vf[1][0]=pack8(av[1],0); vf[1][1]=pack8(av[1],1); PIN(vf[1][0]); PIN(vf[1][1]); SBAR();
    if(wc==0){ KV_STORE(); }
    SBAR();
    G1_Q(2); G1_Q(3); G1_Q(4); G1_Q(5); G1_Q(6); G1_Q(7);
    lds_wait(cnt+C_RD,4);
#pragma unroll
    for(int rt=0;rt<2;rt++){
      lwptr lq=(lwptr)lds+(1-wc)*HR+49152+(2*wr+rt)*2048+lane*16;
#pragma unroll
      for(int s=0;s<2;s++) *(LAS u32x4*)(lq+s*1024)=pack8(aq[rt],s,qs);
    }
    if(R<2){
#pragma unroll 1
      for(int i=0;i<8*(2-R);i++) *(LAS u32x4*)((lwptr)lds+(1-wc)*HR+wr*24576+i*1024+lane*16)=u32x4{0,0,0,0};
    }
    if(wc==1){ lds_wait(cnt+C_KV0,2); KV_STORE(); }
    WAITV(0);
    arrive_publish(cnt+C_KV0+wc,2,flagp(ws,b,FK_KV0+wc),nonce,lane);
    if(wid==0){
      lds_wait(cnt+C_KV0,2);
      const int kind=(lane>>2)&1, ri=lane&3;
      const gu64* fp=kvflag(ws,R,C,kind,0,ri);
      unsigned todo=(R>=2)?0x77u:((R==1)?0x66u:0x44u);
#pragma unroll 1
      for(unsigned sp=0;sp<SPIN_CAP && todo;sp++){
        u64 fv=0;
        if(lane<8 && ((todo>>lane)&1)) fv=__hip_atomic_load(fp,RLX_AGENT);
        unsigned rdy=(unsigned)__builtin_amdgcn_ballot_w64(fv==nonce)&todo;
        MEMFENCE();
        todo&=~rdy;
        if(!rdy) __builtin_amdgcn_s_sleep(2);
#pragma unroll 1
        while(rdy){
          const int bit=__builtin_ctz(rdy); rdy&=rdy-1;
          const int kd=bit>>2, i=bit&3;
          const char* src=ws+WS_KV+(size_t)blk_of(R-2+i,(C&12)|((C-1-kd)&3))*32768+kd*8192;
          const unsigned dst=lds0+HR+(1-kd)*24576+i*8192;
          dma4_sc1(lane*16,src,dst); dma4_sc1(lane*16,src+4096,dst+4096);
        }
      }
      WAITV(0);
      arrive(cnt+C_H0K);
    } else lds_wait(cnt+C_H0K,1);
    __builtin_amdgcn_s_setprio(1);
  }
  const int late=wid>>2;
  if(R>=2) attention<false>(lds,cnt,ws,wsr,b,R,C,wid,lane,nonce);
  else     attention<true >(lds,cnt,ws,wsr,b,R,C,wid,lane,nonce);
  if(!late){
    WAITV(0);
    arrive_publish(cnt+C_O0,4,flagp(ws,b,FK_O0),nonce,lane);
    if(wid<3){ lds_wait(cnt+C_E,4); lds_wait(cnt+C_P1L,4);
#pragma unroll 1
      for(int i=WO_P1+wid;i<32;i+=3) wo_block(i);
    }
    const int T2=R2*4+wid;
    const unsigned ob=WS_O+(unsigned)T2*65536u+lane*16u;
    u32x4 of[G2D][2];
#define G2_LDO(i,n) do{ const unsigned so_=(unsigned)((((n)+KO)&15)*4096); of[(i)%G2D][0]=__builtin_amdgcn_raw_buffer_load_b128(wsr,obv,so_,SC1); of[(i)%G2D][1]=__builtin_amdgcn_raw_buffer_load_b128(wsr,obv+1024,so_,SC1); }while(0)
    const int h=lane>>5,blk=(lane>>4)&1,q=(lane&15)>>2,p=lane&3;
    lptr wa[2];
#pragma unroll
    for(int ct=0;ct<2;ct++){ int c=(ct*4+2*blk+(p>>1))^(((q>>1)&1)<<2); wa[ct]=(lptr)lds+L_WO+(31-G2D)*4096+(4*h+q)*128+c*16+8*(p&1); }
    f32x16 acc[2]; acc[0]=f32x16{}; acc[1]=f32x16{};
    s16x4 wf[2][8];
#define G2_LDW(i) do{ _Pragma("unroll") for(int s_=0;s_<2;s_++) _Pragma("unroll") for(int ct_=0;ct_<2;ct_++){ lptr a_=wa[ct_]+(G2D-(i))*4096+s_*2048; \
      wf[(i)&1][4*s_+2*ct_]=ldstr(a_); wf[(i)&1][4*s_+2*ct_+1]=ldstr(a_+1024); } }while(0)
    lds_wait(cnt+C_W1,4);
    G2_LDW(0);
    PIN(acc[0]); PIN(acc[1]);
    if(wid==3){ wait_flags(flagp(ws,blk_of(R2,lane&15),FK_O0),nonce); arrive(cnt+C_EF); } else lds_wait(cnt+C_EF,1);
    unsigned obv=ob;
#pragma unroll
    for(int i=0;i<G2D;i++) G2_LDO(i,i);
    u64 f1=0;
    constexpr int NIT=32/G2D, ITL=16/G2D-1;
#pragma unroll 1
    for(int it=0;it<NIT;it++){
      if(it==1) arrive(cnt+C_W2);
      if(it==ITL){
        if(wid==3){ if(!allof(f1==nonce)) wait_flags(flagp(ws,blk_of(R2,lane&15),FK_O1),nonce); MEMFENCE(); arrive(cnt+C_LF); }
        else lds_wait(cnt+C_LF,1);
        __builtin_amdgcn_s_setprio(3);
      }
      if(it==ITL+1) lds_wait(cnt+C_W2,4);
      if(it==NIT-1) arrive(cnt+C_G2);
      PIN(wa[0]); PIN(wa[1]);
      const int nb=G2D*it+G2D;
      obv=ob+((it<ITL)?0u:((it==NIT-1)?0x40000000u:2048u)); PIN(obv);
#pragma unroll
      for(int i=0;i<G2D;i++){
        SBAR();
        if(i==G2D-4){ if(wid==3 && it==ITL-1) f1=__hip_atomic_load(flagp(ws,blk_of(R2,lane&15),FK_O1),RLX_AGENT); }
        lptr w0_=wa[0]+(G2D-(i+1))*4096, w1_=wa[1]+(G2D-(i+1))*4096; s16x4* wn_=wf[(i+1)&1]; s16x4* wc_=wf[i&1];
        unsigned so_=(unsigned)(((nb+i+KO)&15)*4096);
        SBAR();
        acc[0]=MFMA32(cat(wc_[0],wc_[1]),B8(of[i][0]),acc[0]); PIN(acc[0]); wn_[0]=ldstr(w0_); wn_[1]=ldstr(w0_+1024); asm volatile("":"+s"(so_)); SBAR();
        acc[1]=MFMA32(cat(wc_[2],wc_[3]),B8(of[i][0]),acc[1]); PIN(acc[1]); wn_[2]=ldstr(w1_); wn_[3]=ldstr(w1_+1024); SBAR();
        of[i][0]=__builtin_amdgcn_raw_buffer_load_b128(wsr,obv,so_,SC1); SBAR();
        acc[0]=MFMA32(cat(wc_[4],wc_[5]),B8(of[i][1]),acc[0]); PIN(acc[0]); wn_[4]=ldstr(w0_+2048); wn_[5]=ldstr(w0_+3072); SBAR();
        acc[1]=MFMA32(cat(wc_[6],wc_[7]),B8(of[i][1]),acc[1]); PIN(acc[1]); wn_[6]=ldstr(w1_+2048); wn_[7]=ldstr(w1_+3072); SBAR();
        of[i][1]=__builtin_amdgcn_raw_buffer_load_b128(wsr,obv+1024,so_,SC1);
      }
      wa[0]-=G2D*4096; wa[1]-=G2D*4096;
    }
    lwptr stg=(lwptr)lds+L_WO+(31-wid)*4096;
#pragma unroll
    for(int ct=0;ct<2;ct++){
#pragma unroll
      for(int gp=0;gp<2;gp++){
        unsigned a0=pk(acc[ct][8*gp],acc[ct][8*gp+1]), a1=pk(acc[ct][8*gp+2],acc[ct][8*gp+3]);
        unsigned b0=pk(acc[ct][8*gp+4],acc[ct][8*gp+5]), b1=pk(acc[ct][8*gp+6],acc[ct][8*gp+7]);
        auto r0=__builtin_amdgcn_permlane32_swap(a0,b0,false,false); auto r1=__builtin_amdgcn_permlane32_swap(a1,b1,false,false);
        u32x4 vv={r0[0],r1[0],r0[1],r1[1]};
        *(LAS u32x4*)(stg+(lane&31)*128+(((ct*4+gp*2+h)^(lane&7))*16))=vv;
      }
    }
#pragma unroll
    for(int i=0;i<4;i++){
      const int row=8*i+(lane>>3);
      const u32x4 vv=*(const LAS u32x4*)(stg+row*128+(((lane&7)^(row&7))*16));
      __builtin_amdgcn_raw_buffer_store_b128(vv,outr,(unsigned)(T2*32+row)*4096u+2048u+C2*128u+(lane&7)*16u,0,SC1);
    }
  } else {
    const int v=wid-4;
    const int chb=xcd*128+(v&1)*64, rb=512*(j>>3)+8*(j&7)+4*(v>>1)+64*(lane>>3);
    u32x4 qq[4],yy[4];
    const bool yr=__builtin_amdgcn_readfirstlane(*(volatile LAS unsigned*)(cnt+C_YF))!=0;
    MEMFENCE();
    unsigned yo=WS_Y+(unsigned)(chb+(lane>>2))*4096u+(unsigned)(j*128+(v>>1)*64+(lane&3)*16);
    unsigned yo1=yr? yo : 0x40000000u;
#pragma unroll
    for(int i=0;i<4;i++){ qq[i]=*(const u32x4*)(Aq+(size_t)(rb+i)*1024+chb+(lane&7)*8); yy[i]=__builtin_amdgcn_raw_buffer_load_b128(wsr,yo1,i*65536,SC1); }
    WAITV(8);
    arrive_publish(cnt+C_O1,4,flagp(ws,b,FK_O1),nonce,lane);
    __builtin_amdgcn_s_setprio(0);
    if(!yr){ wait_flags<8>(flagp(ws,(lane&31)*8+xcd,FK_Y),nonce);
#pragma unroll
      for(int i=0;i<4;i++) yy[i]=__builtin_amdgcn_raw_buffer_load_b128(wsr,yo,i*65536,SC1); }
    lds_wait(cnt+C_L,4);
    lwptr stg=(lwptr)lds+v*4096;
#pragma unroll
    for(int i=0;i<4;i++) *(LAS u32x4*)(stg+i*1024+lane*16)=yy[i];
    { const int g=lane>>4,q=(lane&15)>>2,p=lane&3;
      bf16x8 tt[4];
#pragma unroll
      for(int n=0;n<4;n++){ lptr a=(lptr)stg+(8*(2*n+(g>>1))+q)*64+(g&1)*32+8*p; tt[n]=cat(ldstr(a),ldstr(a+256)); }
      MEMFENCE();
      const int srow=16*(g&1)+(lane&15);
#pragma unroll
      for(int n=0;n<4;n++) *(LAS bf16x8*)(stg+srow*128+(((2*n+(g>>1))^(srow&7))*16))=tt[n];
    }
    u32x4 ro[4];
#pragma unroll
    for(int i=0;i<4;i++){
      const int row=8*i+(lane>>3);
      const u32x4 yt=*(const LAS u32x4*)(stg+row*128+(((lane&7)^(row&7))*16));
#pragma unroll
      for(int d=0;d<4;d++) ro[i][d]=pk(lo16(qq[i][d])*lo16(yt[d]),hi16(qq[i][d])*hi16(yt[d]));
      PIN(ro[i]);
    }
    if(b==0 && wid==7){
#pragma unroll 1
      for(unsigned sp=0;sp<SPIN_CAP;sp++){
        u64 f[4];
#pragma unroll
        for(int i=0;i<4;i++) f[i]=__hip_atomic_load(flagp(ws,i*64+lane,FK_Y),RLX_AGENT);
        if(allof(f[0]==nonce && f[1]==nonce && f[2]==nonce && f[3]==nonce)) break;
        __builtin_amdgcn_s_sleep(16);
      }
      if(lane==0) __hip_atomic_store((gu64*)(ws+F_E),nonce,RLX_AGENT);
    }
    lds_wait(cnt+C_G2,4);
#pragma unroll
    for(int i=0;i<4;i++) __builtin_amdgcn_raw_buffer_store_b128(ro[i],outr,(unsigned)(rb+i)*4096u+chb*2u+(lane&7)*16u,0,SC1);
  }
}

extern "C" void kernel_launch(void* const* d_in, const int* in_sizes, int n_in, void* d_out, int out_size, void* d_ws, size_t ws_size, hipStream_t stream){
  (void)hipFuncSetAttribute((const void*)fused,hipFuncAttributeMaxDynamicSharedMemorySize,LDS_BYTES);
  fused<<<256,512,LDS_BYTES,stream>>>((const u16*)d_in[0],(const u16*)d_in[1],(const u16*)d_in[2],(const u16*)d_in[3],(const u16*)d_in[4],(const u16*)d_in[5],
                                      (const u16*)d_in[6],(const u16*)d_in[7],(const u16*)d_in[8],(u16*)d_out,(char*)d_ws);
}
```
